```python
import torch

def _softmax(x, axis=-1):
    return torch.softmax(x, dim=axis)
def _gelu(x):
    return torch.nn.functional.gelu(x, approximate="tanh")
def _silu(x):
    return torch.nn.functional.silu(x)
def _sigmoid(x):
    return torch.sigmoid(x)
def _layernorm(x, axis=-1, eps=1e-5):
    m = x.mean(dim=axis, keepdim=True)
    v = x.var(dim=axis, keepdim=True, unbiased=False)
    return (x - m) / torch.sqrt(v + eps)
def _scatter_add(tgt, idx, src, axis=0):
    return tgt.clone().index_add_(axis, idx, src)
def _segment_sum(src, seg, n):
    out = torch.zeros((n,) + src.shape[1:], dtype=src.dtype)
    return out.index_add_(0, seg, src)
def _pad(x, pw):
    flat = tuple(v for pair in reversed(pw) for v in pair)
    return torch.nn.functional.pad(x, flat)

kernel_name = 'parallel_sum__mha_block__mixer_channel_mlp__prompt'

def setup_inputs():

    _w = (lambda a: (lambda t: t.to(torch.bfloat16) if t.is_floating_point() else t)(torch.as_tensor(a)))
    def _A_setup():
        B, S, D, H = 1, 2048, 640, 32
        return {
            "x": _w((lambda *s: torch.randn(*s, dtype=torch.bfloat16) * 0.05)(B, S, D)),
            "wqkv": _w((lambda *s: torch.randn(*s, dtype=torch.bfloat16) * 0.05)(D, 3 * D)),
            "wo": _w((lambda *s: torch.randn(*s, dtype=torch.bfloat16) * 0.05)(D, D)),
        }
    _A_in = _A_setup()

    def _B_setup():
        B, S, D, F = 1, 2048, 640, 1536
        return {
            "x": _w((lambda *s: torch.randn(*s, dtype=torch.bfloat16) * 0.05)(B, S, D)),
            "w1": _w((lambda *s: torch.randn(*s, dtype=torch.bfloat16) * 3.2)(D, F) * (D ** -0.5)),
            "b1": _w((lambda *s: torch.randn(*s, dtype=torch.bfloat16) * 1.6)(F)),
            "w2": _w((lambda *s: torch.randn(*s, dtype=torch.bfloat16) * 0.1)(F, D) * (F ** -0.5)),
            "b2": _w((lambda *s: torch.randn(*s, dtype=torch.bfloat16) * 0.2)(D)),
        }
    _B_in = _B_setup()

    return {
        "A_x": _A_in["x"],
        "A_wqkv": _A_in["wqkv"],
        "A_wo": _A_in["wo"],
        "B_x": _B_in["x"],
        "B_w1": _B_in["w1"],
        "B_b1": _B_in["b1"],
        "B_w2": _B_in["w2"],
        "B_b2": _B_in["b2"],
    }

def reference(A_x, A_wqkv, A_wo, B_x, B_w1, B_b1, B_w2, B_b2):
    def _A_ref(x, wqkv, wo):
        B, S, D = x.shape
        H = 32
        Dh = D // H
        qkv = (lambda x, *d: x.permute(*d))((x @ wqkv).reshape(B, S, 3, H, Dh), 2, 0, 3, 1, 4)
        q, k, v = qkv[0], qkv[1], qkv[2]
        scores = torch.einsum("bhqd,bhkd->bhqk", q, k) * (Dh ** -0.5)
        p = _softmax(scores, axis=-1)
        o = torch.einsum("bhqk,bhkd->bhqd", p, v)
        return (lambda x, *d: x.permute(*d))(o, 0, 2, 1, 3).reshape(B, S, D) @ wo

    def _B_ref(x, w1, b1, w2, b2):
        y = _layernorm(x, axis=-1)
        h = _gelu(torch.einsum("bsd,df->bsf", y, w1) + b1)
        return torch.einsum("bsf,fd->bsd", h, w2) + b2

    x = A_x
    xn = _layernorm(x, axis=-1)
    return x + _A_ref(xn, A_wqkv, A_wo) + _B_ref(xn, B_w1, B_b1, B_w2, B_b2)
```

```cpp
#include <hip/hip_runtime.h>
#include <hip/hip_bf16.h>
#include <stdint.h>

namespace {

typedef __attribute__((ext_vector_type(8))) __bf16 bf16x8;
typedef __attribute__((ext_vector_type(4))) __bf16 bf16x4;
typedef __attribute__((ext_vector_type(2))) __bf16 bf16x2;
typedef __attribute__((ext_vector_type(8))) short s16x8;
typedef __attribute__((ext_vector_type(4))) short s16x4;
typedef __attribute__((ext_vector_type(16))) float f32x16;
typedef __attribute__((ext_vector_type(4))) float f32x4;
typedef __attribute__((ext_vector_type(2))) float f32x2;
typedef __attribute__((ext_vector_type(4))) unsigned u32x4;
typedef __attribute__((ext_vector_type(2))) unsigned u32x2;
typedef __attribute__((address_space(3))) char* lds_ptr;
typedef __attribute__((address_space(3))) const char* lds_cptr;

constexpr int S_ = 2048, D_ = 640, H_ = 32, F_ = 1536;
constexpr float EPS = 1e-5f;
constexpr float QSCALE = 0.22360679774997896f * 1.4426950408889634f;

constexpr int QT_B = 1280;
constexpr int KVT_B = 2624;
constexpr size_t WS_Q = 0;
constexpr size_t WS_KV = WS_Q + (size_t)H_ * 64 * QT_B;
constexpr size_t WS_TOK = WS_KV + (size_t)H_ * 64 * KVT_B + 4096;
constexpr size_t WS_CS1 = WS_TOK + (size_t)S_ * 16;
constexpr size_t WS_H = WS_CS1 + 8192;
constexpr size_t WS_PART = WS_H + (size_t)64 * 96 * 1024;
constexpr size_t WS_O = WS_PART + (size_t)2 * 64 * 20 * 4096;
constexpr size_t WS_END = WS_O + (size_t)64 * 40 * 1024;

#define SBAR() __builtin_amdgcn_sched_barrier(0)
#define PIN(x) asm volatile("" : "+v"(x))
#define WAIT_BAR(N) asm volatile("s_waitcnt vmcnt(" #N ") lgkmcnt(0)\n\ts_barrier" ::: "memory")

__device__ __forceinline__ f32x16 mfma16(bf16x8 a, bf16x8 b, f32x16 c) { return __builtin_amdgcn_mfma_f32_32x32x16_bf16(a, b, c, 0, 0, 0); }
__device__ __forceinline__ f32x16 mfma8(s16x4 a, s16x4 b, f32x16 c) { return __builtin_amdgcn_mfma_f32_32x32x8bf16_1k(a, b, c, 0, 0, 0); }
__device__ __forceinline__ unsigned pk2(float lo, float hi) { f32x2 v = {lo, hi}; return __builtin_bit_cast(unsigned, __builtin_convertvector(v, bf16x2)); }
__device__ __forceinline__ float bf_lo(unsigned u) { return __uint_as_float(u << 16); }
__device__ __forceinline__ float bf_hi(unsigned u) { return __uint_as_float(u & 0xffff0000u); }
__device__ __forceinline__ void glds16(const void* g, unsigned lds_base) {
    unsigned sv;
    asm volatile("s_mov_b32 %0, m0\n\ts_mov_b32 m0, %2\n\ts_nop 0\n\tglobal_load_lds_dwordx4 %1, off\n\ts_mov_b32 m0, %0" : "=&s"(sv) : "v"(g), "s"(lds_base) : "memory");
}
__device__ __forceinline__ bf16x8 lds_r128(lds_cptr p) { return *(const __attribute__((address_space(3))) bf16x8*)p; }
__device__ __forceinline__ s16x4 lds_r64(lds_cptr p) { return *(const __attribute__((address_space(3))) s16x4*)p; }
__device__ __forceinline__ s16x4 lds_tr(lds_cptr p) { return __builtin_amdgcn_ds_read_tr16_b64_v4i16((__attribute__((address_space(3))) s16x4*)p); }
__device__ __forceinline__ bf16x8 cat8(s16x4 a, s16x4 b) { s16x8 v = {a[0], a[1], a[2], a[3], b[0], b[1], b[2], b[3]}; return __builtin_bit_cast(bf16x8, v); }
__device__ __forceinline__ float dot2(unsigned a, unsigned b, float c) { return __builtin_amdgcn_fdot2_f32_bf16(__builtin_bit_cast(bf16x2, a), __builtin_bit_cast(bf16x2, b), c, false); }
__device__ __forceinline__ float swap32_add(float v) { auto rr = __builtin_amdgcn_permlane32_swap(__float_as_uint(v), __float_as_uint(v), false, false); return __uint_as_float(rr[0]) + __uint_as_float(rr[1]); }
__device__ __forceinline__ float swap32_max(float v) { auto rr = __builtin_amdgcn_permlane32_swap(__float_as_uint(v), __float_as_uint(v), false, false); return __builtin_fmaxf(__uint_as_float(rr[0]), __uint_as_float(rr[1])); }
__device__ __forceinline__ void split3(float v, unsigned& a, unsigned& b, unsigned& c) {
    a = pk2(v, 0.f) & 0xffffu; float r = v - bf_lo(a);
    b = pk2(r, 0.f) & 0xffffu; r = r - bf_lo(b);
    c = pk2(r, 0.f) & 0xffffu;
}
constexpr unsigned ONES2 = 0x3f803f80u;

constexpr int K1_STAGE = 32768, K1_NST = 4, K1_LDS = K1_STAGE * K1_NST + 8192 + 1024;

__device__ __forceinline__ unsigned k1_perm(int j) {
    return j == 0 ? 0x76453120u : (j == 3 ? 0x57064231u : (j == 4 ? 0x76254130u : 0x75643120u));
}
__device__ __forceinline__ int k1_piece_off(int P, int r, int& head) {
    head = P / 5; int rr = P - head * 5; const int h7 = head & 7;
    int role = rr;
    if (h7 == 1) role = (0x32410 >> (4 * rr)) & 7;
    if (h7 == 3) role = (0x32104 >> (4 * rr)) & 7;
    return role == 4 ? 1024 + r * 8 : (role >> 1) * 512 + r * 16 + (role & 1) * 8;
}

template <bool VORI>
__device__ __forceinline__ void k1_body(const __bf16* __restrict__ x, const __bf16* __restrict__ w, char* __restrict__ ws, char* lds, int mt, int nt) {
    const int tid = threadIdx.x, lane = tid & 63, r = lane & 31, hh = lane >> 5;
    const int wid = __builtin_amdgcn_readfirstlane(tid >> 6), wm = wid & 3, wn = wid >> 2;
    const unsigned lds0 = (unsigned)(uintptr_t)lds;
    const char* xsrc[2]; const char* wsrc[2];
#pragma unroll
    for (int i = 0; i < 2; ++i) {
        const int ii = 2 * wid + i;
        const int row = 8 * ii + (lane >> 3);
        xsrc[i] = (const char*)x + (size_t)(mt * 128 + row) * (D_ * 2) + (((lane & 7) ^ ((row >> 1) & 7)) * 16);
        const int krow = 4 * ii + (lane >> 4);
        wsrc[i] = (const char*)w + (size_t)krow * (3 * D_ * 2) + nt * 256 + (((lane & 15) ^ ((krow & 3) << 2)) * 16);
    }
    auto issue = [&](int st) {
        const unsigned base = lds0 + (st & 3) * K1_STAGE + wid * 2048;
        glds16(xsrc[0] + st * 128, base);
        glds16(xsrc[1] + st * 128, base + 1024);
        glds16(wsrc[0] + (size_t)st * 64 * (3 * D_ * 2), base + 16384);
        glds16(wsrc[1] + (size_t)st * 64 * (3 * D_ * 2), base + 16384 + 1024);
    };
    issue(0); issue(1); issue(2); issue(3);
    const int xrow = 32 * wm + r;
    const int xoff = xrow * 128 + ((hh ^ ((xrow >> 1) & 7)) * 16);
    const int q4 = (lane & 15) >> 2, p4 = lane & 3, gi = (lane >> 4) & 1;
    int woff[2];
#pragma unroll
    for (int f = 0; f < 2; ++f) {
        const int ft = 2 * wn + f;
        int rel = 4 * gi + p4;
        if (!VORI) { const int T = (nt % 5) * 4 + ft; rel = (k1_perm(T % 5) >> (4 * rel)) & 7; }
        const int pc = ft * 8 + rel;
        woff[f] = 16384 + (8 * hh + q4) * 256 + (((pc >> 1) ^ (q4 << 2)) * 16) + (pc & 1) * 8;
    }
    f32x16 acc[2]; acc[0] = f32x16{}; acc[1] = f32x16{};
    float sx = 0.f, sxx = 0.f, cs0 = 0.f, cs1 = 0.f;
#pragma unroll
    for (int st = 0; st < 10; ++st) {
        if (st == 0) WAIT_BAR(12); else if (st <= 7) WAIT_BAR(8); else if (st == 8) WAIT_BAR(4); else WAIT_BAR(0);
        if (st >= 1 && st + 3 < 10) issue(st + 3);
        const lds_cptr sb = (lds_cptr)lds + (st & 3) * K1_STAGE;
#pragma unroll
        for (int ks = 0; ks < 4; ++ks) {
            const bf16x8 xf = lds_r128(sb + (xoff ^ (ks << 5)));
            const bf16x8 w0 = cat8(lds_tr(sb + woff[0] + ks * 4096), lds_tr(sb + woff[0] + ks * 4096 + 1024));
            const bf16x8 w1 = cat8(lds_tr(sb + woff[1] + ks * 4096), lds_tr(sb + woff[1] + ks * 4096 + 1024));
            if (VORI) { acc[0] = mfma16(xf, w0, acc[0]); acc[1] = mfma16(xf, w1, acc[1]); }
            else { acc[0] = mfma16(w0, xf, acc[0]); acc[1] = mfma16(w1, xf, acc[1]); }
            if ((ks & 1) == wn) {
                const u32x4 xu = __builtin_bit_cast(u32x4, xf);
#pragma unroll
                for (int i = 0; i < 4; ++i) { sx = dot2(xu[i], ONES2, sx); sxx = dot2(xu[i], xu[i], sxx); }
            }
            if (ks == wm) {
                const u32x4 a = __builtin_bit_cast(u32x4, w0), b = __builtin_bit_cast(u32x4, w1);
#pragma unroll
                for (int i = 0; i < 4; ++i) { cs0 = dot2(a[i], ONES2, cs0); cs1 = dot2(b[i], ONES2, cs1); }
            }
        }
    }
    f32x2* tokp = (f32x2*)(lds + K1_STAGE * K1_NST);
    float* colp = (float*)(lds + K1_STAGE * K1_NST + 4096);
    float* rsl = (float*)(lds + K1_STAGE * K1_NST + 8192) + wid * 32;
    tokp[(wn * 2 + hh) * 128 + xrow] = f32x2{sx, sxx};
    colp[(wm * 2 + hh) * 128 + wn * 64 + r] = cs0;
    colp[(wm * 2 + hh) * 128 + wn * 64 + 32 + r] = cs1;
    __syncthreads();
    float tsx = 0.f, tsxx = 0.f;
#pragma unroll
    for (int i = 0; i < 4; ++i) { const f32x2 t = tokp[i * 128 + xrow]; tsx += t[0]; tsxx += t[1]; }
    float c0 = 0.f, c1 = 0.f;
#pragma unroll
    for (int i = 0; i < 8; ++i) { c0 += colp[i * 128 + wn * 64 + r]; c1 += colp[i * 128 + wn * 64 + 32 + r]; }
    const float mu = tsx / (float)D_;
    const float ex2 = tsxx / (float)D_;
    float var = ex2 - mu * mu;
    if (__builtin_expect(__any(var < ex2 * (1.f / 64.f)), 0)) {
        const u32x4* rowp = (const u32x4*)(x + (size_t)(mt * 128 + xrow) * D_) + hh * 40;
        float a = 0.f;
        for (int i = 0; i < 40; ++i) {
            const u32x4 u = rowp[i];
#pragma unroll
            for (int j = 0; j < 4; ++j) { const float d0 = bf_lo(u[j]) - mu, d1 = bf_hi(u[j]) - mu; a = __builtin_fmaf(d0, d0, a); a = __builtin_fmaf(d1, d1, a); }
        }
        var = swap32_add(a) / (float)D_;
    }
    const float rstd = 1.0f / __builtin_sqrtf(var + EPS);
    if (nt == 0 && wn == 0 && hh == 0) {
        const float sig2 = __builtin_sqrtf(var * (1.f + EPS) + EPS * EPS);
        *(f32x4*)(ws + WS_TOK + (size_t)(mt * 128 + xrow) * 16) = f32x4{mu, 1.0f / sig2, sig2, 0.f};
    }
    {
        unsigned m1, m2, m3; split3(-mu, m1, m2, m3);
        u32x4 tf = {m1 | (m1 << 16), m2 | (m1 << 16), m2 | (m3 << 16), m2 | (m3 << 16)};
        if (hh) tf = u32x4{0, 0, 0, 0};
        unsigned a1, a2, a3; split3(c0, a1, a2, a3);
        const u32x4 f0 = {a1 | (a2 << 16), a1 | (a3 << 16), a2 | (a1 << 16), a3 | (a2 << 16)};
        split3(c1, a1, a2, a3);
        const u32x4 f1 = {a1 | (a2 << 16), a1 | (a3 << 16), a2 | (a1 << 16), a3 | (a2 << 16)};
        const bf16x8 tfb = __builtin_bit_cast(bf16x8, tf), f0b = __builtin_bit_cast(bf16x8, f0), f1b = __builtin_bit_cast(bf16x8, f1);
        if (VORI) { acc[0] = mfma16(tfb, f0b, acc[0]); acc[1] = mfma16(tfb, f1b, acc[1]); }
        else { acc[0] = mfma16(f0b, tfb, acc[0]); acc[1] = mfma16(f1b, tfb, acc[1]); }
    }
    const int tt = mt * 4 + wm;
    if (!VORI) {
        const bool isq = nt < 5;
        const float sc = isq ? rstd * QSCALE : rstd;
        char* base = isq ? ws + WS_Q + (size_t)tt * QT_B : ws + WS_KV + (size_t)tt * KVT_B;
        const size_t hstride = isq ? (size_t)64 * QT_B : (size_t)64 * KVT_B;
#pragma unroll
        for (int f = 0; f < 2; ++f) {
            const int T = (nt % 5) * 4 + 2 * wn + f;
            const unsigned perm = k1_perm(T % 5);
            unsigned u[8];
#pragma unroll
            for (int i = 0; i < 8; ++i) u[i] = pk2(acc[f][2 * i] * sc, acc[f][2 * i + 1] * sc);
            int head;
            int off = k1_piece_off(8 * T + ((perm >> (4 * hh)) & 7), r, head);
            *(u32x4*)(base + head * hstride + off) = u32x4{u[0], u[1], u[2], u[3]};
            off = k1_piece_off(8 * T + ((perm >> (4 * (4 + hh))) & 7), r, head);
            *(u32x2*)(base + head * hstride + off) = u32x2{u[4], u[5]};
            off = k1_piece_off(8 * T + ((perm >> (4 * (6 + hh))) & 7), r, head);
            *(u32x2*)(base + head * hstride + off) = u32x2{u[6], u[7]};
        }
    } else {
        if (hh == 0) rsl[r] = rstd;
        __builtin_amdgcn_wave_barrier();
        asm volatile("s_waitcnt lgkmcnt(0)" ::: "memory");
        float rs[16];
#pragma unroll
        for (int g = 0; g < 4; ++g) { const f32x4 t = *(const f32x4*)(rsl + 8 * g + 4 * hh); rs[4 * g] = t[0]; rs[4 * g + 1] = t[1]; rs[4 * g + 2] = t[2]; rs[4 * g + 3] = t[3]; }
#pragma unroll
        for (int f = 0; f < 2; ++f) {
            const int col = (nt - 10) * 128 + wn * 64 + 32 * f + r;
            const int head = col / 20, d = col - head * 20;
            char* p = ws + WS_KV + ((size_t)head * 64 + tt) * KVT_B + 1280 + hh * 336 + d * 16;
            unsigned u[8];
#pragma unroll
            for (int i = 0; i < 8; ++i) u[i] = pk2(acc[f][2 * i] * rs[2 * i], acc[f][2 * i + 1] * rs[2 * i + 1]);
            *(u32x4*)(p) = u32x4{u[0], u[1], u[2], u[3]};
            *(u32x4*)(p + 672) = u32x4{u[4], u[5], u[6], u[7]};
            if (d == 19) { *(u32x4*)(p + 16) = u32x4{ONES2, ONES2, ONES2, ONES2}; *(u32x4*)(p + 672 + 16) = u32x4{ONES2, ONES2, ONES2, ONES2}; }
        }
    }
}

__global__ __launch_bounds__(512, 2) void k1_qkv(const __bf16* __restrict__ x, const __bf16* __restrict__ wqkv, const __bf16* __restrict__ w1, char* __restrict__ ws) {
    extern __shared__ __attribute__((aligned(16))) char lds[];
    const int b = blockIdx.x, xcd = b & 7, idx = b >> 3, mg = xcd >> 1;
    if ((xcd & 1) && idx >= 28) {
        const int job = mg * 4 + (idx - 28);
        const int tid = threadIdx.x;
        float* red = (float*)lds;
        const int ch = tid % 12, rg = tid / 12;
        if (rg < 40) {
            float a[8] = {0, 0, 0, 0, 0, 0, 0, 0};
            const char* p = (const char*)w1 + (size_t)rg * 16 * (F_ * 2) + job * 192 + ch * 16;
#pragma unroll
            for (int i = 0; i < 16; ++i) {
                const u32x4 u = *(const u32x4*)(p + (size_t)i * (F_ * 2));
#pragma unroll
                for (int j = 0; j < 4; ++j) { a[2 * j] += bf_lo(u[j]); a[2 * j + 1] += bf_hi(u[j]); }
            }
#pragma unroll
            for (int j = 0; j < 8; ++j) red[rg * 96 + ch * 8 + j] = a[j];
        }
        __syncthreads();
        if (tid < 96) {
            float s = 0.f;
            for (int i = 0; i < 40; ++i) s += red[i * 96 + tid];
            ((float*)(ws + WS_CS1))[job * 96 + tid] = s;
        }
        return;
    }
    const int mt = mg * 4 + (idx & 3), nt = (xcd & 1) * 8 + (idx >> 2);
    if (nt < 10) k1_body<false>(x, wqkv, ws, lds, mt, nt);
    else k1_body<true>(x, wqkv, ws, lds, mt, nt);
}

constexpr int AT_SLOT = 8 * KVT_B;
constexpr int AT_LDS = 3 * AT_SLOT + 256;

__device__ __forceinline__ void attn_slow(const char* __restrict__ kv, bf16x8 qf, s16x4 qt, f32x16& o, int r, int hh) {
    float m = -INFINITY;
    o = f32x16{};
    for (int t = 0; t < 64; ++t) {
        const char* p = kv + (size_t)t * KVT_B;
        const bf16x8 kf = *(const bf16x8*)(p + hh * 512 + r * 16);
        const s16x4 kt = *(const s16x4*)(p + 1024 + r * 8);
        const int rv = r < 20 ? r : 20;
        const bf16x8 v0 = *(const bf16x8*)(p + 1280 + hh * 336 + rv * 16);
        const bf16x8 v1 = *(const bf16x8*)(p + 1280 + 672 + hh * 336 + rv * 16);
        f32x16 s = mfma16(kf, qf, f32x16{});
        s = mfma8(kt, qt, s);
        float tm = s[0];
#pragma unroll
        for (int i = 1; i < 16; ++i) tm = __builtin_fmaxf(tm, s[i]);
        tm = swap32_max(tm);
        const float mn = __builtin_fmaxf(m, tm);
        const float al = __builtin_amdgcn_exp2f(m - mn);
        m = mn;
        u32x4 p0, p1;
#pragma unroll
        for (int i = 0; i < 4; ++i) {
            p0[i] = pk2(__builtin_amdgcn_exp2f(s[2 * i] - m), __builtin_amdgcn_exp2f(s[2 * i + 1] - m));
            p1[i] = pk2(__builtin_amdgcn_exp2f(s[8 + 2 * i] - m), __builtin_amdgcn_exp2f(s[8 + 2 * i + 1] - m));
        }
#pragma unroll
        for (int i = 0; i < 16; ++i) o[i] *= al;
        o = mfma16(v0, __builtin_bit_cast(bf16x8, p0), o);
        o = mfma16(v1, __builtin_bit_cast(bf16x8, p1), o);
    }
}

__global__ __launch_bounds__(512, 2) void k2_attn(char* __restrict__ ws) {
    extern __shared__ __attribute__((aligned(16))) char lds[];
    const int tid = threadIdx.x, lane = tid & 63, r = lane & 31, hh = lane >> 5;
    const int wid = __builtin_amdgcn_readfirstlane(tid >> 6);
    const int b = blockIdx.x, head = (b & 7) * 4 + ((b >> 3) & 3), qb = b >> 5;
    const int qt_idx = qb * 8 + wid;
    const unsigned lds0 = (unsigned)(uintptr_t)lds;
    const char* kvh = ws + WS_KV + (size_t)head * 64 * KVT_B;
    const char* src = kvh + (size_t)wid * KVT_B + lane * 16;
    auto issue = [&](int c, int slot) {
        const unsigned base = lds0 + slot * AT_SLOT + wid * KVT_B;
        const char* s = src + (size_t)c * AT_SLOT;
        glds16(s, base);
        glds16(s + 1024, base + 1024);
        if (lane < 36) glds16(s + 2048, base + 2048);
    };
    issue(0, 0); issue(1, 1);
    const char* qp = ws + WS_Q + ((size_t)head * 64 + qt_idx) * QT_B;
    const bf16x8 qf = *(const bf16x8*)(qp + hh * 512 + r * 16);
    s16x4 qt = *(const s16x4*)(qp + 1024 + r * 8);
    if (hh) qt = s16x4{0, 0, 0, 0};
    const int koff = hh * 512 + r * 16, ktoff = 1024 + r * 8, voff = 1280 + hh * 336 + r * 16;

    f32x16 o = f32x16{};
    f32x16 negm, sA, sB;
    u32x4 pA0, pA1, pB0, pB1;
    bf16x8 kf, v0, v1; s16x4 kt;
    WAIT_BAR(3);
    issue(2, 2);
    {
        const lds_cptr sb = (lds_cptr)lds;
        kf = lds_r128(sb + koff); kt = lds_r64(sb + ktoff);
        f32x16 s = mfma16(kf, qf, f32x16{});
        s = mfma8(kt, qt, s);
        float tm = s[0];
#pragma unroll
        for (int i = 1; i < 16; ++i) tm = __builtin_fmaxf(tm, s[i]);
        tm = -swap32_max(tm);
#pragma unroll
        for (int i = 0; i < 16; ++i) { negm[i] = tm; sA[i] = s[i] + tm; }
        PIN(negm);
        kf = lds_r128(sb + KVT_B + koff); kt = lds_r64(sb + KVT_B + ktoff);
        pB0 = u32x4{0, 0, 0, 0}; pB1 = u32x4{0, 0, 0, 0};
        v0 = lds_r128(sb + voff); v1 = v0;
    }
#define EX(v) __builtin_amdgcn_exp2f(v)
#define GRP(MF, SC, i, PW, j) do { MF; const float e0 = EX(SC[i]), e1 = EX(SC[i + 1]), e2 = EX(SC[i + 2]), e3 = EX(SC[i + 3]); PW[j] = pk2(e0, e1); PW[j + 1] = pk2(e2, e3); PIN(PW); SBAR(); } while (0)
#define STEP(SC, SN, PC0, PC1, PP0, PP1, KP, VP) do { \
        GRP(SN = mfma16(kf, qf, negm), SC, 0, PC0, 0); \
        kf = lds_r128((KP) + koff); SBAR(); \
        GRP(o = mfma16(v0, __builtin_bit_cast(bf16x8, PP0), o), SC, 4, PC0, 2); \
        v0 = lds_r128((VP) + voff); SBAR(); \
        GRP(SN = mfma8(kt, qt, SN), SC, 8, PC1, 0); \
        kt = lds_r64((KP) + ktoff); SBAR(); \
        GRP(o = mfma16(v1, __builtin_bit_cast(bf16x8, PP1), o), SC, 12, PC1, 2); \
        v1 = lds_r128((VP) + voff + 672); SBAR(); \
    } while (0)
    int slot = 0;
    for (int c = 0; c < 8; ++c) {
        const int nslot = slot == 2 ? 0 : slot + 1;
        const lds_cptr sb = (lds_cptr)lds + slot * AT_SLOT;
        const lds_cptr nb = (lds_cptr)lds + nslot * AT_SLOT;
        STEP(sA, sB, pA0, pA1, pB0, pB1, sb + 2 * KVT_B, sb + 0 * KVT_B);
        STEP(sB, sA, pB0, pB1, pA0, pA1, sb + 3 * KVT_B, sb + 1 * KVT_B);
        STEP(sA, sB, pA0, pA1, pB0, pB1, sb + 4 * KVT_B, sb + 2 * KVT_B);
        STEP(sB, sA, pB0, pB1, pA0, pA1, sb + 5 * KVT_B, sb + 3 * KVT_B);
        STEP(sA, sB, pA0, pA1, pB0, pB1, sb + 6 * KVT_B, sb + 4 * KVT_B);
        STEP(sB, sA, pB0, pB1, pA0, pA1, sb + 7 * KVT_B, sb + 5 * KVT_B);
        WAIT_BAR(0);
        if (c >= 1 && c + 2 < 8) issue(c + 2, slot == 0 ? 2 : slot - 1);
        STEP(sA, sB, pA0, pA1, pB0, pB1, nb + 0 * KVT_B, sb + 6 * KVT_B);
        STEP(sB, sA, pB0, pB1, pA0, pA1, nb + 1 * KVT_B, sb + 7 * KVT_B);
        slot = nslot;
    }
    o = mfma16(v0, __builtin_bit_cast(bf16x8, pB0), o);
    o = mfma16(v1, __builtin_bit_cast(bf16x8, pB1), o);
#undef STEP
#undef GRP
#undef EX
    float l = swap32_add(hh ? o[8] : 0.f);
    bool bad = !(l < 1e30f);
#pragma unroll
    for (int i = 0; i < 8; ++i) bad |= !(__builtin_fabsf(o[i]) < 3e38f);
#pragma unroll
    for (int i = 8; i < 12; ++i) bad |= (hh == 0) && !(__builtin_fabsf(o[i]) < 3e38f);
    if (__builtin_expect(__any(bad), 0)) {
        attn_slow(kvh, qf, qt, o, r, hh);
        l = swap32_add(hh ? o[8] : 0.f);
    }
    const float inv = 1.0f / l;
    char* ob = ws + WS_O + (size_t)qt_idx * 40 * 1024;
    *(u32x4*)(ob + head * 1024 + lane * 16) = u32x4{pk2(o[0] * inv, o[1] * inv), pk2(o[2] * inv, o[3] * inv), pk2(o[4] * inv, o[5] * inv), pk2(o[6] * inv, o[7] * inv)};
    if (hh == 0) *(u32x2*)(ob + (32 + (head >> 2)) * 1024 + (((head >> 1) & 1) * 32 + r) * 16 + (head & 1) * 8) = u32x2{pk2(o[8] * inv, o[9] * inv), pk2(o[10] * inv, o[11] * inv)};
}

constexpr int M1_STAGE = 16384 + 12288, M1_LDS = 3 * M1_STAGE;

__device__ __forceinline__ float gelu_tanh(float v) {
    constexpr float C0 = -2.0f * 0.7978845608028654f * 1.4426950408889634f, C1 = C0 * 0.044715f;
    const float z = v * __builtin_fmaf(v * v, C1, C0);
    return v * __builtin_amdgcn_rcpf(1.0f + __builtin_amdgcn_exp2f(z));
}

__global__ __launch_bounds__(256, 2) void k_mlp1(const __bf16* __restrict__ x, const __bf16* __restrict__ w1, const __bf16* __restrict__ b1, char* __restrict__ ws) {
    extern __shared__ __attribute__((aligned(16))) char lds[];
    const int tid = threadIdx.x, lane = tid & 63, r = lane & 31, hh = lane >> 5;
    const int wid = __builtin_amdgcn_readfirstlane(tid >> 6);
    const int b = blockIdx.x, head = (b & 7) * 4 + ((b >> 3) & 3), qb = b >> 5;
    const int mt = qb * 2 + (head & 1), nt = head >> 1;
    const unsigned lds0 = (unsigned)(uintptr_t)lds;
    const char* xsrc[4]; const char* wsrc[3];
#pragma unroll
    for (int i = 0; i < 4; ++i) {
        const int row = 8 * (4 * wid + i) + (lane >> 3);
        xsrc[i] = (const char*)x + (size_t)(mt * 128 + row) * (D_ * 2) + (((lane & 7) ^ ((row >> 1) & 7)) * 16);
    }
#pragma unroll
    for (int i = 0; i < 3; ++i) {
        const int idx = (3 * wid + i) * 64 + lane, krow = idx / 12, ch = idx - krow * 12;
        wsrc[i] = (const char*)w1 + (size_t)krow * (F_ * 2) + nt * 192 + ch * 16;
    }
    auto issue = [&](int st, int slot) {
        const unsigned base = lds0 + slot * M1_STAGE;
#pragma unroll
        for (int i = 0; i < 4; ++i) glds16(xsrc[i] + st * 128, base + (4 * wid + i) * 1024);
#pragma unroll
        for (int i = 0; i < 3; ++i) glds16(wsrc[i] + (size_t)st * 64 * (F_ * 2), base + 16384 + (3 * wid + i) * 1024);
    };
    issue(0, 0); issue(1, 1); issue(2, 2);
    const int xrow = 32 * wid + r;
    const int xoff = xrow * 128 + ((hh ^ ((xrow >> 1) & 7)) * 16);
    const int q4 = (lane & 15) >> 2, p4 = lane & 3, gi = (lane >> 4) & 1;
    const int woff = 16384 + (8 * hh + q4) * 192 + (4 * gi + p4) * 8;
    const f32x4 ts = *(const f32x4*)(ws + WS_TOK + (size_t)(mt * 128 + xrow) * 16);
    float csv[3]; unsigned b1v[3];
#pragma unroll
    for (int f = 0; f < 3; ++f) { csv[f] = ((const float*)(ws + WS_CS1))[nt * 96 + 32 * f + r]; b1v[f] = ((const unsigned short*)b1)[nt * 96 + 32 * f + r]; }
    f32x16 acc[3]; acc[0] = f32x16{}; acc[1] = f32x16{}; acc[2] = f32x16{};
#pragma unroll
    for (int st = 0; st < 10; ++st) {
        if (st == 0) WAIT_BAR(14); else if (st <= 8) WAIT_BAR(7); else WAIT_BAR(0);
        if (st >= 1 && st + 2 < 10) issue(st + 2, (st + 2) % 3);
        const lds_cptr sb = (lds_cptr)lds + (st % 3) * M1_STAGE;
#pragma unroll
        for (int ks = 0; ks < 4; ++ks) {
            const bf16x8 xf = lds_r128(sb + (xoff ^ (ks << 5)));
#pragma unroll
            for (int f = 0; f < 3; ++f) {
                const bf16x8 wf = cat8(lds_tr(sb + woff + ks * 3072 + f * 64), lds_tr(sb + woff + ks * 3072 + 768 + f * 64));
                acc[f] = mfma16(wf, xf, acc[f]);
            }
        }
    }
    {
        unsigned m1, m2, m3; split3(hh ? ts[2] : -ts[0], m1, m2, m3);
        u32x4 tf = {m1 | (m1 << 16), m2 | (m1 << 16), m2 | (m3 << 16), m2 | (m3 << 16)};
        if (hh) tf = u32x4{m1 | (m2 << 16), m3, 0, 0};
#pragma unroll
        for (int f = 0; f < 3; ++f) {
            unsigned a1, a2, a3; split3(csv[f], a1, a2, a3);
            u32x4 ff = {a1 | (a2 << 16), a1 | (a3 << 16), a2 | (a1 << 16), a3 | (a2 << 16)};
            if (hh) ff = u32x4{b1v[f] | (b1v[f] << 16), b1v[f], 0, 0};
            acc[f] = mfma16(__builtin_bit_cast(bf16x8, ff), __builtin_bit_cast(bf16x8, tf), acc[f]);
        }
    }
    const float s2 = ts[1];
    char* hb = ws + WS_H + ((size_t)(mt * 4 + wid) * 96 + nt * 6) * 1024 + lane * 16;
#pragma unroll
    for (int f = 0; f < 3; ++f) {
#pragma unroll
        for (int s = 0; s < 2; ++s) {
            u32x4 u;
#pragma unroll
            for (int i = 0; i < 4; ++i) u[i] = pk2(gelu_tanh(acc[f][8 * s + 2 * i] * s2), gelu_tanh(acc[f][8 * s + 2 * i + 1] * s2));
            *(u32x4*)(hb + (2 * f + s) * 1024) = u;
        }
    }
}

constexpr int M2_STAGE = 20480, M2_LDS = 4 * M2_STAGE;

__global__ __launch_bounds__(256, 2) void k_mlp2(const __bf16* __restrict__ x, const __bf16* __restrict__ w2, const __bf16* __restrict__ b2, char* __restrict__ ws) {
    extern __shared__ __attribute__((aligned(16))) char lds[];
    const int tid = threadIdx.x, lane = tid & 63, r = lane & 31, hh = lane >> 5;
    const int wid = __builtin_amdgcn_readfirstlane(tid >> 6), wm = wid & 1, wk = wid >> 1;
    const int b = blockIdx.x;
    const int ng = b & 3, kh = (b >> 2) & 1, mt = b >> 3;
    const unsigned lds0 = (unsigned)(uintptr_t)lds;
    const char* wsrc[5];
#pragma unroll
    for (int i = 0; i < 5; ++i) {
        const int idx = (5 * wid + i) * 64 + lane, row = idx / 20, ch = idx - row * 20;
        const int k = kh * 768 + (row >> 5) * 384 + (row & 31);
        wsrc[i] = (const char*)w2 + (size_t)k * (D_ * 2) + ng * 320 + ch * 16;
    }
    auto issue = [&](int st) {
        const unsigned base = lds0 + (st & 3) * M2_STAGE + wid * 5120;
#pragma unroll
        for (int i = 0; i < 5; ++i) glds16(wsrc[i] + (size_t)st * 32 * (D_ * 2), base + i * 1024);
    };
    issue(0); issue(1); issue(2); issue(3);
    const int q4 = (lane & 15) >> 2, p4 = lane & 3, gi = (lane >> 4) & 1;
    const int piece = (0x75643120u >> (4 * (4 * gi + p4))) & 7;
    const int woff = wk * 10240 + (4 * hh + q4) * 320 + piece * 8;
    const int tt = mt * 2 + wm;
    const char* hp = ws + WS_H + ((size_t)tt * 96 + kh * 48 + wk * 24) * 1024 + lane * 16;
    f32x16 acc[5];
#pragma unroll
    for (int f = 0; f < 5; ++f) acc[f] = f32x16{};
    bf16x8 hf[24];
#pragma unroll
    for (int i = 0; i < 24; ++i) hf[i] = *(const bf16x8*)(hp + i * 1024);
#pragma unroll
    for (int st = 0; st < 12; ++st) {
        if (st == 0) { asm volatile("s_waitcnt vmcnt(0)" ::: "memory"); WAIT_BAR(0); } else if (st <= 9) WAIT_BAR(10); else if (st == 10) WAIT_BAR(5); else WAIT_BAR(0);
        if (st >= 1 && st + 3 < 12) issue(st + 3);
        const lds_cptr sb = (lds_cptr)lds + (st & 3) * M2_STAGE;
#pragma unroll
        for (int ks = 0; ks < 2; ++ks) {
#pragma unroll
            for (int f = 0; f < 5; ++f) {
                const bf16x8 wf = cat8(lds_tr(sb + woff + ks * 5120 + f * 64), lds_tr(sb + woff + ks * 5120 + 2560 + f * 64));
                acc[f] = mfma16(wf, hf[2 * st + ks], acc[f]);
            }
        }
    }
    __syncthreads();
    f32x4* red = (f32x4*)(lds + wm * 20480);
    if (wk == 1) {
#pragma unroll
        for (int f = 0; f < 5; ++f)
#pragma unroll
            for (int g = 0; g < 4; ++g) red[(f * 4 + g) * 64 + lane] = f32x4{acc[f][4 * g], acc[f][4 * g + 1], acc[f][4 * g + 2], acc[f][4 * g + 3]};
    }
    __syncthreads();
    if (wk == 0) {
        char* pp = ws + WS_PART + (((size_t)kh * 64 + tt) * 20 + ng * 5) * 4096 + lane * 16;
#pragma unroll
        for (int f = 0; f < 5; ++f) {
#pragma unroll
            for (int c = 0; c < 2; ++c) {
                f32x4 a = red[(f * 4 + 2 * c) * 64 + lane], bb = red[(f * 4 + 2 * c + 1) * 64 + lane];
#pragma unroll
                for (int i = 0; i < 4; ++i) { a[i] += acc[f][8 * c + i]; bb[i] += acc[f][8 * c + 4 + i]; }
                if (kh == 0) {
                    const int col = ng * 160 + f * 32 + c * 16 + hh * 8;
                    const u32x4 xv = *(const u32x4*)(x + (size_t)(tt * 32 + r) * D_ + col);
                    const u32x4 bv = *(const u32x4*)(b2 + col);
                    a[0] += bf_lo(xv[0]) + bf_lo(bv[0]); a[1] += bf_hi(xv[0]) + bf_hi(bv[0]); a[2] += bf_lo(xv[1]) + bf_lo(bv[1]); a[3] += bf_hi(xv[1]) + bf_hi(bv[1]);
                    bb[0] += bf_lo(xv[2]) + bf_lo(bv[2]); bb[1] += bf_hi(xv[2]) + bf_hi(bv[2]); bb[2] += bf_lo(xv[3]) + bf_lo(bv[3]); bb[3] += bf_hi(xv[3]) + bf_hi(bv[3]);
                }
                *(f32x4*)(pp + f * 4096 + (2 * c) * 1024) = a;
                *(f32x4*)(pp + f * 4096 + (2 * c + 1) * 1024) = bb;
            }
        }
    }
}

constexpr int K3_LDS = 128 * 320 * 2 + 5 * 4096;

__global__ __launch_bounds__(640, 1) void k3_out(const __bf16* __restrict__ wo, const char* __restrict__ ws, __bf16* __restrict__ out) {
    extern __shared__ __attribute__((aligned(16))) char lds[];
    const int tid = threadIdx.x, lane = tid & 63, r = lane & 31, hh = lane >> 5;
    const int wid = __builtin_amdgcn_readfirstlane(tid >> 6), f = wid % 5, kq = wid / 5;
    const int b = blockIdx.x, ng = b & 3, tt = (b >> 3) * 2 + ((b >> 2) & 1);
    const unsigned lds0 = (unsigned)(uintptr_t)lds;
    const char* wsrc[5];
#pragma unroll
    for (int i = 0; i < 5; ++i) {
        const int idx = (5 * wid + i) * 64 + lane, row = idx / 20, ch = idx - row * 20;
        const int k = (row / 80) * 320 + (row % 80);
        wsrc[i] = (const char*)wo + (size_t)k * (D_ * 2) + ng * 320 + ch * 16;
    }
    auto issue = [&](int rd) {
        const unsigned base = lds0 + (rd % 3) * 51200 + wid * 5120;
#pragma unroll
        for (int i = 0; i < 5; ++i) glds16(wsrc[i] + (size_t)rd * 80 * (D_ * 2), base + i * 1024);
    };
    issue(0); issue(1); issue(2);
    const char* op = ws + WS_O + (size_t)tt * 40 * 1024 + lane * 16;
    bf16x8 of[20];
#pragma unroll
    for (int rd = 0; rd < 4; ++rd) {
#pragma unroll
        for (int i = 0; i < 4; ++i) of[rd * 5 + i] = *(const bf16x8*)(op + (kq * 16 + rd * 4 + i) * 1024);
        of[rd * 5 + 4] = *(const bf16x8*)(op + (32 + kq * 4 + rd) * 1024);
    }
    f32x16 acc;
    {
        const char* pp = ws + WS_PART + (((size_t)kq * 64 + tt) * 20 + ng * 5 + f) * 4096 + lane * 16;
#pragma unroll
        for (int g = 0; g < 4; ++g) { const f32x4 t = *(const f32x4*)(pp + g * 1024); acc[4 * g] = t[0]; acc[4 * g + 1] = t[1]; acc[4 * g + 2] = t[2]; acc[4 * g + 3] = t[3]; }
    }
    const int q4 = (lane & 15) >> 2, p4 = lane & 3, gi = (lane >> 4) & 1;
    const int piece = (0x75643120u >> (4 * (4 * gi + p4))) & 7;
    const int woff = kq * 25600 + (4 * hh + q4) * 320 + f * 64 + piece * 8;
    const int wtoff = kq * 25600 + (40 * hh + 16 + q4) * 320 + f * 64 + piece * 8;
#pragma unroll
    for (int rd = 0; rd < 4; ++rd) {
        WAIT_BAR(0);
        if (rd == 1) issue(3);
        const lds_cptr sb = (lds_cptr)lds + (rd % 3) * 51200;
#pragma unroll
        for (int i = 0; i < 4; ++i) {
            const bf16x8 wf = cat8(lds_tr(sb + woff + i * 6400), lds_tr(sb + woff + i * 6400 + 2560));
            acc = mfma16(wf, of[rd * 5 + i], acc);
        }
        const bf16x8 wf = cat8(lds_tr(sb + wtoff), lds_tr(sb + wtoff + 6400));
        acc = mfma16(wf, of[rd * 5 + 4], acc);
    }
    __syncthreads();
    f32x4* red = (f32x4*)(lds + f * 4096);
    if (kq == 1) {
#pragma unroll
        for (int g = 0; g < 4; ++g) red[g * 64 + lane] = f32x4{acc[4 * g], acc[4 * g + 1], acc[4 * g + 2], acc[4 * g + 3]};
    }
    __syncthreads();
    if (kq == 0) {
#pragma unroll
        for (int c = 0; c < 2; ++c) {
            const f32x4 a = red[(2 * c) * 64 + lane], bb = red[(2 * c + 1) * 64 + lane];
            u32x4 u;
            u[0] = pk2(a[0] + acc[8 * c], a[1] + acc[8 * c + 1]); u[1] = pk2(a[2] + acc[8 * c + 2], a[3] + acc[8 * c + 3]);
            u[2] = pk2(bb[0] + acc[8 * c + 4], bb[1] + acc[8 * c + 5]); u[3] = pk2(bb[2] + acc[8 * c + 6], bb[3] + acc[8 * c + 7]);
            *(u32x4*)(out + (size_t)(tt * 32 + r) * D_ + ng * 160 + f * 32 + c * 16 + hh * 8) = u;
        }
    }
}

}

extern "C" void kernel_launch(void* const* d_in, const int* in_sizes, int n_in, void* d_out, int out_size, void* d_ws, size_t ws_size, hipStream_t stream) {
    const __bf16* x = (const __bf16*)d_in[0];
    const __bf16* wqkv = (const __bf16*)d_in[1];
    const __bf16* wo = (const __bf16*)d_in[2];
    const __bf16* w1 = (const __bf16*)d_in[4];
    const __bf16* b1 = (const __bf16*)d_in[5];
    const __bf16* w2 = (const __bf16*)d_in[6];
    const __bf16* b2 = (const __bf16*)d_in[7];
    char* ws = (char*)d_ws;
    (void)hipFuncSetAttribute((const void*)k1_qkv, hipFuncAttributeMaxDynamicSharedMemorySize, K1_LDS);
    (void)hipFuncSetAttribute((const void*)k_mlp1, hipFuncAttributeMaxDynamicSharedMemorySize, M1_LDS);
    (void)hipFuncSetAttribute((const void*)k_mlp2, hipFuncAttributeMaxDynamicSharedMemorySize, M2_LDS);
    (void)hipFuncSetAttribute((const void*)k3_out, hipFuncAttributeMaxDynamicSharedMemorySize, 3 * 51200);
    k1_qkv<<<256, 512, K1_LDS, stream>>>(x, wqkv, w1, ws);
    k_mlp1<<<256, 256, M1_LDS, stream>>>(x, w1, b1, ws);
    k_mlp2<<<256, 256, M2_LDS, stream>>>(x, w2, b2, ws);
    k2_attn<<<256, 512, AT_LDS, stream>>>(ws);
    k3_out<<<256, 640, 3 * 51200, stream>>>(wo, ws, (__bf16*)d_out);
}
```

```cpp
#include <hip/hip_runtime.h>
#include <hip/hip_bf16.h>
#include <stdint.h>

namespace {

typedef __attribute__((ext_vector_type(8))) __bf16 bf16x8;
typedef __attribute__((ext_vector_type(2))) __bf16 bf16x2;
typedef __attribute__((ext_vector_type(8))) short s16x8;
typedef __attribute__((ext_vector_type(4))) short s16x4;
typedef __attribute__((ext_vector_type(16))) float f32x16;
typedef __attribute__((ext_vector_type(4))) float f32x4;
typedef __attribute__((ext_vector_type(2))) float f32x2;
typedef __attribute__((ext_vector_type(4))) unsigned u32x4;
typedef __attribute__((ext_vector_type(2))) unsigned u32x2;
typedef __attribute__((address_space(3))) char* lds_ptr;
typedef __attribute__((address_space(3))) const char* lds_cptr;

constexpr int S_ = 2048, D_ = 640, H_ = 32, F_ = 1536;
constexpr float EPS = 1e-5f;
constexpr float QSCALE = 0.22360679774997896f * 1.4426950408889634f;

constexpr int QT_B = 1280;
constexpr int KVT_B = 2624;
constexpr size_t WS_Q = 0;
constexpr size_t WS_KV = WS_Q + (size_t)H_ * 64 * QT_B;
constexpr size_t WS_TOK = WS_KV + (size_t)H_ * 64 * KVT_B + 4096;
constexpr size_t WS_CS1 = WS_TOK + (size_t)S_ * 16;
constexpr size_t WS_PW = WS_CS1 + 8192;
constexpr size_t WS_PWO = WS_PW + (size_t)16 * 240 * 1024;
constexpr size_t WS_PART = WS_PWO + (size_t)20 * 40 * 1024;
constexpr size_t WS_O = WS_PART + (size_t)4 * 64 * 20 * 2048;
constexpr size_t WS_END = WS_O + (size_t)64 * 40 * 1024;

#define STAMPW(kid, i, w)
#define STAMP(kid, i) STAMPW(kid, i, 0)
#define SBAR() __builtin_amdgcn_sched_barrier(0)
#define PIN(x) asm volatile("" : "+v"(x))
#define WAIT_BAR(N) asm volatile("s_waitcnt vmcnt(" #N ") lgkmcnt(0)\n\ts_barrier" ::: "memory")
#define WAIT_VM(N) asm volatile("s_waitcnt vmcnt(" #N ")" ::: "memory")

__device__ __forceinline__ f32x16 mfma16(bf16x8 a, bf16x8 b, f32x16 c) { return __builtin_amdgcn_mfma_f32_32x32x16_bf16(a, b, c, 0, 0, 0); }
__device__ __forceinline__ f32x16 mfma8(s16x4 a, s16x4 b, f32x16 c) { return __builtin_amdgcn_mfma_f32_32x32x8bf16_1k(a, b, c, 0, 0, 0); }
__device__ __forceinline__ unsigned pk2(float lo, float hi) { f32x2 v = {lo, hi}; return __builtin_bit_cast(unsigned, __builtin_convertvector(v, bf16x2)); }
__device__ __forceinline__ float bf_lo(unsigned u) { return __uint_as_float(u << 16); }
__device__ __forceinline__ float bf_hi(unsigned u) { return __uint_as_float(u & 0xffff0000u); }
__device__ __forceinline__ void glds16(const void* g, unsigned lds_base) {
    unsigned sv;
    asm volatile("s_mov_b32 %0, m0\n\ts_mov_b32 m0, %2\n\ts_nop 0\n\tglobal_load_lds_dwordx4 %1, off\n\ts_mov_b32 m0, %0" : "=&s"(sv) : "v"(g), "s"(lds_base) : "memory");
}
__device__ __forceinline__ void glds4(const void* g, unsigned lds_base) {
    unsigned sv;
    asm volatile("s_mov_b32 %0, m0\n\ts_mov_b32 m0, %2\n\ts_nop 0\n\tglobal_load_lds_dword %1, off\n\ts_mov_b32 m0, %0" : "=&s"(sv) : "v"(g), "s"(lds_base) : "memory");
}
__device__ __forceinline__ bf16x8 lds_r128(lds_cptr p) { return *(const __attribute__((address_space(3))) bf16x8*)p; }
__device__ __forceinline__ s16x4 lds_r64(lds_cptr p) { return *(const __attribute__((address_space(3))) s16x4*)p; }
__device__ __forceinline__ s16x4 lds_tr(lds_cptr p) { return __builtin_amdgcn_ds_read_tr16_b64_v4i16((__attribute__((address_space(3))) s16x4*)p); }
__device__ __forceinline__ bf16x8 cat8(s16x4 a, s16x4 b) { s16x8 v = {a[0], a[1], a[2], a[3], b[0], b[1], b[2], b[3]}; return __builtin_bit_cast(bf16x8, v); }
__device__ __forceinline__ float dot2(unsigned a, unsigned b, float c) { return __builtin_amdgcn_fdot2_f32_bf16(__builtin_bit_cast(bf16x2, a), __builtin_bit_cast(bf16x2, b), c, false); }
__device__ __forceinline__ float swap32_add(float v) { auto rr = __builtin_amdgcn_permlane32_swap(__float_as_uint(v), __float_as_uint(v), false, false); return __uint_as_float(rr[0]) + __uint_as_float(rr[1]); }
__device__ __forceinline__ float swap32_max(float v) { auto rr = __builtin_amdgcn_permlane32_swap(__float_as_uint(v), __float_as_uint(v), false, false); return __builtin_fmaxf(__uint_as_float(rr[0]), __uint_as_float(rr[1])); }
__device__ __forceinline__ void split3(float v, unsigned& a, unsigned& b, unsigned& c) {
    a = pk2(v, 0.f) & 0xffffu; float r = v - bf_lo(a);
    b = pk2(r, 0.f) & 0xffffu; r = r - bf_lo(b);
    c = pk2(r, 0.f) & 0xffffu;
}
constexpr unsigned ONES2 = 0x3f803f80u;
__device__ __forceinline__ void lds_signal(unsigned addr, unsigned val = 1u) {
    asm volatile("s_mov_b64 exec, 1\n\tds_add_u32 %0, %1\n\ts_mov_b64 exec, -1" :: "v"(addr), "v"(val) : "memory");
}
__device__ __forceinline__ unsigned lds_peek(unsigned addr) { return *(const volatile __attribute__((address_space(3))) unsigned*)(uintptr_t)addr; }
__device__ __forceinline__ void lds_await(unsigned addr, unsigned first, unsigned target) {
    unsigned v = __builtin_amdgcn_readfirstlane(first);
    while (v < target) { __builtin_amdgcn_s_sleep(1); v = __builtin_amdgcn_readfirstlane(lds_peek(addr)); }
    asm volatile("" ::: "memory");
}

constexpr int K1_STAGE = 16384 + 20480, K1_AUX = 4 * K1_STAGE, K1_LDS = K1_AUX + 4096 + 5120 + 1024 + 256;

__device__ __forceinline__ constexpr unsigned k1_perm(int j) {
    return j == 0 ? 0x76453120u : (j == 3 ? 0x57064231u : (j == 4 ? 0x76254130u : 0x75643120u));
}
__device__ __forceinline__ int k1_piece_off(int P, int r, int& head) {
    head = P / 5; int rr = P - head * 5; const int h7 = head & 7;
    int role = rr;
    if (h7 == 1) role = (0x32410 >> (4 * rr)) & 7;
    if (h7 == 3) role = (0x32104 >> (4 * rr)) & 7;
    return role == 4 ? 1024 + r * 8 : (role >> 1) * 512 + r * 16 + (role & 1) * 8;
}

template <bool VORI, int WN>
__device__ __forceinline__ void k1_body(const __bf16* __restrict__ x, const __bf16* __restrict__ w, char* __restrict__ ws, char* lds, int mt, int nt, int pfi) {
    constexpr int NF = WN ? 2 : 3, F0 = WN ? 3 : 0, NW = WN ? 3 : 2;
    const int tid = threadIdx.x, lane = tid & 63, r = lane & 31, hh = lane >> 5;
    const int wid = __builtin_amdgcn_readfirstlane(tid >> 6), wm = wid & 3;
    const unsigned lds0 = (unsigned)(uintptr_t)lds;
    const char* xsrc[2]; const char* wsrc[NW];
#pragma unroll
    for (int i = 0; i < 2; ++i) {
        const int row = 8 * (2 * wid + i) + (lane >> 3);
        xsrc[i] = (const char*)x + (size_t)(mt * 128 + row) * (D_ * 2) + (((lane & 7) ^ ((row >> 1) & 7)) * 16);
    }
    const int wp0 = WN ? 8 + 3 * wm : 2 * wm;
#pragma unroll
    for (int i = 0; i < NW; ++i) {
        const int idx = (wp0 + i) * 64 + lane, krow = idx / 20, ch = idx - krow * 20;
        wsrc[i] = (const char*)w + (size_t)krow * (3 * D_ * 2) + nt * 320 + ch * 16;
    }
    auto issue = [&](int st) {
        const unsigned base = lds0 + (st & 3) * K1_STAGE;
        glds16(xsrc[0] + st * 128, base + wid * 2048);
        glds16(xsrc[1] + st * 128, base + wid * 2048 + 1024);
#pragma unroll
        for (int i = 0; i < NW; ++i) glds16(wsrc[i] + (size_t)st * 64 * (3 * D_ * 2), base + 16384 + (wp0 + i) * 1024);
    };
    STAMP(0, 0);
    issue(0); issue(1); issue(2); issue(3);
    const bool pfw = WN == 0 && wm == 3 && pfi < 23;
    if (pfw) {
        const int id = pfi * 64 + lane;
        const char* p; size_t step;
        if (id < 512) { p = (const char*)x + (size_t)((mt & ~3) * 128 + id) * (D_ * 2); step = 128; }
        else { const int q = id - 512, row = q / 15, ln = q - row * 15; p = (const char*)w + (size_t)row * (3 * D_ * 2) + (nt / 6) * 1920 + ln * 128; step = (size_t)64 * (3 * D_ * 2); }
#pragma unroll
        for (int st = 4; st < 10; ++st) glds4(p + st * step, lds0 + K1_AUX + 4096 + 5120 + 1024);
    }
    const int xrow = 32 * wm + r;
    const int xoff = xrow * 128 + ((hh ^ ((xrow >> 1) & 7)) * 16);
    const int q4 = (lane & 15) >> 2, p4 = lane & 3, gi = (lane >> 4) & 1;
    int woff[NF];
#pragma unroll
    for (int f = 0; f < NF; ++f) {
        int rel = 4 * gi + p4;
        if (!VORI) rel = (k1_perm(F0 + f) >> (4 * rel)) & 7;
        woff[f] = 16384 + (8 * hh + q4) * 320 + (F0 + f) * 64 + rel * 8;
    }
    f32x16 acc[NF];
    float cs[NF];
#pragma unroll
    for (int f = 0; f < NF; ++f) { acc[f] = f32x16{}; cs[f] = 0.f; }
    float sx = 0.f, sxx = 0.f;
#pragma unroll
    for (int st = 0; st < 10; ++st) {
        if (WN) { if (st == 0) WAIT_BAR(15); else if (st <= 7) WAIT_BAR(10); else if (st == 8) WAIT_BAR(5); else WAIT_BAR(0); }
        else if (st < 4) { if (pfw) { if (st == 0) WAIT_BAR(18); else WAIT_BAR(14); } else { if (st == 0) WAIT_BAR(12); else WAIT_BAR(8); } }
        else { if (st <= 7) WAIT_BAR(8); else if (st == 8) WAIT_BAR(4); else WAIT_BAR(0); }
        if (st == 0) STAMP(0, 1);
        if (st == 5) STAMP(0, 2);
        if (st >= 1 && st + 3 < 10) issue(st + 3);
        const lds_cptr sb = (lds_cptr)lds + (st & 3) * K1_STAGE;
#pragma unroll
        for (int ks = 0; ks < 4; ++ks) {
            const bf16x8 xf = lds_r128(sb + (xoff ^ (ks << 5)));
            bf16x8 wf[NF];
#pragma unroll
            for (int f = 0; f < NF; ++f) wf[f] = cat8(lds_tr(sb + woff[f] + ks * 5120), lds_tr(sb + woff[f] + ks * 5120 + 1280));
#pragma unroll
            for (int f = 0; f < NF; ++f) acc[f] = VORI ? mfma16(xf, wf[f], acc[f]) : mfma16(wf[f], xf, acc[f]);
            if ((ks & 1) == WN) {
                const u32x4 xu = __builtin_bit_cast(u32x4, xf);
#pragma unroll
                for (int i = 0; i < 4; ++i) { sx = dot2(xu[i], ONES2, sx); sxx = dot2(xu[i], xu[i], sxx); }
            }
            if (ks == wm) {
#pragma unroll
                for (int f = 0; f < NF; ++f) {
                    const u32x4 a = __builtin_bit_cast(u32x4, wf[f]);
#pragma unroll
                    for (int i = 0; i < 4; ++i) cs[f] = dot2(a[i], ONES2, cs[f]);
                }
            }
        }
    }
    STAMP(0, 3);
    f32x2* tokp = (f32x2*)(lds + K1_AUX);
    float* colp = (float*)(lds + K1_AUX + 4096);
    float* rsl = (float*)(lds + K1_AUX + 4096 + 5120) + wid * 32;
    tokp[(WN * 2 + hh) * 128 + xrow] = f32x2{sx, sxx};
#pragma unroll
    for (int f = 0; f < NF; ++f) colp[(wm * 2 + hh) * 160 + (F0 + f) * 32 + r] = cs[f];
    __syncthreads();
    float tsx = 0.f, tsxx = 0.f;
#pragma unroll
    for (int i = 0; i < 4; ++i) { const f32x2 t = tokp[i * 128 + xrow]; tsx += t[0]; tsxx += t[1]; }
#pragma unroll
    for (int f = 0; f < NF; ++f) {
        float c = 0.f;
#pragma unroll
        for (int i = 0; i < 8; ++i) c += colp[i * 160 + (F0 + f) * 32 + r];
        cs[f] = c;
    }
    const float mu = tsx / (float)D_;
    const float ex2 = tsxx / (float)D_;
    float var = ex2 - mu * mu;
    if (__builtin_expect(__any(var < ex2 * (1.f / 64.f)), 0)) {
        const u32x4* rowp = (const u32x4*)(x + (size_t)(mt * 128 + xrow) * D_) + hh * 40;
        float a = 0.f;
        for (int i = 0; i < 40; ++i) {
            const u32x4 u = rowp[i];
#pragma unroll
            for (int j = 0; j < 4; ++j) { const float d0 = bf_lo(u[j]) - mu, d1 = bf_hi(u[j]) - mu; a = __builtin_fmaf(d0, d0, a); a = __builtin_fmaf(d1, d1, a); }
        }
        var = swap32_add(a) / (float)D_;
    }
    const float rstd = 1.0f / __builtin_sqrtf(var + EPS);
    if (nt == 0 && WN == 0 && hh == 0) {
        const float sig2 = __builtin_sqrtf(var * (1.f + EPS) + EPS * EPS);
        *(f32x4*)(ws + WS_TOK + (size_t)(mt * 128 + xrow) * 16) = f32x4{mu, 1.0f / sig2, sig2, 0.f};
    }
    {
        unsigned m1, m2, m3; split3(-mu, m1, m2, m3);
        u32x4 tf = {m1 | (m1 << 16), m2 | (m1 << 16), m2 | (m3 << 16), m2 | (m3 << 16)};
        if (hh) tf = u32x4{0, 0, 0, 0};
        const bf16x8 tfb = __builtin_bit_cast(bf16x8, tf);
#pragma unroll
        for (int f = 0; f < NF; ++f) {
            unsigned a1, a2, a3; split3(cs[f], a1, a2, a3);
            const u32x4 ff = {a1 | (a2 << 16), a1 | (a3 << 16), a2 | (a1 << 16), a3 | (a2 << 16)};
            acc[f] = VORI ? mfma16(tfb, __builtin_bit_cast(bf16x8, ff), acc[f]) : mfma16(__builtin_bit_cast(bf16x8, ff), tfb, acc[f]);
        }
    }
    STAMP(0, 4);
    const int tt = mt * 4 + wm;
    if (!VORI) {
        const bool isq = nt < 4;
        const float sc = isq ? rstd * QSCALE : rstd;
        char* base = isq ? ws + WS_Q + (size_t)tt * QT_B : ws + WS_KV + (size_t)tt * KVT_B;
        const size_t hstride = isq ? (size_t)64 * QT_B : (size_t)64 * KVT_B;
#pragma unroll
        for (int f = 0; f < NF; ++f) {
            const int T = (nt & 3) * 5 + F0 + f;
            const unsigned perm = k1_perm(F0 + f);
            unsigned u[8];
#pragma unroll
            for (int i = 0; i < 8; ++i) u[i] = pk2(acc[f][2 * i] * sc, acc[f][2 * i + 1] * sc);
            int head;
            int off = k1_piece_off(8 * T + ((perm >> (4 * hh)) & 7), r, head);
            *(u32x4*)(base + head * hstride + off) = u32x4{u[0], u[1], u[2], u[3]};
            off = k1_piece_off(8 * T + ((perm >> (4 * (4 + hh))) & 7), r, head);
            *(u32x2*)(base + head * hstride + off) = u32x2{u[4], u[5]};
            off = k1_piece_off(8 * T + ((perm >> (4 * (6 + hh))) & 7), r, head);
            *(u32x2*)(base + head * hstride + off) = u32x2{u[6], u[7]};
        }
    } else {
        if (hh == 0) rsl[r] = rstd;
        __builtin_amdgcn_wave_barrier();
        asm volatile("s_waitcnt lgkmcnt(0)" ::: "memory");
        float rs[16];
#pragma unroll
        for (int g = 0; g < 4; ++g) { const f32x4 t = *(const f32x4*)(rsl + 8 * g + 4 * hh); rs[4 * g] = t[0]; rs[4 * g + 1] = t[1]; rs[4 * g + 2] = t[2]; rs[4 * g + 3] = t[3]; }
#pragma unroll
        for (int f = 0; f < NF; ++f) {
            const int col = (nt - 8) * 160 + (F0 + f) * 32 + r;
            const int head = col / 20, d = col - head * 20;
            char* p = ws + WS_KV + ((size_t)head * 64 + tt) * KVT_B + 1280 + hh * 336 + d * 16;
            unsigned u[8];
#pragma unroll
            for (int i = 0; i < 8; ++i) u[i] = pk2(acc[f][2 * i] * rs[2 * i], acc[f][2 * i + 1] * rs[2 * i + 1]);
            *(u32x4*)(p) = u32x4{u[0], u[1], u[2], u[3]};
            *(u32x4*)(p + 672) = u32x4{u[4], u[5], u[6], u[7]};
            if (d == 19) { *(u32x4*)(p + 16) = u32x4{ONES2, ONES2, ONES2, ONES2}; *(u32x4*)(p + 672 + 16) = u32x4{ONES2, ONES2, ONES2, ONES2}; }
        }
    }
}

template <int KIND>
__device__ __forceinline__ void k1_pack(const __bf16* __restrict__ wsrc, char* __restrict__ ws, char* lds, int strip, int third) {
    constexpr int NI = KIND == 1 ? 8 : 10, NKS = KIND == 1 ? 4 : 5, ROWB = (KIND == 0 ? F_ : D_) * 2;
    const int tid = threadIdx.x, lane = tid & 63, r = lane & 31, hh = lane >> 5;
    const int wid = __builtin_amdgcn_readfirstlane(tid >> 6);
    const unsigned lds0 = (unsigned)(uintptr_t)lds + wid * 10240;
    const int row0 = third * 512 + wid * NI * 8;
    const char* src = (const char*)wsrc + (size_t)(row0 + (lane >> 3)) * ROWB + strip * 128 + (((lane & 7) ^ (((lane >> 4) & 1) * 4)) * 16);
#pragma unroll
    for (int i = 0; i < NI; ++i) glds16(src + (size_t)i * 8 * ROWB, lds0 + i * 1024);
    const int q4 = (lane & 15) >> 2, p4 = lane & 3, gi = (lane >> 4) & 1;
    int pc = 4 * gi + p4;
    if (KIND != 0) pc = (0x75643120u >> (4 * pc)) & 7;
    const lds_cptr sb = (lds_cptr)lds + wid * 10240;
    float cs[2] = {0.f, 0.f};
    WAIT_VM(0);
#pragma unroll
    for (int ks = 0; ks < NKS; ++ks) {
        int ra, rb;
        if (KIND == 0) { ra = 16 * ks + 8 * hh; rb = ra + 4; }
        else if (KIND == 1) { ra = 16 * ks + 4 * hh; rb = ra + 8; }
        else if (ks < 4) { ra = 20 * ks + 4 * hh; rb = ra + 8; }
        else { ra = 40 * hh + 16; rb = ra + 20; }
#pragma unroll
        for (int f = 0; f < 2; ++f) {
            const int co = ((((f * 8 + pc) >> 1) ^ ((q4 >> 1) * 4)) * 16) + (pc & 1) * 8;
            const bf16x8 fr = cat8(lds_tr(sb + (ra + q4) * 128 + co), lds_tr(sb + (rb + q4) * 128 + co));
            const int ft = strip * 2 + f;
            size_t dst;
            if (KIND == 0) dst = WS_PW + ((size_t)(ft / 3) * 240 + (wid * 5 + ks) * 3 + ft % 3) * 1024;
            else if (KIND == 1) { const int g = third * 32 + wid * 4 + ks; const int f5 = ft % 5; dst = WS_PW + ((size_t)((g / 24) * 4 + ft / 5) * 240 + (f5 < 3 ? 120 + (g % 24) * 3 + f5 : 192 + (g % 24) * 2 + f5 - 3)) * 1024; }
            else dst = WS_PWO + ((size_t)ft * 40 + (ks < 4 ? wid * 4 + ks : 32 + wid)) * 1024;
            *(bf16x8*)(ws + dst + lane * 16) = fr;
            if (KIND == 0) {
                const u32x4 a = __builtin_bit_cast(u32x4, fr);
#pragma unroll
                for (int i = 0; i < 4; ++i) cs[f] = dot2(a[i], ONES2, cs[f]);
            }
        }
    }
    if (KIND == 0) {
        float* red = (float*)(lds + 81920);
        red[(wid * 2 + hh) * 64 + r] = cs[0];
        red[(wid * 2 + hh) * 64 + 32 + r] = cs[1];
        __syncthreads();
        if (tid < 64) {
            float s = 0.f;
#pragma unroll
            for (int i = 0; i < 16; ++i) s += red[i * 64 + tid];
            ((float*)(ws + WS_CS1))[strip * 64 + tid] = s;
        }
    }
}

__global__ __launch_bounds__(512, 2) void k1_qkv(const __bf16* __restrict__ x, const __bf16* __restrict__ wqkv, const __bf16* __restrict__ wo, const __bf16* __restrict__ w1,
                                                 const __bf16* __restrict__ w2, char* __restrict__ ws) {
    extern __shared__ __attribute__((aligned(16))) char lds[];
    const int b = blockIdx.x, xcd = b & 7, idx = b >> 3;
    if (idx >= 24) {
        const int job = (idx - 24) * 8 + xcd;
        if (job < 24) k1_pack<0>(w1, ws, lds, job, 0);
        else if (job < 54) k1_pack<1>(w2, ws, lds, (job - 24) % 10, (job - 24) / 10);
        else k1_pack<2>(wo, ws, lds, job - 54, 0);
        STAMP(3, 0);
        return;
    }
    const int mt = (xcd >> 1) * 4 + (idx & 3), nt = (xcd & 1) * 6 + (idx >> 2);
    const bool up = threadIdx.x >= 256;
    if (nt < 8) { if (up) k1_body<false, 1>(x, wqkv, ws, lds, mt, nt, idx); else k1_body<false, 0>(x, wqkv, ws, lds, mt, nt, idx); }
    else { if (up) k1_body<true, 1>(x, wqkv, ws, lds, mt, nt, idx); else k1_body<true, 0>(x, wqkv, ws, lds, mt, nt, idx); }
    STAMP(0, 5);
}

#define MLP_NB 20
#define MLP_PRIO 3
template <int N> __device__ __forceinline__ void wait_vm() { asm volatile("s_waitcnt vmcnt(%0)" :: "n"(N) : "memory"); }
#define WAIT_VMX(N) wait_vm<(N)>()
constexpr int AT_SLOT = 8 * KVT_B;
constexpr int K2_X = 4 * AT_SLOT, K2_H = K2_X + 40960, K2_CNT = K2_H + 24576, K2_B2 = K2_CNT + 128, K2_LDS = K2_B2 + 1280;
constexpr int C_FULL = 0, C_FREE = 64, C_X = 112, C_H = 116;

__device__ __forceinline__ void attn_slow(const char* __restrict__ kv, bf16x8 qf, s16x4 qt, f32x16& o, int r, int hh) {
    float m = -INFINITY;
    o = f32x16{};
    if (hh) qt = s16x4{0, 0, 0, 0};
    for (int t = 0; t < 64; ++t) {
        const char* p = kv + (size_t)t * KVT_B;
        const bf16x8 kf = *(const bf16x8*)(p + hh * 512 + r * 16);
        const s16x4 kt = *(const s16x4*)(p + 1024 + r * 8);
        const int rv = r < 20 ? r : 20;
        const bf16x8 v0 = *(const bf16x8*)(p + 1280 + hh * 336 + rv * 16);
        const bf16x8 v1 = *(const bf16x8*)(p + 1280 + 672 + hh * 336 + rv * 16);
        f32x16 s = mfma16(kf, qf, f32x16{});
        s = mfma8(kt, qt, s);
        float tm = s[0];
#pragma unroll
        for (int i = 1; i < 16; ++i) tm = __builtin_fmaxf(tm, s[i]);
        tm = swap32_max(tm);
        const float mn = __builtin_fmaxf(m, tm);
        const float al = __builtin_amdgcn_exp2f(m - mn);
        m = mn;
        u32x4 p0, p1;
#pragma unroll
        for (int i = 0; i < 4; ++i) {
            p0[i] = pk2(__builtin_amdgcn_exp2f(s[2 * i] - m), __builtin_amdgcn_exp2f(s[2 * i + 1] - m));
            p1[i] = pk2(__builtin_amdgcn_exp2f(s[8 + 2 * i] - m), __builtin_amdgcn_exp2f(s[8 + 2 * i + 1] - m));
        }
#pragma unroll
        for (int i = 0; i < 16; ++i) o[i] *= al;
        o = mfma16(v0, __builtin_bit_cast(bf16x8, p0), o);
        o = mfma16(v1, __builtin_bit_cast(bf16x8, p1), o);
    }
}

__device__ __forceinline__ void attn_finish(char* __restrict__ ws, const char* kvh, bf16x8 qf, s16x4 qt, f32x16 o, int qt_idx, int head, int lane) {
    const int r = lane & 31, hh = lane >> 5;
    float l = swap32_add(hh ? o[8] : 0.f);
    bool bad = !(l < 1e30f);
#pragma unroll
    for (int i = 0; i < 8; ++i) bad |= !(__builtin_fabsf(o[i]) < 3e38f);
#pragma unroll
    for (int i = 8; i < 12; ++i) bad |= (hh == 0) && !(__builtin_fabsf(o[i]) < 3e38f);
    if (__builtin_expect(__any(bad), 0)) {
        attn_slow(kvh, qf, qt, o, r, hh);
        l = swap32_add(hh ? o[8] : 0.f);
    }
    const float inv = 1.0f / l;
    char* ob = ws + WS_O + (size_t)qt_idx * 40 * 1024;
    *(u32x4*)(ob + head * 1024 + lane * 16) = u32x4{pk2(o[0] * inv, o[1] * inv), pk2(o[2] * inv, o[3] * inv), pk2(o[4] * inv, o[5] * inv), pk2(o[6] * inv, o[7] * inv)};
    if (hh == 0) *(u32x2*)(ob + (32 + (head >> 2)) * 1024 + (((head >> 1) & 1) * 32 + r) * 16 + (head & 1) * 8) = u32x2{pk2(o[8] * inv, o[9] * inv), pk2(o[10] * inv, o[11] * inv)};
}

__device__ __forceinline__ void kv_issue(const char* src, unsigned dst, unsigned l16) {
    unsigned sv;
    asm volatile("s_mov_b32 %0, m0\n\ts_mov_b32 m0, %3\n\ts_nop 0\n\t"
                 "global_load_lds_dwordx4 %1, %2\n\tglobal_load_lds_dwordx4 %1, %2 offset:1024\n\t"
                 "global_load_lds_dwordx4 %1, %2 offset:2048\n\tglobal_load_lds_dwordx4 %1, %2 offset:3072\n\t"
                 "s_mov_b32 m0, %5\n\ts_nop 0\n\tglobal_load_lds_dwordx4 %1, %4\n\t"
                 "s_mov_b64 exec, 0xff\n\tglobal_load_lds_dwordx4 %1, %4 offset:1024\n\ts_mov_b64 exec, -1\n\ts_mov_b32 m0, %0"
                 : "=&s"(sv) : "v"(l16), "s"(src), "s"(dst), "s"(src + 4096), "s"(dst + 4096) : "memory");
}

__device__ __forceinline__ void k2_attn(char* __restrict__ ws, char* lds, int wid, int head, int qb) {
    const int lane = threadIdx.x & 63, r = lane & 31, hh = lane >> 5;
    const unsigned lds0 = (unsigned)(uintptr_t)lds, cnt = lds0 + K2_CNT;
    const int qt_idx = qb * 8 + wid;
    const char* kvh = ws + WS_KV + (size_t)head * 64 * KVT_B;
    const char* src = kvh + wid * KVT_B + lane * 16;
    auto tile_issue = [&](int c) {
        const unsigned base = lds0 + c * AT_SLOT + wid * KVT_B;
        glds16(src + c * AT_SLOT, base);
        glds16(src + c * AT_SLOT + 1024, base + 1024);
        if (lane < 36) glds16(src + c * AT_SLOT + 2048, base + 2048);
    };
    tile_issue(0);
    {
        const char* qp = ws + WS_Q + ((size_t)head * 64 + qt_idx) * QT_B + lane * 16;
        glds16(qp, lds0 + K2_H + wid * QT_B);
        if (lane < 16) glds16(qp + 1024, lds0 + K2_H + wid * QT_B + 1024);
    }
    if (wid == 0 && lane < 32) *(__attribute__((address_space(3))) unsigned*)(uintptr_t)(cnt + lane * 4) = lane == 8 ? 8u : 0u;
    asm volatile("s_waitcnt lgkmcnt(0)\n\ts_barrier" ::: "memory");
    const int koff = hh * 512 + r * 16, ktoff = hh ? 1600 : 1024 + r * 8, voff = 1280 + hh * 336 + r * 16;
    WAIT_VM(0);
    lds_signal(cnt + C_FULL);
    tile_issue(1);
    const lds_cptr qp = (lds_cptr)lds + K2_H + wid * QT_B;
    const bf16x8 qf = lds_r128(qp + koff);
    s16x4 qt = lds_r64(qp + 1024 + r * 8);
    lds_await(cnt + C_FULL, lds_peek(cnt + C_FULL), 8);
    STAMP(1, 1);
    f32x16 o = f32x16{}, sA, sB;
    u32x4 pA0, pA1, pB0 = {0, 0, 0, 0}, pB1 = {0, 0, 0, 0};
    bf16x8 kf, v0, v1; s16x4 kt;
    {
        const lds_cptr sb = (lds_cptr)lds;
        kf = lds_r128(sb + koff); kt = lds_r64(sb + ktoff);
        if (hh) qt = s16x4{0, 0, 0, 0};
        sA = mfma16(kf, qf, f32x16{});
        sA = mfma8(kt, qt, sA);
        float m0 = sA[0];
#pragma unroll
        for (int i = 1; i < 16; ++i) m0 = __builtin_fmaxf(m0, sA[i]);
        m0 = swap32_max(m0);
        const unsigned nm = pk2(-m0, 0.f) & 0xffffu;
        if (hh) qt[0] = (short)nm;
#pragma unroll
        for (int i = 0; i < 16; ++i) sA[i] += bf_lo(nm);
        kf = lds_r128(sb + KVT_B + koff); kt = lds_r64(sb + KVT_B + ktoff);
        v0 = lds_r128(sb + voff); v1 = v0;
    }
#define EX(v) __builtin_amdgcn_exp2f(v)
#define GRP(MF, SC, i, PW, j) do { MF; SBAR(); const float e0 = EX(SC[i]), e1 = EX(SC[i + 1]), e2 = EX(SC[i + 2]), e3 = EX(SC[i + 3]); PW[j] = pk2(e0, e1); PW[j + 1] = pk2(e2, e3); PIN(PW); SBAR(); } while (0)
#define STEP(SC, SN, PC0, PC1, PP0, PP1, KP, VP) do { \
        GRP(SN = mfma16(kf, qf, f32x16{}), SC, 0, PC0, 0); \
        kf = lds_r128((KP) + koff); SBAR(); \
        GRP(o = mfma16(v0, __builtin_bit_cast(bf16x8, PP0), o), SC, 4, PC0, 2); \
        v0 = lds_r128((VP) + voff); SBAR(); \
        GRP(SN = mfma8(kt, qt, SN), SC, 8, PC1, 0); \
        kt = lds_r64((KP) + ktoff); SBAR(); \
        GRP(o = mfma16(v1, __builtin_bit_cast(bf16x8, PP1), o), SC, 12, PC1, 2); \
        v1 = lds_r128((VP) + voff + 672); SBAR(); \
    } while (0)
    for (int c = 0; c < 8; ++c) {
        const lds_cptr sb = (lds_cptr)lds + (c & 3) * AT_SLOT;
        const lds_cptr nb = (lds_cptr)lds + ((c + 1) & 3) * AT_SLOT;
        STEP(sA, sB, pA0, pA1, pB0, pB1, sb + 2 * KVT_B, sb + 0 * KVT_B);
        STEP(sB, sA, pB0, pB1, pA0, pA1, sb + 3 * KVT_B, sb + 1 * KVT_B);
        STEP(sA, sB, pA0, pA1, pB0, pB1, sb + 4 * KVT_B, sb + 2 * KVT_B);
        if (c == 0) { WAIT_VM(0); lds_signal(cnt + C_FULL + 4); }
        STEP(sB, sA, pB0, pB1, pA0, pA1, sb + 5 * KVT_B, sb + 3 * KVT_B);
        STEP(sA, sB, pA0, pA1, pB0, pB1, sb + 6 * KVT_B, sb + 4 * KVT_B);
        const unsigned pk = lds_peek(cnt + C_FULL + 4 * (c + 1));
        SBAR();
        STEP(sB, sA, pB0, pB1, pA0, pA1, sb + 7 * KVT_B, sb + 5 * KVT_B);
        lds_await(cnt + C_FULL + 4 * (c + 1), pk, 8);
        STEP(sA, sB, pA0, pA1, pB0, pB1, nb + 0 * KVT_B, sb + 6 * KVT_B);
        STEP(sB, sA, pB0, pB1, pA0, pA1, nb + 1 * KVT_B, sb + 7 * KVT_B);
        lds_signal(cnt + C_FREE + 4 * c);
        STAMP(1, 2 + c);
    }
    o = mfma16(v0, __builtin_bit_cast(bf16x8, pB0), o);
    o = mfma16(v1, __builtin_bit_cast(bf16x8, pB1), o);
#undef STEP
#undef GRP
#undef EX
    attn_finish(ws, kvh, qf, qt, o, qt_idx, head, lane);
    STAMP(1, 10);
}

constexpr int mlp_cnt(int i, int nb) {
    int n = 239 - i < nb - 1 ? 239 - i : nb - 1;
    const int pos[6] = {0, 36, 78, 120, 150, 192}, cnt[6] = {12, 6, 6, 6, 6, 6};
    for (int e = 0; e < 6; ++e) if (pos[e] <= i && i < pos[e] + nb) n += cnt[e];
    return n;
}
__device__ __forceinline__ f32x2 pk_mul(f32x2 a, f32x2 b) { return a * b; }
__device__ __forceinline__ f32x2 pk_fma(f32x2 a, f32x2 b, f32x2 c) { return __builtin_elementwise_fma(a, b, c); }

__device__ __forceinline__ void k2_mlp(const __bf16* __restrict__ x, const __bf16* __restrict__ b1, const __bf16* __restrict__ b2, char* ws, char* lds, int w, int head, int j, int tt) {
    constexpr int NB = MLP_NB;
    const int lane = threadIdx.x & 63, r = lane & 31, hh = lane >> 5;
    const unsigned lds0 = (unsigned)(uintptr_t)lds, cnt = lds0 + K2_CNT;
    asm volatile("s_barrier" ::: "memory");
    __builtin_amdgcn_s_setprio(MLP_PRIO);
    lds_await(cnt + C_FULL, lds_peek(cnt + C_FULL), 8);
    const int l16 = lane * 16;
    f32x4 ts; float csv[3]; unsigned b1v[3];
    {
        const char* tp = ws + WS_TOK + (size_t)tt * 512;
        const char* cp = ws + WS_CS1 + (j * 384 + w * 96) * 4;
        const char* bp = (const char*)b1 + (j * 384 + w * 96) * 2;
        asm volatile("global_load_dwordx4 %0, %1, %2" : "=v"(ts) : "v"(r * 16), "s"(tp));
        asm volatile("global_load_dword %0, %3, %4\n\tglobal_load_dword %1, %3, %4 offset:128\n\tglobal_load_dword %2, %3, %4 offset:256" : "=&v"(csv[0]), "=&v"(csv[1]), "=&v"(csv[2]) : "v"(r * 4), "s"(cp));
        asm volatile("global_load_ushort %0, %3, %4\n\tglobal_load_ushort %1, %3, %4 offset:64\n\tglobal_load_ushort %2, %3, %4 offset:128" : "=&v"(b1v[0]), "=&v"(b1v[1]), "=&v"(b1v[2]) : "v"(r * 2), "s"(bp));
    }
    {
        const int row = 8 * w + (lane >> 3);
        const char* xs = (const char*)x + (size_t)(tt * 32 + row) * (D_ * 2) + (((lane & 7) ^ ((row >> 1) & 7)) * 16);
#pragma unroll
        for (int st = 0; st < 10; ++st) glds16(xs + st * 128, lds0 + K2_X + st * 4096 + w * 1024);
        if (lane < 20) glds16((const char*)b2 + w * 320 + lane * 16, lds0 + K2_B2 + w * 320);
    }
    const uint64_t wbase = (uint64_t)(ws + WS_PW + (size_t)(j * 4 + w) * 240 * 1024);
    const u32x4 wr = {(unsigned)wbase, (unsigned)(wbase >> 32) & 0xffffu, 240u * 1024u, 0x00020000u};
    bf16x8 wb[NB];
#define ABL_LIM 240
#define WLD(i) do { if ((i) < NB) asm volatile("buffer_load_dwordx4 %0, %1, %2, %3 offen offset:%4" : "=v"(wb[(i) % NB]) : "v"(l16), "s"(wr), "s"(((i) >> 2) * 4096), "n"(((i) & 3) * 1024)); \
        else if ((i) < ABL_LIM) asm volatile("buffer_load_dwordx4 %0, %1, %2, %3 offen offset:%4" : "+v"(wb[(i) % NB]) : "v"(l16), "s"(wr), "s"(((i) >> 2) * 4096), "n"(((i) & 3) * 1024)); SBAR(); } while (0)
#define WWAIT(i) asm volatile("s_waitcnt vmcnt(%1)" : "+v"(wb[(i) % NB]) : "n"(mlp_cnt(i, NB)))
#define MMA(i, b, c) ({ WWAIT(i); mfma16(wb[(i) % NB], b, c); })
#pragma unroll
    for (int i = 0; i < NB; ++i) WLD(i);
    const char* kvsrc = ws + WS_KV + (size_t)head * 64 * KVT_B + w * 5248;
    kv_issue(kvsrc + 2 * AT_SLOT, lds0 + 2 * AT_SLOT + w * 5248, l16);
    kv_issue(kvsrc + 3 * AT_SLOT, lds0 + 3 * AT_SLOT + w * 5248, l16);
    asm volatile("s_waitcnt vmcnt(%7)" : "+v"(ts), "+v"(csv[0]), "+v"(csv[1]), "+v"(csv[2]), "+v"(b1v[0]), "+v"(b1v[1]), "+v"(b1v[2]) : "n"(NB + 12) : "memory");
    lds_signal(cnt + C_X);
    lds_await(cnt + C_X, lds_peek(cnt + C_X), 4);
    STAMPW(2, 1, 8);
#define SVW(N)
#define SERVICE(NVM, cf, ci) do { \
        SVW(NVM); \
        lds_signal(cnt + C_FULL + 4 * (cf), 2u); \
        if ((cf) == 3) lds_signal(cnt + C_FULL + 8, 2u); \
        if ((ci) < 8) { \
            lds_await(cnt + C_FREE + 4 * ((ci) - 4), lds_peek(cnt + C_FREE + 4 * ((ci) - 4)), 8); \
            kv_issue(kvsrc + (size_t)(ci) * AT_SLOT, lds0 + ((ci) & 3) * AT_SLOT + w * 5248, l16); \
        } \
        STAMPW(2, (cf) - 1, 8); \
    } while (0)
    const lds_cptr xb = (lds_cptr)lds + K2_X;
    unsigned xa[4];
#pragma unroll
    for (int k = 0; k < 4; ++k) { xa[k] = lds0 + K2_X + r * 128 + (((2 * k + hh) ^ ((r >> 1) & 7)) * 16); PIN(xa[k]); }
    f32x16 acc[3]; acc[0] = f32x16{}; acc[1] = f32x16{}; acc[2] = f32x16{};
    bf16x8 xf[2];
    xf[0] = lds_r128((lds_cptr)(uintptr_t)xa[0]);
#pragma unroll
    for (int ks = 0; ks < 40; ++ks) {
        if (ks == 12) SERVICE(24, 3, 4);
        if (ks == 26) SERVICE(24, 4, 5);
        if (ks + 1 < 40) xf[(ks + 1) & 1] = lds_r128((lds_cptr)(uintptr_t)xa[(ks + 1) & 3] + ((ks + 1) >> 2) * 4096);
        SBAR();
#pragma unroll
        for (int f = 0; f < 3; ++f) {
            acc[f] = MMA(3 * ks + f, xf[ks & 1], acc[f]);
            SBAR();
            WLD(3 * ks + f + NB);
        }
        SBAR();
    }
    SERVICE(24, 5, 6);
    {
        unsigned m1, m2, m3; split3(hh ? ts[2] : -ts[0], m1, m2, m3);
        u32x4 tf = {m1 | (m1 << 16), m2 | (m1 << 16), m2 | (m3 << 16), m2 | (m3 << 16)};
        if (hh) tf = u32x4{m1 | (m2 << 16), m3, 0, 0};
#pragma unroll
        for (int f = 0; f < 3; ++f) {
            unsigned a1, a2, a3; split3(csv[f], a1, a2, a3);
            u32x4 ff = {a1 | (a2 << 16), a1 | (a3 << 16), a2 | (a1 << 16), a3 | (a2 << 16)};
            if (hh) ff = u32x4{b1v[f] | (b1v[f] << 16), b1v[f], 0, 0};
            acc[f] = mfma16(__builtin_bit_cast(bf16x8, ff), __builtin_bit_cast(bf16x8, tf), acc[f]);
            SBAR();
        }
    }
    {
        constexpr float C0 = -2.0f * 0.7978845608028654f * 1.4426950408889634f, C1 = C0 * 0.044715f;
        const float s2 = ts[1], sg = ts[2];
        const f32x2 k0 = {C0 * s2, C0 * s2}, k1 = {C1 * s2 * s2 * s2, C1 * s2 * s2 * s2}, sg2 = {sg, sg};
        __attribute__((address_space(3))) u32x4* hp = (__attribute__((address_space(3))) u32x4*)(uintptr_t)(lds0 + K2_H + w * 6144 + lane * 16);
#pragma unroll
        for (int f = 0; f < 3; ++f) {
#pragma unroll
            for (int s = 0; s < 2; ++s) {
                u32x4 u;
#pragma unroll
                for (int i = 0; i < 4; ++i) {
                    const f32x2 a = {acc[f][8 * s + 2 * i], acc[f][8 * s + 2 * i + 1]};
                    const f32x2 z = pk_mul(a, pk_fma(pk_mul(a, a), k1, k0));
                    const f32x2 e = {__builtin_amdgcn_exp2f(z[0]), __builtin_amdgcn_exp2f(z[1])};
                    const f32x2 d = pk_fma(e, sg2, sg2);
                    const f32x2 hv = pk_mul(a, f32x2{__builtin_amdgcn_rcpf(d[0]), __builtin_amdgcn_rcpf(d[1])});
                    u[i] = pk2(hv[0], hv[1]);
                }
                hp[(2 * f + s) * 64] = u;
                SBAR();
            }
        }
    }
    asm volatile("s_waitcnt lgkmcnt(0)" ::: "memory");
    lds_signal(cnt + C_H);
    lds_await(cnt + C_H, lds_peek(cnt + C_H), 4);
    STAMPW(2, 8, 8);
    unsigned ha = lds0 + K2_H + lane * 16;
    PIN(ha);
    const lds_cptr hb = (lds_cptr)(uintptr_t)ha;
    char* pp = ws + WS_PART + (((size_t)j * 64 + tt) * 20 + w * 5) * 2048 + lane * 16;
    auto store_tile = [&](const f32x16& t, int f) {
#pragma unroll
        for (int c = 0; c < 2; ++c) {
            float a[8];
#pragma unroll
            for (int i = 0; i < 8; ++i) a[i] = t[8 * c + i];
            if (j == 0) {
                const int col = (w * 5 + f) * 32 + c * 16 + hh * 8;
                const u32x4 xv = __builtin_bit_cast(u32x4, lds_r128(xb + (col >> 6) * 4096 + r * 128 + ((((col >> 3) & 7) ^ ((r >> 1) & 7)) * 16)));
                const u32x4 bv = __builtin_bit_cast(u32x4, lds_r128((lds_cptr)lds + K2_B2 + col * 2));
#pragma unroll
                for (int i = 0; i < 4; ++i) { a[2 * i] += bf_lo(xv[i]) + bf_lo(bv[i]); a[2 * i + 1] += bf_hi(xv[i]) + bf_hi(bv[i]); }
            }
            *(u32x4*)(pp + (f * 2 + c) * 1024) = u32x4{pk2(a[0], a[1]), pk2(a[2], a[3]), pk2(a[4], a[5]), pk2(a[6], a[7])};
            SBAR();
        }
    };
    acc[0] = f32x16{}; acc[1] = f32x16{}; acc[2] = f32x16{};
    xf[0] = lds_r128(hb);
#pragma unroll
    for (int ks = 0; ks < 24; ++ks) {
        if (ks == 10) SERVICE(24, 6, 7);
        xf[(ks + 1) & 1] = lds_r128(hb + ((ks + 1) % 24) * 1024);
        SBAR();
#pragma unroll
        for (int f = 0; f < 3; ++f) {
            acc[f] = MMA(120 + 3 * ks + f, xf[ks & 1], acc[f]);
            SBAR();
            WLD(120 + 3 * ks + f + NB);
        }
        SBAR();
    }
    SERVICE(24, 7, 8);
    store_tile(acc[0], 0); store_tile(acc[1], 1); store_tile(acc[2], 2);
    SBAR();
    f32x16 ac2[2]; ac2[0] = f32x16{}; ac2[1] = f32x16{};
#pragma unroll
    for (int ks = 0; ks < 24; ++ks) {
        if (ks + 1 < 24) xf[(ks + 1) & 1] = lds_r128(hb + (ks + 1) * 1024);
        SBAR();
#pragma unroll
        for (int f = 0; f < 2; ++f) {
            ac2[f] = MMA(192 + 2 * ks + f, xf[ks & 1], ac2[f]);
            SBAR();
            WLD(192 + 2 * ks + f + NB);
        }
        SBAR();
    }
    STAMPW(2, 9, 8);
#undef WLD
#undef SERVICE
    store_tile(ac2[0], 3); store_tile(ac2[1], 4);
    STAMPW(2, 10, 8);
}

__global__ __launch_bounds__(768, 3) void k2_main(const __bf16* __restrict__ x, const __bf16* __restrict__ b1, const __bf16* __restrict__ b2, char* ws) {
    extern __shared__ __attribute__((aligned(16))) char lds[];
    const int wid = __builtin_amdgcn_readfirstlane(threadIdx.x >> 6);
    const int b = blockIdx.x, xcd = b & 7, head = xcd * 4 + ((b >> 3) & 3), qb = b >> 5;
    STAMP(1, 0);
    STAMPW(2, 0, 8);
    if (wid < 8) k2_attn(ws, lds, wid, head, qb);
    else k2_mlp(x, b1, b2, ws, lds, wid - 8, head, xcd & 3, (xcd >> 2) * 32 + (b >> 3));
}

constexpr int K3_LDS = 40960 + 5 * 4096;

__global__ __launch_bounds__(640, 1) void k3_out(const char* ws, __bf16* __restrict__ out) {
    extern __shared__ __attribute__((aligned(16))) char lds[];
    const int tid = threadIdx.x, lane = tid & 63, r = lane & 31, hh = lane >> 5;
    const int wid = __builtin_amdgcn_readfirstlane(tid >> 6), f = wid % 5, kq = wid / 5;
    const int b = blockIdx.x, i = b >> 3, tt = (b & 7) * 8 + (i >> 2), ng = i & 3, ft = ng * 5 + f;
    const unsigned lds0 = (unsigned)(uintptr_t)lds;
    STAMP(4, 0);
    const char* op = ws + WS_O + ((size_t)tt * 40 + wid * 4) * 1024 + lane * 16;
#pragma unroll
    for (int k = 0; k < 4; ++k) glds16(op + k * 1024, lds0 + (wid * 4 + k) * 1024);
    const char* wp = ws + WS_PWO + (size_t)ft * 40 * 1024 + lane * 16;
    bf16x8 wf[20];
#pragma unroll
    for (int k = 0; k < 16; ++k) { wf[k] = *(const bf16x8*)(wp + (kq * 16 + k) * 1024); SBAR(); }
#pragma unroll
    for (int k = 0; k < 4; ++k) { wf[16 + k] = *(const bf16x8*)(wp + (32 + kq * 4 + k) * 1024); SBAR(); }
    u32x4 pv[4];
#pragma unroll
    for (int k = 0; k < 4; ++k) pv[k] = *(const u32x4*)(ws + WS_PART + (((size_t)k * 64 + tt) * 20 + ft) * 2048 + kq * 1024 + lane * 16);
    SBAR();
    WAIT_BAR(24);
    STAMP(4, 1);
    f32x16 acc = f32x16{};
    const lds_cptr ob = (lds_cptr)lds + lane * 16;
#pragma unroll
    for (int k = 0; k < 20; ++k) acc = mfma16(wf[k], lds_r128(ob + (k < 16 ? kq * 16 + k : 32 + kq * 4 + (k - 16)) * 1024), acc);
    STAMP(4, 2);
    f32x4* red = (f32x4*)(lds + 40960 + f * 4096);
    {
        const int c = 1 - kq;
        red[(2 * c) * 64 + lane] = kq ? f32x4{acc[0], acc[1], acc[2], acc[3]} : f32x4{acc[8], acc[9], acc[10], acc[11]};
        red[(2 * c + 1) * 64 + lane] = kq ? f32x4{acc[4], acc[5], acc[6], acc[7]} : f32x4{acc[12], acc[13], acc[14], acc[15]};
    }
    __syncthreads();
    {
        const f32x4 a = red[(2 * kq) * 64 + lane], bb = red[(2 * kq + 1) * 64 + lane];
        float m[8];
#pragma unroll
        for (int k = 0; k < 8; ++k) m[k] = kq ? acc[8 + k] : acc[k];
#pragma unroll
        for (int k = 0; k < 4; ++k) {
            m[2 * k] += (bf_lo(pv[0][k]) + bf_lo(pv[1][k])) + (bf_lo(pv[2][k]) + bf_lo(pv[3][k]));
            m[2 * k + 1] += (bf_hi(pv[0][k]) + bf_hi(pv[1][k])) + (bf_hi(pv[2][k]) + bf_hi(pv[3][k]));
        }
        u32x4 u;
        u[0] = pk2(a[0] + m[0], a[1] + m[1]); u[1] = pk2(a[2] + m[2], a[3] + m[3]);
        u[2] = pk2(bb[0] + m[4], bb[1] + m[5]); u[3] = pk2(bb[2] + m[6], bb[3] + m[7]);
        *(u32x4*)(out + (size_t)(tt * 32 + r) * D_ + ft * 32 + kq * 16 + hh * 8) = u;
    }
    STAMP(4, 3);
}

}

extern "C" void kernel_launch(void* const* d_in, const int* in_sizes, int n_in, void* d_out, int out_size, void* d_ws, size_t ws_size, hipStream_t stream) {
    const __bf16* x = (const __bf16*)d_in[0];
    const __bf16* wqkv = (const __bf16*)d_in[1];
    const __bf16* wo = (const __bf16*)d_in[2];
    const __bf16* w1 = (const __bf16*)d_in[4];
    const __bf16* b1 = (const __bf16*)d_in[5];
    const __bf16* w2 = (const __bf16*)d_in[6];
    const __bf16* b2 = (const __bf16*)d_in[7];
    char* ws = (char*)d_ws;
    (void)hipFuncSetAttribute((const void*)k1_qkv, hipFuncAttributeMaxDynamicSharedMemorySize, K1_LDS);
    (void)hipFuncSetAttribute((const void*)k2_main, hipFuncAttributeMaxDynamicSharedMemorySize, K2_LDS);
    (void)hipFuncSetAttribute((const void*)k3_out, hipFuncAttributeMaxDynamicSharedMemorySize, K3_LDS);
    k1_qkv<<<256, 512, K1_LDS, stream>>>(x, wqkv, wo, w1, w2, ws);
    k2_main<<<256, 768, K2_LDS, stream>>>(x, b1, b2, ws);
    k3_out<<<256, 640, K3_LDS, stream>>>(ws, (__bf16*)d_out);
}
```

```cpp
#include <hip/hip_runtime.h>
#include <hip/hip_bf16.h>
#include <stdint.h>

namespace {

typedef __attribute__((ext_vector_type(8))) __bf16 bf16x8;
typedef __attribute__((ext_vector_type(2))) __bf16 bf16x2;
typedef __attribute__((ext_vector_type(8))) short s16x8;
typedef __attribute__((ext_vector_type(4))) short s16x4;
typedef __attribute__((ext_vector_type(16))) float f32x16;
typedef __attribute__((ext_vector_type(4))) float f32x4;
typedef __attribute__((ext_vector_type(2))) float f32x2;
typedef __attribute__((ext_vector_type(4))) unsigned u32x4;
typedef __attribute__((ext_vector_type(2))) unsigned u32x2;
typedef __attribute__((address_space(3))) char* lds_ptr;
typedef __attribute__((address_space(3))) const char* lds_cptr;

constexpr int S_ = 2048, D_ = 640, H_ = 32, F_ = 1536;
constexpr float EPS = 1e-5f;
constexpr float QSCALE = 0.22360679774997896f * 1.4426950408889634f;

constexpr int QT_B = 1280;
constexpr int VROW_B = 352;
constexpr int KVT_B = 1280 + 4 * VROW_B;
constexpr size_t WS_Q = 0;
constexpr size_t WS_KV = WS_Q + (size_t)H_ * 64 * QT_B;
constexpr size_t WS_TOK = WS_KV + (size_t)H_ * 64 * KVT_B + 4096;
constexpr size_t WS_CS1 = WS_TOK + (size_t)S_ * 16;
constexpr size_t WS_PW = WS_CS1 + 8192;
constexpr size_t WS_PWO = WS_PW + (size_t)16 * 240 * 1024;
constexpr size_t WS_PART = WS_PWO + (size_t)20 * 40 * 1024;
constexpr size_t WS_O = WS_PART + (size_t)4 * 64 * 20 * 2048;
constexpr size_t WS_EPOCH = WS_O + (size_t)64 * 40 * 1024;
constexpr size_t WS_RT = WS_EPOCH + 256;
constexpr size_t WS_RC = WS_RT + (size_t)S_ * 16;
constexpr size_t WS_END = WS_RC + (size_t)4 * 3 * D_ * 8;

#define STAMPW(kid, i, w)
#define STAMP(kid, i) STAMPW(kid, i, 0)
#define SBAR() __builtin_amdgcn_sched_barrier(0)
#define PIN(x) asm volatile("" : "+v"(x))
#define WAIT_BAR(N) asm volatile("s_waitcnt vmcnt(" #N ") lgkmcnt(0)\n\ts_barrier" ::: "memory")
#define WAIT_VM(N) asm volatile("s_waitcnt vmcnt(" #N ")" ::: "memory")

__device__ __forceinline__ f32x16 mfma16(bf16x8 a, bf16x8 b, f32x16 c) { return __builtin_amdgcn_mfma_f32_32x32x16_bf16(a, b, c, 0, 0, 0); }
__device__ __forceinline__ f32x16 mfma8(s16x4 a, s16x4 b, f32x16 c) { return __builtin_amdgcn_mfma_f32_32x32x8bf16_1k(a, b, c, 0, 0, 0); }
__device__ __forceinline__ unsigned pk2(float lo, float hi) { f32x2 v = {lo, hi}; return __builtin_bit_cast(unsigned, __builtin_convertvector(v, bf16x2)); }
__device__ __forceinline__ float bf_lo(unsigned u) { return __uint_as_float(u << 16); }
__device__ __forceinline__ float bf_hi(unsigned u) { return __uint_as_float(u & 0xffff0000u); }
__device__ __forceinline__ void glds16(const void* g, unsigned lds_base) {
    unsigned sv;
    asm volatile("s_mov_b32 %0, m0\n\ts_mov_b32 m0, %2\n\ts_nop 0\n\tglobal_load_lds_dwordx4 %1, off\n\ts_mov_b32 m0, %0" : "=&s"(sv) : "v"(g), "s"(lds_base) : "memory");
}
__device__ __forceinline__ void glds4(const void* g, unsigned lds_base) {
    unsigned sv;
    asm volatile("s_mov_b32 %0, m0\n\ts_mov_b32 m0, %2\n\ts_nop 0\n\tglobal_load_lds_dword %1, off\n\ts_mov_b32 m0, %0" : "=&s"(sv) : "v"(g), "s"(lds_base) : "memory");
}
__device__ __forceinline__ bf16x8 lds_r128(lds_cptr p) { return *(const __attribute__((address_space(3))) bf16x8*)p; }
__device__ __forceinline__ s16x4 lds_r64(lds_cptr p) { return *(const __attribute__((address_space(3))) s16x4*)p; }
__device__ __forceinline__ s16x4 lds_tr(lds_cptr p) { return __builtin_amdgcn_ds_read_tr16_b64_v4i16((__attribute__((address_space(3))) s16x4*)p); }
__device__ __forceinline__ bf16x8 cat8(s16x4 a, s16x4 b) { s16x8 v = {a[0], a[1], a[2], a[3], b[0], b[1], b[2], b[3]}; return __builtin_bit_cast(bf16x8, v); }
__device__ __forceinline__ float dot2(unsigned a, unsigned b, float c) { return __builtin_amdgcn_fdot2_f32_bf16(__builtin_bit_cast(bf16x2, a), __builtin_bit_cast(bf16x2, b), c, false); }
__device__ __forceinline__ float swap32_add(float v) { auto rr = __builtin_amdgcn_permlane32_swap(__float_as_uint(v), __float_as_uint(v), false, false); return __uint_as_float(rr[0]) + __uint_as_float(rr[1]); }
__device__ __forceinline__ float swap32_max(float v) { auto rr = __builtin_amdgcn_permlane32_swap(__float_as_uint(v), __float_as_uint(v), false, false); return __builtin_fmaxf(__uint_as_float(rr[0]), __uint_as_float(rr[1])); }
__device__ __forceinline__ void split3(float v, unsigned& a, unsigned& b, unsigned& c) {
    a = pk2(v, 0.f) & 0xffffu; float r = v - bf_lo(a);
    b = pk2(r, 0.f) & 0xffffu; r = r - bf_lo(b);
    c = pk2(r, 0.f) & 0xffffu;
}
__device__ __forceinline__ void st16(void* p, u32x4 v) {
    asm volatile("global_store_dwordx4 %0, %1, off sc1\n\ts_nop 1" :: "v"(p), "v"(v) : "memory");
}
__device__ __forceinline__ void st8(void* p, u32x2 v) { asm volatile("global_store_dwordx2 %0, %1, off sc1\n\ts_nop 1" :: "v"(p), "v"(v) : "memory"); }
#define QST16(p, v) (*(u32x4*)(p) = (v))
#define QST8(p, v) (*(u32x2*)(p) = (v))
#define PST16(p, v) (*(u32x4*)(p) = (v))
constexpr unsigned ONES2 = 0x3f803f80u;
typedef unsigned long long u64;
__device__ __forceinline__ u64 ld_agent(const u64* p) { return __hip_atomic_load(p, __ATOMIC_RELAXED, __HIP_MEMORY_SCOPE_AGENT); }
__device__ __forceinline__ void st_agent(u64* p, float v, unsigned tag) { __hip_atomic_store(p, (u64)__float_as_uint(v) | ((u64)tag << 32), __ATOMIC_RELAXED, __HIP_MEMORY_SCOPE_AGENT); }
#define K1_SPIN 4096
__device__ __forceinline__ void lds_signal(unsigned addr, unsigned val = 1u) {
    asm volatile("s_mov_b64 exec, 1\n\tds_add_u32 %0, %1\n\ts_mov_b64 exec, -1" :: "v"(addr), "v"(val) : "memory");
}
__device__ __forceinline__ unsigned lds_peek(unsigned addr) { return *(const volatile __attribute__((address_space(3))) unsigned*)(uintptr_t)addr; }
__device__ __forceinline__ void lds_await(unsigned addr, unsigned first, unsigned target) {
    unsigned v = __builtin_amdgcn_readfirstlane(first);
    while (v < target) { __builtin_amdgcn_s_sleep(1); v = __builtin_amdgcn_readfirstlane(lds_peek(addr)); }
    asm volatile("" ::: "memory");
}

constexpr int K1_STAGE = 16384 + 20480, K1_AUX = 4 * K1_STAGE, K1_LDS = K1_AUX + 4096 + 5120 + 1024 + 256;

__device__ __forceinline__ constexpr unsigned k1_perm(int j) {
    return j == 0 ? 0x76453120u : (j == 3 ? 0x57064231u : (j == 4 ? 0x76254130u : 0x75643120u));
}
__device__ __forceinline__ int k1_piece_off(int P, int r, int& head) {
    head = P / 5; int rr = P - head * 5; const int h7 = head & 7;
    int role = rr;
    if (h7 == 1) role = (0x32410 >> (4 * rr)) & 7;
    if (h7 == 3) role = (0x32104 >> (4 * rr)) & 7;
    return role == 4 ? 1024 + r * 8 : (role >> 1) * 512 + r * 16 + (role & 1) * 8;
}

constexpr int K1_TF = K1_AUX, K1_FF = K1_AUX + 4096, K1_RS = K1_AUX + 9216;

template <bool VORI, int WN>
__device__ __forceinline__ void k1_body(char* __restrict__ ws, char* lds, int mt, int nt) {
    constexpr int NF = WN ? 2 : 3, F0 = WN ? 3 : 0;
    const int tid = threadIdx.x, lane = tid & 63, r = lane & 31, hh = lane >> 5;
    const int wid = __builtin_amdgcn_readfirstlane(tid >> 6), wm = wid & 3;
    const unsigned lds0 = (unsigned)(uintptr_t)lds;
    STAMP(0, 0);
    const int xrow = 32 * wm + r;
    const int q4 = (lane & 15) >> 2, p4 = lane & 3, gi = (lane >> 4) & 1;
    unsigned xa[4], wa[NF];
#pragma unroll
    for (int k = 0; k < 4; ++k) xa[k] = lds0 + xrow * 128 + (((2 * k + hh) ^ ((xrow >> 1) & 7)) * 16);
#pragma unroll
    for (int f = 0; f < NF; ++f) {
        int rel = 4 * gi + p4;
        if (!VORI) rel = (k1_perm(F0 + f) >> (4 * rel)) & 7;
        wa[f] = lds0 + 16384 + (8 * hh + q4) * 320 + (F0 + f) * 64 + rel * 8;
    }
    const int tt = mt * 4 + wm;
    const bool isq = nt < 4;
    char* const base = (!VORI && isq) ? ws + WS_Q + (size_t)tt * QT_B : ws + WS_KV + (size_t)tt * KVT_B;
    unsigned so[NF][3];
#pragma unroll
    for (int f = 0; f < NF; ++f) {
        if (!VORI) {
            const unsigned hstride = isq ? 64 * QT_B : 64 * KVT_B;
            const int T = (nt & 3) * 5 + F0 + f;
            const unsigned perm = k1_perm(F0 + f);
#pragma unroll
            for (int i = 0; i < 3; ++i) {
                int head;
                const int off = k1_piece_off(8 * T + ((perm >> (4 * ((i ? 2 + 2 * i : 0) + hh))) & 7), r, head);
                so[f][i] = head * hstride + off;
                PIN(so[f][i]);
            }
        } else {
            const int col = (nt - 8) * 160 + (F0 + f) * 32 + r;
            const int head = col / 20, d = col - head * 20;
            so[f][0] = head * (64 * KVT_B) + 1280 + hh * VROW_B + d * 16;
            so[f][1] = d;
            PIN(so[f][0]); PIN(so[f][1]);
        }
    }
    f32x16 acc[NF];
#pragma unroll
    for (int f = 0; f < NF; ++f) acc[f] = f32x16{};
    bf16x8 xf[4], wf[4][NF];
#define K1_RD(sl, ks) do { xf[ks] = lds_r128((lds_cptr)(uintptr_t)(xa[ks] + (sl))); \
        _Pragma("unroll") for (int f = 0; f < NF; ++f) { const lds_cptr p = (lds_cptr)(uintptr_t)(wa[f] + (sl)) + (ks) * 5120; wf[ks][f] = cat8(lds_tr(p), lds_tr(p + 1280)); } SBAR(); } while (0)
#define K1_MM(b) do { _Pragma("unroll") for (int f = 0; f < NF; ++f) acc[f] = VORI ? mfma16(xf[b], wf[b][f], acc[f]) : mfma16(wf[b][f], xf[b], acc[f]); SBAR(); } while (0)
    asm volatile("s_barrier" ::: "memory");
    STAMP(0, 1);
    __builtin_amdgcn_s_waitcnt(0xc07f);
    K1_RD(0u, 0); K1_RD(0u, 1);
#pragma unroll 1
    for (int st = 0; st < 10; ++st) {
        const unsigned sl = (st & 3) * K1_STAGE, sn = ((st + 1) & 3) * K1_STAGE;
        K1_RD(sl, 2); K1_MM(0);
        K1_RD(sl, 3); K1_MM(1);
        if (st < 9) asm volatile("s_waitcnt lgkmcnt(0)\n\ts_barrier" ::: "memory");
        K1_RD(sn, 0); K1_MM(2);
        K1_RD(sn, 1); K1_MM(3);
    }
#undef K1_RD
#undef K1_MM
    STAMP(0, 3);
    asm volatile("s_barrier" ::: "memory");
    const lds_cptr aux = (lds_cptr)lds;
    const bf16x8 tfb = lds_r128(aux + K1_TF + wm * 1024 + lane * 16);
    bf16x8 ffb[NF];
#pragma unroll
    for (int f = 0; f < NF; ++f) ffb[f] = lds_r128(aux + K1_FF + (F0 + f) * 1024 + lane * 16);
    const float* rsl = (const float*)(lds + K1_RS) + wm * 32;
    float rs[VORI ? 16 : 1];
    if (VORI) {
#pragma unroll
        for (int g = 0; g < 4; ++g) { const f32x4 t = *(const f32x4*)(rsl + 8 * g + 4 * hh); rs[4 * g] = t[0]; rs[4 * g + 1] = t[1]; rs[4 * g + 2] = t[2]; rs[4 * g + 3] = t[3]; }
    } else rs[0] = rsl[r] * (isq ? QSCALE : 1.f);
#pragma unroll
    for (int f = 0; f < NF; ++f) acc[f] = VORI ? mfma16(tfb, ffb[f], acc[f]) : mfma16(ffb[f], tfb, acc[f]);
    STAMP(0, 4);
#pragma unroll
    for (int f = 0; f < NF; ++f) {
        unsigned u[8];
#pragma unroll
        for (int i = 0; i < 8; ++i) u[i] = pk2(acc[f][2 * i] * rs[VORI ? 2 * i : 0], acc[f][2 * i + 1] * rs[VORI ? 2 * i + 1 : 0]);
        if (!VORI) {
            QST16(base + so[f][0], (u32x4{u[0], u[1], u[2], u[3]}));
            QST8(base + so[f][1], (u32x2{u[4], u[5]}));
            QST8(base + so[f][2], (u32x2{u[6], u[7]}));
        } else {
            char* p = base + so[f][0];
            QST16(p, (u32x4{u[0], u[1], u[2], u[3]}));
            QST16(p + 2 * VROW_B, (u32x4{u[4], u[5], u[6], u[7]}));
            if (so[f][1] == 19) {
                QST16(p + 16, (u32x4{ONES2, ONES2, ONES2, ONES2})); QST16(p + 2 * VROW_B + 16, (u32x4{ONES2, ONES2, ONES2, ONES2}));
                QST16(p + 32, (u32x4{0, 0, 0, 0})); QST16(p + 2 * VROW_B + 32, (u32x4{0, 0, 0, 0}));
            }
        }
    }
}

template <bool VORI>
__device__ __forceinline__ void k1_helper(const __bf16* __restrict__ x, const __bf16* __restrict__ w, char* __restrict__ ws, char* lds, int mt, int nt, int hw) {
    const int lane = threadIdx.x & 63, r = lane & 31, hh = lane >> 5;
    const unsigned lds0 = (unsigned)(uintptr_t)lds;
    const unsigned tag = __hip_atomic_load((const unsigned*)(ws + WS_EPOCH), __ATOMIC_RELAXED, __HIP_MEMORY_SCOPE_AGENT) + 1u;
    unsigned xo[4], wo[5];
#pragma unroll
    for (int i = 0; i < 4; ++i) {
        const int row = 8 * (4 * hw + i) + (lane >> 3);
        xo[i] = row * (D_ * 2) + (((lane & 7) ^ ((row >> 1) & 7)) * 16) - i * 1024;
    }
#pragma unroll
    for (int i = 0; i < 5; ++i) {
        const int idx = (5 * hw + i) * 64 + lane, krow = idx / 20, ch = idx - krow * 20;
        wo[i] = krow * (3 * D_ * 2) + ch * 16 - (i & 3) * 1024;
    }
    const char* xs = (const char*)x + (size_t)mt * 128 * (D_ * 2);
    const char* wsr = (const char*)w + nt * 320;
#define K1_M0(v) asm volatile("s_mov_b32 m0, %0\n\ts_nop 0" :: "s"(v) : "memory")
#define K1_DMA(vo, sp, imm) asm volatile("global_load_lds_dwordx4 %0, %1 offset:" #imm :: "v"(vo), "s"(sp) : "memory")
#define K1_PIECE(i, xp, wp, lb) do { \
        if ((i) == 0) { K1_M0((lb) + hw * 4096); K1_DMA(xo[0], xp, 0); } \
        if ((i) == 1) K1_DMA(xo[1], xp, 1024); \
        if ((i) == 2) K1_DMA(xo[2], xp, 2048); \
        if ((i) == 3) K1_DMA(xo[3], xp, 3072); \
        if ((i) == 4) { K1_M0((lb) + 16384 + hw * 5120); K1_DMA(wo[0], wp, 0); } \
        if ((i) == 5) K1_DMA(wo[1], wp, 1024); \
        if ((i) == 6) K1_DMA(wo[2], wp, 2048); \
        if ((i) == 7) K1_DMA(wo[3], wp, 3072); \
        if ((i) == 8) { K1_M0((lb) + 16384 + hw * 5120 + 4096); K1_DMA(wo[4], wp, 0); } \
        SBAR(); } while (0)
#pragma unroll 1
    for (int st = 0; st < 4; ++st) {
        const char* xp = xs + st * 128; const char* wp = wsr + (size_t)st * 64 * (3 * D_ * 2);
        const unsigned lb = lds0 + st * K1_STAGE;
#pragma unroll
        for (int i = 0; i < 9; ++i) K1_PIECE(i, xp, wp, lb);
    }
    WAIT_BAR(27);
#pragma unroll 1
    for (int st = 0; st < 7; ++st) {
        if (st) {
            const char* xp = xs + (st + 3) * 128; const char* wp = wsr + (size_t)(st + 3) * 64 * (3 * D_ * 2);
            const unsigned lb = lds0 + ((st + 3) & 3) * K1_STAGE;
#pragma unroll
            for (int i = 0; i < 9; ++i) K1_PIECE(i, xp, wp, lb);
        }
        WAIT_BAR(18);
    }
#undef K1_PIECE
#undef K1_DMA
#undef K1_M0
    const int tok = mt * 128 + 32 * hw + r;
    int col[2];
#pragma unroll
    for (int f = 0; f < 2; ++f) {
        const int t = f ? 0 : hw + 1;
        const int pc = VORI ? (r >> 2) : (int)((k1_perm(t) >> (4 * (r >> 2))) & 7);
        col[f] = nt * 160 + t * 32 + pc * 4 + (r & 3);
    }
    const u64* rt = (const u64*)(ws + WS_RT) + tok * 2 + hh;
    const u64* rc = (const u64*)(ws + WS_RC) + hh * (2 * 3 * D_);
    u64 rec[5];
#define K1_POLL() do { rec[0] = ld_agent(rt); rec[1] = ld_agent(rc + col[0]); rec[2] = ld_agent(rc + 3 * D_ + col[0]); rec[3] = ld_agent(rc + col[1]); rec[4] = ld_agent(rc + 3 * D_ + col[1]); } while (0)
    K1_POLL();
    WAIT_BAR(14);
    WAIT_BAR(5);
    bool ok = false;
#pragma unroll 1
    for (int it = 0; it < K1_SPIN; ++it) {
        bool good = true;
#pragma unroll
        for (int i = 0; i < 5; ++i) good = good && (unsigned)(rec[i] >> 32) == tag;
        if (__all(good)) { ok = true; break; }
        __builtin_amdgcn_s_sleep(8);
        K1_POLL();
    }
#undef K1_POLL
    float tv = __uint_as_float((unsigned)rec[0]);
    float cs0 = __uint_as_float((unsigned)rec[1]) + __uint_as_float((unsigned)rec[2]), cs1 = __uint_as_float((unsigned)rec[3]) + __uint_as_float((unsigned)rec[4]);
    if (__builtin_expect(!ok, 0)) {
        const u32x4* rowp = (const u32x4*)(x + (size_t)tok * D_) + hh * 40;
        float a = 0.f;
        for (int i = 0; i < 40; ++i) { const u32x4 u = rowp[i]; for (int j = 0; j < 4; ++j) a = dot2(u[j], ONES2, a); }
        const float mu = swap32_add(a) * (1.f / D_);
        a = 0.f;
        for (int i = 0; i < 40; ++i) {
            const u32x4 u = rowp[i];
            for (int j = 0; j < 4; ++j) { const float d0 = bf_lo(u[j]) - mu, d1 = bf_hi(u[j]) - mu; a = __builtin_fmaf(d0, d0, a); a = __builtin_fmaf(d1, d1, a); }
        }
        const float rsd = __builtin_amdgcn_rsqf(swap32_add(a) * (1.f / D_) + EPS);
        tv = hh ? rsd : mu;
        cs0 = 0.f; cs1 = 0.f;
        const unsigned short* wc = (const unsigned short*)w + (size_t)hh * 320 * (3 * D_);
        for (int k = 0; k < 320; ++k) { cs0 += bf_lo(wc[k * (3 * D_) + col[0]]); cs1 += bf_lo(wc[k * (3 * D_) + col[1]]); }
    }
    {
        unsigned m1, m2, m3; split3(-tv, m1, m2, m3);
        u32x4 tf = {m1 | (m1 << 16), m2 | (m1 << 16), m2 | (m3 << 16), m2 | (m3 << 16)};
        if (hh) tf = u32x4{0, 0, 0, 0};
        *(u32x4*)(lds + K1_TF + hw * 1024 + lane * 16) = tf;
        unsigned a1, a2, a3; split3(swap32_add(cs0), a1, a2, a3);
        *(u32x4*)(lds + K1_FF + (hw + 1) * 1024 + lane * 16) = u32x4{a1 | (a2 << 16), a1 | (a3 << 16), a2 | (a1 << 16), a3 | (a2 << 16)};
        if (hw == 0) {
            split3(swap32_add(cs1), a1, a2, a3);
            *(u32x4*)(lds + K1_FF + lane * 16) = u32x4{a1 | (a2 << 16), a1 | (a3 << 16), a2 | (a1 << 16), a3 | (a2 << 16)};
        }
        if (hh) ((float*)(lds + K1_RS))[32 * hw + r] = tv;
    }
    asm volatile("s_waitcnt lgkmcnt(0)\n\ts_barrier" ::: "memory");
}

struct K1Stat { u32x4 xr[5], wr[5]; unsigned tag; };
__device__ __forceinline__ void k1_stat_load(K1Stat& sv, const __bf16* __restrict__ x, const __bf16* __restrict__ w, char* __restrict__ ws, int job) {
    const int tid = threadIdx.x, lane = tid & 63, wid = tid >> 6;
    sv.tag = __hip_atomic_load((const unsigned*)(ws + WS_EPOCH), __ATOMIC_RELAXED, __HIP_MEMORY_SCOPE_AGENT) + 1u;
    const u32x4* xp = (const u32x4*)(x + (size_t)(job * 32 + wid * 4 + (lane >> 4)) * D_) + (lane & 15);
    const int jc = job < 60 ? job : 59;
    const u32x4* wp = (const u32x4*)(w + (size_t)((jc / 15) * 160 + wid * 20 + (lane >> 4)) * (3 * D_) + (jc % 15) * 128) + (lane & 15);
#pragma unroll
    for (int i = 0; i < 5; ++i) sv.xr[i] = xp[16 * i];
#pragma unroll
    for (int i = 0; i < 5; ++i) sv.wr[i] = wp[(size_t)i * 4 * (3 * D_ / 8)];
}
__device__ __forceinline__ float red16(float v) {
    v += __shfl_xor(v, 1); v += __shfl_xor(v, 2); v += __shfl_xor(v, 4); v += __shfl_xor(v, 8);
    return v;
}
__device__ __forceinline__ void k1_stat(const K1Stat& sv, char* __restrict__ ws, char* lds, int job) {
    const int tid = threadIdx.x, lane = tid & 63, wid = tid >> 6;
    float a = 0.f;
#pragma unroll
    for (int i = 0; i < 5; ++i)
#pragma unroll
        for (int j = 0; j < 4; ++j) a = dot2(sv.xr[i][j], ONES2, a);
    const float mu = red16(a) * (1.f / D_);
    float a0 = 0.f, a1 = 0.f;
#pragma unroll
    for (int i = 0; i < 5; ++i)
#pragma unroll
        for (int j = 0; j < 4; ++j) { const float d0 = bf_lo(sv.xr[i][j]) - mu, d1 = bf_hi(sv.xr[i][j]) - mu; a0 = __builtin_fmaf(d0, d0, a0); a1 = __builtin_fmaf(d1, d1, a1); }
    const float var = red16(a0 + a1) * (1.f / D_);
    const int tok = job * 32 + wid * 4 + (lane >> 4), c = lane & 15;
    if (c < 2) st_agent((u64*)(ws + WS_RT) + tok * 2 + c, c ? __builtin_amdgcn_rsqf(var + EPS) : mu, sv.tag);
    if (c == 2) {
        const float sig2 = __builtin_sqrtf(var * (1.f + EPS) + EPS * EPS);
        *(f32x4*)(ws + WS_TOK + (size_t)tok * 16) = f32x4{mu, 1.0f / sig2, sig2, 0.f};
    }
    float cs[8];
#pragma unroll
    for (int j = 0; j < 4; ++j) {
        cs[2 * j] = bf_lo(sv.wr[0][j]); cs[2 * j + 1] = bf_hi(sv.wr[0][j]);
#pragma unroll
        for (int i = 1; i < 5; ++i) { cs[2 * j] += bf_lo(sv.wr[i][j]); cs[2 * j + 1] += bf_hi(sv.wr[i][j]); }
    }
    float* red = (float*)(lds + 86016);
    *(f32x4*)(red + (wid * 4 + (lane >> 4)) * 128 + c * 8) = f32x4{cs[0], cs[1], cs[2], cs[3]};
    *(f32x4*)(red + (wid * 4 + (lane >> 4)) * 128 + c * 8 + 4) = f32x4{cs[4], cs[5], cs[6], cs[7]};
    asm volatile("s_waitcnt lgkmcnt(0)\n\ts_barrier" ::: "memory");
    if (tid < 128 && job < 60) {
        float t[4] = {0.f, 0.f, 0.f, 0.f};
#pragma unroll
        for (int i = 0; i < 32; ++i) t[i & 3] += red[i * 128 + tid];
        st_agent((u64*)(ws + WS_RC) + (job / 15) * (3 * D_) + (job % 15) * 128 + tid, (t[0] + t[1]) + (t[2] + t[3]), sv.tag);
    }
}

template <int KIND>
__device__ __forceinline__ void k1_pack(const __bf16* __restrict__ wsrc, char* __restrict__ ws, char* lds, int strip, int third, const K1Stat& sv, int job) {
    constexpr int NI = KIND == 1 ? 8 : 10, NKS = KIND == 1 ? 4 : 5, ROWB = (KIND == 0 ? F_ : D_) * 2;
    const int tid = threadIdx.x, lane = tid & 63, r = lane & 31, hh = lane >> 5;
    const int wid = __builtin_amdgcn_readfirstlane(tid >> 6);
    const unsigned lds0 = (unsigned)(uintptr_t)lds + wid * 10240;
    const int row0 = third * 512 + wid * NI * 8;
    const char* src = (const char*)wsrc + (size_t)(row0 + (lane >> 3)) * ROWB + strip * 128 + (((lane & 7) ^ (((lane >> 4) & 1) * 4)) * 16);
#pragma unroll
    for (int i = 0; i < NI; ++i) glds16(src + (size_t)i * 8 * ROWB, lds0 + i * 1024);
    const int q4 = (lane & 15) >> 2, p4 = lane & 3, gi = (lane >> 4) & 1;
    int pc = 4 * gi + p4;
    if (KIND != 0) pc = (0x75643120u >> (4 * pc)) & 7;
    const lds_cptr sb = (lds_cptr)lds + wid * 10240;
    float cs[2] = {0.f, 0.f};
    k1_stat(sv, ws, lds, job);
    WAIT_VM(0);
#pragma unroll
    for (int ks = 0; ks < NKS; ++ks) {
        int ra, rb;
        if (KIND == 0) { ra = 16 * ks + 8 * hh; rb = ra + 4; }
        else if (KIND == 1) { ra = 16 * ks + 4 * hh; rb = ra + 8; }
        else if (ks < 4) { ra = 20 * ks + 4 * hh; rb = ra + 8; }
        else { ra = 40 * hh + 16; rb = ra + 20; }
#pragma unroll
        for (int f = 0; f < 2; ++f) {
            const int co = ((((f * 8 + pc) >> 1) ^ ((q4 >> 1) * 4)) * 16) + (pc & 1) * 8;
            const bf16x8 fr = cat8(lds_tr(sb + (ra + q4) * 128 + co), lds_tr(sb + (rb + q4) * 128 + co));
            const int ft = strip * 2 + f;
            size_t dst;
            if (KIND == 0) dst = WS_PW + ((size_t)(ft / 3) * 240 + (wid * 5 + ks) * 3 + ft % 3) * 1024;
            else if (KIND == 1) { const int g = third * 32 + wid * 4 + ks; const int f5 = ft % 5; dst = WS_PW + ((size_t)((g / 24) * 4 + ft / 5) * 240 + (f5 < 3 ? 120 + (g % 24) * 3 + f5 : 192 + (g % 24) * 2 + f5 - 3)) * 1024; }
            else dst = WS_PWO + ((size_t)ft * 40 + (ks < 4 ? wid * 4 + ks : 32 + wid)) * 1024;
            PST16(ws + dst + lane * 16, __builtin_bit_cast(u32x4, fr));
            if (KIND == 0) {
                const u32x4 a = __builtin_bit_cast(u32x4, fr);
#pragma unroll
                for (int i = 0; i < 4; ++i) cs[f] = dot2(a[i], ONES2, cs[f]);
            }
        }
    }
    if (KIND == 0) {
        float* red = (float*)(lds + 81920);
        red[(wid * 2 + hh) * 64 + r] = cs[0];
        red[(wid * 2 + hh) * 64 + 32 + r] = cs[1];
        __syncthreads();
        if (tid < 64) {
            float s = 0.f;
#pragma unroll
            for (int i = 0; i < 16; ++i) s += red[i * 64 + tid];
            ((float*)(ws + WS_CS1))[strip * 64 + tid] = s;
        }
    }
}

__global__ __launch_bounds__(768, 3) void k1_qkv(const __bf16* __restrict__ x, const __bf16* __restrict__ wqkv, const __bf16* __restrict__ wo, const __bf16* __restrict__ w1,
                                                 const __bf16* __restrict__ w2, char* __restrict__ ws) {
    extern __shared__ __attribute__((aligned(16))) char lds[];
    const int b = blockIdx.x, xcd = b & 7, idx = b >> 3;
    const int wid = __builtin_amdgcn_readfirstlane(threadIdx.x >> 6);
    if (idx >= 24) {
        if (wid >= 8) return;
        const int job = (idx - 24) * 8 + xcd;
        K1Stat sv;
        k1_stat_load(sv, x, wqkv, ws, job);
        if (job < 24) k1_pack<0>(w1, ws, lds, job, 0, sv, job);
        else if (job < 54) k1_pack<1>(w2, ws, lds, (job - 24) % 10, (job - 24) / 10, sv, job);
        else k1_pack<2>(wo, ws, lds, job - 54, 0, sv, job);
        STAMP(3, 0);
        return;
    }
    const int mt = (xcd >> 1) * 4 + (idx & 3), nt = (xcd & 1) * 6 + (idx >> 2);
    if (wid >= 8) { if (nt < 8) k1_helper<false>(x, wqkv, ws, lds, mt, nt, wid - 8); else k1_helper<true>(x, wqkv, ws, lds, mt, nt, wid - 8); return; }
    if (nt < 8) { if (wid >= 4) k1_body<false, 1>(ws, lds, mt, nt); else k1_body<false, 0>(ws, lds, mt, nt); }
    else { if (wid >= 4) k1_body<true, 1>(ws, lds, mt, nt); else k1_body<true, 0>(ws, lds, mt, nt); }
    STAMP(0, 5);
}

#define MLP_NB 20
#define AP_HI 2
#define AP_MID 1
#define AP_LO 0
#define GELU_PRIO 1
#define MLP_SLEEP 0
#define AW 7
#define MLP_PRIO 0
template <int N> __device__ __forceinline__ void wait_vm() { asm volatile("s_waitcnt vmcnt(%0)" :: "n"(N) : "memory"); }
#define WAIT_VMX(N) wait_vm<(N)>()
constexpr int AT_SLOT = 8 * KVT_B;
constexpr int K2_X = 4 * AT_SLOT, K2_H = K2_X + 40960, K2_CNT = K2_H + 24576, K2_B2 = K2_CNT + 128, K2_LDS = K2_B2 + 1280;
constexpr int C_FULL = 0, C_FREE = 64, C_X = 112, C_H = 116;

__device__ __forceinline__ void attn_slow(const char* __restrict__ kv, bf16x8 qf, s16x4 qt, f32x16& o, int r, int hh) {
    float m = -INFINITY;
    o = f32x16{};
    if (hh) qt = s16x4{0, 0, 0, 0};
    for (int t = 0; t < 64; ++t) {
        const char* p = kv + (size_t)t * KVT_B;
        const bf16x8 kf = *(const bf16x8*)(p + hh * 512 + r * 16);
        const s16x4 kt = *(const s16x4*)(p + 1024 + r * 8);
        const int rv = r < 20 ? r : 20;
        const bf16x8 v0 = *(const bf16x8*)(p + 1280 + hh * VROW_B + rv * 16);
        const bf16x8 v1 = *(const bf16x8*)(p + 1280 + 2 * VROW_B + hh * VROW_B + rv * 16);
        f32x16 s = mfma16(kf, qf, f32x16{});
        s = mfma8(kt, qt, s);
        float tm = s[0];
#pragma unroll
        for (int i = 1; i < 16; ++i) tm = __builtin_fmaxf(tm, s[i]);
        tm = swap32_max(tm);
        const float mn = __builtin_fmaxf(m, tm);
        const float al = __builtin_amdgcn_exp2f(m - mn);
        m = mn;
        u32x4 p0, p1;
#pragma unroll
        for (int i = 0; i < 4; ++i) {
            p0[i] = pk2(__builtin_amdgcn_exp2f(s[2 * i] - m), __builtin_amdgcn_exp2f(s[2 * i + 1] - m));
            p1[i] = pk2(__builtin_amdgcn_exp2f(s[8 + 2 * i] - m), __builtin_amdgcn_exp2f(s[8 + 2 * i + 1] - m));
        }
#pragma unroll
        for (int i = 0; i < 16; ++i) o[i] *= al;
        o = mfma16(v0, __builtin_bit_cast(bf16x8, p0), o);
        o = mfma16(v1, __builtin_bit_cast(bf16x8, p1), o);
    }
}

__device__ __forceinline__ void attn_finish(char* __restrict__ ws, const char* kvh, bf16x8 qf, s16x4 qt, f32x16 o, int qt_idx, int head, int lane) {
    const int r = lane & 31, hh = lane >> 5;
    float l = swap32_add(hh ? o[8] : 0.f);
    bool bad = !(l < 1e30f);
#pragma unroll
    for (int i = 0; i < 8; ++i) bad |= !(__builtin_fabsf(o[i]) < 3e38f);
#pragma unroll
    for (int i = 8; i < 12; ++i) bad |= (hh == 0) && !(__builtin_fabsf(o[i]) < 3e38f);
    if (__builtin_expect(__any(bad), 0)) {
        attn_slow(kvh, qf, qt, o, r, hh);
        l = swap32_add(hh ? o[8] : 0.f);
    }
    const float inv = 1.0f / l;
    char* ob = ws + WS_O + (size_t)qt_idx * 40 * 1024;
    *(u32x4*)(ob + head * 1024 + lane * 16) = u32x4{pk2(o[0] * inv, o[1] * inv), pk2(o[2] * inv, o[3] * inv), pk2(o[4] * inv, o[5] * inv), pk2(o[6] * inv, o[7] * inv)};
    if (hh == 0) *(u32x2*)(ob + (32 + (head >> 2)) * 1024 + (((head >> 1) & 1) * 32 + r) * 16 + (head & 1) * 8) = u32x2{pk2(o[8] * inv, o[9] * inv), pk2(o[10] * inv, o[11] * inv)};
}

__device__ __forceinline__ void k2_attn(char* __restrict__ ws, char* lds, int wid, int head, int qb) {
    const int lane = threadIdx.x & 63, r = lane & 31, hh = lane >> 5;
    const unsigned lds0 = (unsigned)(uintptr_t)lds, cnt = lds0 + K2_CNT;
    const int qt_idx = qb * 8 + wid;
    const char* kvh = ws + WS_KV + (size_t)head * 64 * KVT_B;
    const char* src = kvh + wid * KVT_B + lane * 16;
    auto tile_issue = [&](int c) {
        const unsigned base = lds0 + (c & 3) * AT_SLOT + wid * KVT_B;
        glds16(src + c * AT_SLOT, base);
        glds16(src + c * AT_SLOT + 1024, base + 1024);
        if (lane < (KVT_B - 2048) / 16) glds16(src + c * AT_SLOT + 2048, base + 2048);
    };
    tile_issue(0);
    {
        const char* qp = ws + WS_Q + ((size_t)head * 64 + qt_idx) * QT_B + lane * 16;
        glds16(qp, lds0 + K2_H + wid * QT_B);
        if (lane < 16) glds16(qp + 1024, lds0 + K2_H + wid * QT_B + 1024);
    }
    if (wid == 0 && lane < 32) *(__attribute__((address_space(3))) unsigned*)(uintptr_t)(cnt + lane * 4) = lane == 8 ? 8u : 0u;
    asm volatile("s_waitcnt lgkmcnt(0)\n\ts_barrier" ::: "memory");
    const int koff = hh * 512 + r * 16, ktoff = hh ? 1600 : 1024 + r * 8, voff = 1280 + hh * VROW_B + (r < 21 ? r : 21) * 16;
    WAIT_VM(0);
    const lds_cptr qp = (lds_cptr)lds + K2_H + wid * QT_B;
    bf16x8 qf = lds_r128(qp + koff);
    s16x4 qt = lds_r64(qp + 1024 + r * 8);
    asm volatile("s_waitcnt lgkmcnt(0)" : "+v"(qf), "+v"(qt) :: "memory");
    lds_signal(cnt + C_FULL);
    tile_issue(1); tile_issue(2); tile_issue(3);
    lds_await(cnt + C_FULL, lds_peek(cnt + C_FULL), 8);
    STAMPW(1, 1, AW);
    f32x16 o = f32x16{}, sA, sB;
    u32x4 pA0, pA1, pB0 = {0, 0, 0, 0}, pB1 = {0, 0, 0, 0};
    bf16x8 kf, v0, v1; s16x4 kt;
    {
        const lds_cptr sb = (lds_cptr)lds;
        kf = lds_r128(sb + koff); kt = lds_r64(sb + ktoff);
        if (hh) qt = s16x4{0, 0, 0, 0};
        sA = mfma16(kf, qf, f32x16{});
        sA = mfma8(kt, qt, sA);
        float m0 = sA[0];
#pragma unroll
        for (int i = 1; i < 16; ++i) m0 = __builtin_fmaxf(m0, sA[i]);
        m0 = swap32_max(m0);
        const unsigned nm = pk2(-m0, 0.f) & 0xffffu;
        if (hh) qt[0] = (short)nm;
#pragma unroll
        for (int i = 0; i < 16; ++i) sA[i] += bf_lo(nm);
        kf = lds_r128(sb + KVT_B + koff); kt = lds_r64(sb + KVT_B + ktoff);
        v0 = lds_r128(sb + voff); v1 = v0;
    }
#define EX(v) __builtin_amdgcn_exp2f(v)
#define GRP(MF, SC, i, PW, j) do { MF; SBAR(); const float e0 = EX(SC[i]), e1 = EX(SC[i + 1]), e2 = EX(SC[i + 2]), e3 = EX(SC[i + 3]); PW[j] = pk2(e0, e1); PW[j + 1] = pk2(e2, e3); PIN(PW); SBAR(); } while (0)
#define STEP(SC, SN, PC0, PC1, PP0, PP1, KP, VP) do { \
        GRP(SN = mfma16(kf, qf, f32x16{}), SC, 0, PC0, 0); \
        kf = lds_r128((KP) + koff); SBAR(); \
        GRP(o = mfma16(v0, __builtin_bit_cast(bf16x8, PP0), o), SC, 4, PC0, 2); \
        v0 = lds_r128((VP) + voff); SBAR(); \
        GRP(SN = mfma8(kt, qt, SN), SC, 8, PC1, 0); \
        kt = lds_r64((KP) + ktoff); SBAR(); \
        GRP(o = mfma16(v1, __builtin_bit_cast(bf16x8, PP1), o), SC, 12, PC1, 2); \
        v1 = lds_r128((VP) + voff + 2 * VROW_B); SBAR(); \
    } while (0)
    if (wid >= 4) __builtin_amdgcn_s_setprio(AP_MID);
    for (int c = 0; c < 8; ++c) {
        const lds_cptr sb = (lds_cptr)lds + (c & 3) * AT_SLOT;
        const lds_cptr nb = (lds_cptr)lds + ((c + 1) & 3) * AT_SLOT;
        if (wid < 4) __builtin_amdgcn_s_setprio(AP_HI);
        STEP(sA, sB, pA0, pA1, pB0, pB1, sb + 2 * KVT_B, sb + 0 * KVT_B);
        STEP(sB, sA, pB0, pB1, pA0, pA1, sb + 3 * KVT_B, sb + 1 * KVT_B);
        STEP(sA, sB, pA0, pA1, pB0, pB1, sb + 4 * KVT_B, sb + 2 * KVT_B);
        if (c == 0) { WAIT_VM(6); lds_signal(cnt + C_FULL + 4); }
        STEP(sB, sA, pB0, pB1, pA0, pA1, sb + 5 * KVT_B, sb + 3 * KVT_B);
        STEP(sA, sB, pA0, pA1, pB0, pB1, sb + 6 * KVT_B, sb + 4 * KVT_B);
        if (wid < 4) __builtin_amdgcn_s_setprio(AP_LO);
        const unsigned pk = lds_peek(cnt + C_FULL + 4 * (c + 1)), pf = lds_peek(cnt + C_FREE + 4 * c - 4);
        SBAR();
        STEP(sB, sA, pB0, pB1, pA0, pA1, sb + 7 * KVT_B, sb + 5 * KVT_B);
        lds_await(cnt + C_FULL + 4 * (c + 1), pk, 8);
        if (c <= 4) {
            if (c >= 1) { lds_await(cnt + C_FREE + 4 * c - 4, pf, 8); tile_issue(c + 3); }
            WAIT_VM(3);
        } else WAIT_VM(0);
        if (c <= 5) lds_signal(cnt + C_FULL + 4 * (c + 2));
        SBAR();
        STEP(sA, sB, pA0, pA1, pB0, pB1, nb + 0 * KVT_B, sb + 6 * KVT_B);
        STEP(sB, sA, pB0, pB1, pA0, pA1, nb + 1 * KVT_B, sb + 7 * KVT_B);
        lds_signal(cnt + C_FREE + 4 * c);
        STAMPW(1, 2 + c, AW);
    }
    o = mfma16(v0, __builtin_bit_cast(bf16x8, pB0), o);
    o = mfma16(v1, __builtin_bit_cast(bf16x8, pB1), o);
#undef STEP
#undef GRP
#undef EX
    attn_finish(ws, kvh, qf, qt, o, qt_idx, head, lane);
    STAMPW(1, 10, AW);
}

constexpr int mlp_cnt(int i, int nb) {
    int n = 239 - i < nb - 1 ? 239 - i : nb - 1;
    if (192 <= i && i < 192 + nb) n += 6;
    return n;
}
__device__ __forceinline__ f32x2 pk_mul(f32x2 a, f32x2 b) { return a * b; }
__device__ __forceinline__ f32x2 pk_fma(f32x2 a, f32x2 b, f32x2 c) { return __builtin_elementwise_fma(a, b, c); }

__device__ __forceinline__ void k2_mlp(const __bf16* __restrict__ x, const __bf16* __restrict__ b1, const __bf16* __restrict__ b2, char* ws, char* lds, int w, int head, int j, int tt) {
    constexpr int NB = MLP_NB;
    const int lane = threadIdx.x & 63, r = lane & 31, hh = lane >> 5;
    const unsigned lds0 = (unsigned)(uintptr_t)lds, cnt = lds0 + K2_CNT;
    asm volatile("s_barrier" ::: "memory");
    __builtin_amdgcn_s_setprio(MLP_PRIO);
    lds_await(cnt + C_FULL, lds_peek(cnt + C_FULL), 8);
    const int l16 = lane * 16;
    f32x4 ts; float csv[3]; unsigned b1v[3];
    {
        const char* tp = ws + WS_TOK + (size_t)tt * 512;
        const char* cp = ws + WS_CS1 + (j * 384 + w * 96) * 4;
        const char* bp = (const char*)b1 + (j * 384 + w * 96) * 2;
        asm volatile("global_load_dwordx4 %0, %1, %2" : "=v"(ts) : "v"(r * 16), "s"(tp));
        asm volatile("global_load_dword %0, %3, %4\n\tglobal_load_dword %1, %3, %4 offset:128\n\tglobal_load_dword %2, %3, %4 offset:256" : "=&v"(csv[0]), "=&v"(csv[1]), "=&v"(csv[2]) : "v"(r * 4), "s"(cp));
        asm volatile("global_load_ushort %0, %3, %4\n\tglobal_load_ushort %1, %3, %4 offset:64\n\tglobal_load_ushort %2, %3, %4 offset:128" : "=&v"(b1v[0]), "=&v"(b1v[1]), "=&v"(b1v[2]) : "v"(r * 2), "s"(bp));
    }
    {
        const int row = 8 * w + (lane >> 3);
        const char* xs = (const char*)x + (size_t)(tt * 32 + row) * (D_ * 2) + (((lane & 7) ^ ((row >> 1) & 7)) * 16);
#pragma unroll
        for (int st = 0; st < 10; ++st) glds16(xs + st * 128, lds0 + K2_X + st * 4096 + w * 1024);
        if (lane < 20) glds16((const char*)b2 + w * 320 + lane * 16, lds0 + K2_B2 + w * 320);
    }
    const uint64_t wbase = (uint64_t)(ws + WS_PW + (size_t)(j * 4 + w) * 240 * 1024);
    const u32x4 wr = {(unsigned)wbase, (unsigned)(wbase >> 32) & 0xffffu, 240u * 1024u, 0x00020000u};
    bf16x8 wb[NB];
#define ABL_LIM 240
#define WLD(i) do { if ((i) < NB) asm volatile("buffer_load_dwordx4 %0, %1, %2, %3 offen offset:%4" : "=v"(wb[(i) % NB]) : "v"(l16), "s"(wr), "s"(((i) >> 2) * 4096), "n"(((i) & 3) * 1024)); \
        else if ((i) < ABL_LIM) asm volatile("buffer_load_dwordx4 %0, %1, %2, %3 offen offset:%4" : "+v"(wb[(i) % NB]) : "v"(l16), "s"(wr), "s"(((i) >> 2) * 4096), "n"(((i) & 3) * 1024)); SBAR(); } while (0)
#define WWAIT(i) asm volatile("s_waitcnt vmcnt(%1)" : "+v"(wb[(i) % NB]) : "n"(mlp_cnt(i, NB)))
#define MMA(i, b, c) ({ if (MLP_SLEEP) __builtin_amdgcn_s_sleep(MLP_SLEEP); WWAIT(i); mfma16(wb[(i) % NB], b, c); })
#pragma unroll
    for (int i = 0; i < NB; ++i) WLD(i);
    asm volatile("s_waitcnt vmcnt(%7)" : "+v"(ts), "+v"(csv[0]), "+v"(csv[1]), "+v"(csv[2]), "+v"(b1v[0]), "+v"(b1v[1]), "+v"(b1v[2]) : "n"(NB) : "memory");
    lds_signal(cnt + C_X);
    lds_await(cnt + C_X, lds_peek(cnt + C_X), 4);
    STAMPW(2, 1, 8);
#define SERVICE(NVM, cf, ci) STAMPW(2, (cf) - 1, 8)
    const lds_cptr xb = (lds_cptr)lds + K2_X;
    unsigned xa[4];
#pragma unroll
    for (int k = 0; k < 4; ++k) { xa[k] = lds0 + K2_X + r * 128 + (((2 * k + hh) ^ ((r >> 1) & 7)) * 16); PIN(xa[k]); }
    f32x16 acc[3]; acc[0] = f32x16{}; acc[1] = f32x16{}; acc[2] = f32x16{};
    bf16x8 xf[2];
    xf[0] = lds_r128((lds_cptr)(uintptr_t)xa[0]);
#pragma unroll
    for (int ks = 0; ks < 40; ++ks) {
        if (ks == 12) SERVICE(24, 3, 4);
        if (ks == 26) SERVICE(24, 4, 5);
        if (ks + 1 < 40) xf[(ks + 1) & 1] = lds_r128((lds_cptr)(uintptr_t)xa[(ks + 1) & 3] + ((ks + 1) >> 2) * 4096);
        SBAR();
#pragma unroll
        for (int f = 0; f < 3; ++f) {
            acc[f] = MMA(3 * ks + f, xf[ks & 1], acc[f]);
            SBAR();
            WLD(3 * ks + f + NB);
        }
        SBAR();
    }
    SERVICE(24, 5, 6);
    __builtin_amdgcn_s_setprio(GELU_PRIO);
    {
        unsigned m1, m2, m3; split3(hh ? ts[2] : -ts[0], m1, m2, m3);
        u32x4 tf = {m1 | (m1 << 16), m2 | (m1 << 16), m2 | (m3 << 16), m2 | (m3 << 16)};
        if (hh) tf = u32x4{m1 | (m2 << 16), m3, 0, 0};
#pragma unroll
        for (int f = 0; f < 3; ++f) {
            unsigned a1, a2, a3; split3(csv[f], a1, a2, a3);
            u32x4 ff = {a1 | (a2 << 16), a1 | (a3 << 16), a2 | (a1 << 16), a3 | (a2 << 16)};
            if (hh) ff = u32x4{b1v[f] | (b1v[f] << 16), b1v[f], 0, 0};
            acc[f] = mfma16(__builtin_bit_cast(bf16x8, ff), __builtin_bit_cast(bf16x8, tf), acc[f]);
            SBAR();
        }
    }
    {
        constexpr float C0 = -2.0f * 0.7978845608028654f * 1.4426950408889634f, C1 = C0 * 0.044715f;
        const float s2 = ts[1], sg = ts[2];
        const f32x2 k0 = {C0 * s2, C0 * s2}, k1 = {C1 * s2 * s2 * s2, C1 * s2 * s2 * s2}, sg2 = {sg, sg};
        __attribute__((address_space(3))) u32x4* hp = (__attribute__((address_space(3))) u32x4*)(uintptr_t)(lds0 + K2_H + w * 6144 + lane * 16);
#pragma unroll
        for (int f = 0; f < 3; ++f) {
#pragma unroll
            for (int s = 0; s < 2; ++s) {
                u32x4 u;
#pragma unroll
                for (int i = 0; i < 4; ++i) {
                    const f32x2 a = {acc[f][8 * s + 2 * i], acc[f][8 * s + 2 * i + 1]};
                    const f32x2 z = pk_mul(a, pk_fma(pk_mul(a, a), k1, k0));
                    const f32x2 e = {__builtin_amdgcn_exp2f(z[0]), __builtin_amdgcn_exp2f(z[1])};
                    const f32x2 d = pk_fma(e, sg2, sg2);
                    const f32x2 hv = pk_mul(a, f32x2{__builtin_amdgcn_rcpf(d[0]), __builtin_amdgcn_rcpf(d[1])});
                    u[i] = pk2(hv[0], hv[1]);
                }
                hp[(2 * f + s) * 64] = u;
                SBAR();
            }
        }
    }
    asm volatile("s_waitcnt lgkmcnt(0)" ::: "memory");
    lds_signal(cnt + C_H);
    __builtin_amdgcn_s_setprio(MLP_PRIO);
    lds_await(cnt + C_H, lds_peek(cnt + C_H), 4);
    STAMPW(2, 8, 8);
    unsigned ha = lds0 + K2_H + lane * 16;
    PIN(ha);
    const lds_cptr hb = (lds_cptr)(uintptr_t)ha;
    char* pp = ws + WS_PART + (((size_t)j * 64 + tt) * 20 + w * 5) * 2048 + lane * 16;
    auto store_tile = [&](const f32x16& t, int f) {
#pragma unroll
        for (int c = 0; c < 2; ++c) {
            float a[8];
#pragma unroll
            for (int i = 0; i < 8; ++i) a[i] = t[8 * c + i];
            if (j == 0) {
                const int col = (w * 5 + f) * 32 + c * 16 + hh * 8;
                const u32x4 xv = __builtin_bit_cast(u32x4, lds_r128(xb + (col >> 6) * 4096 + r * 128 + ((((col >> 3) & 7) ^ ((r >> 1) & 7)) * 16)));
                const u32x4 bv = __builtin_bit_cast(u32x4, lds_r128((lds_cptr)lds + K2_B2 + col * 2));
#pragma unroll
                for (int i = 0; i < 4; ++i) { a[2 * i] += bf_lo(xv[i]) + bf_lo(bv[i]); a[2 * i + 1] += bf_hi(xv[i]) + bf_hi(bv[i]); }
            }
            st16(pp + (f * 2 + c) * 1024, u32x4{pk2(a[0], a[1]), pk2(a[2], a[3]), pk2(a[4], a[5]), pk2(a[6], a[7])});
            SBAR();
        }
    };
    acc[0] = f32x16{}; acc[1] = f32x16{}; acc[2] = f32x16{};
    xf[0] = lds_r128(hb);
#pragma unroll
    for (int ks = 0; ks < 24; ++ks) {
        if (ks == 10) SERVICE(24, 6, 7);
        xf[(ks + 1) & 1] = lds_r128(hb + ((ks + 1) % 24) * 1024);
        SBAR();
#pragma unroll
        for (int f = 0; f < 3; ++f) {
            acc[f] = MMA(120 + 3 * ks + f, xf[ks & 1], acc[f]);
            SBAR();
            WLD(120 + 3 * ks + f + NB);
        }
        SBAR();
    }
    SERVICE(24, 7, 8);
    store_tile(acc[0], 0); store_tile(acc[1], 1); store_tile(acc[2], 2);
    SBAR();
    f32x16 ac2[2]; ac2[0] = f32x16{}; ac2[1] = f32x16{};
#pragma unroll
    for (int ks = 0; ks < 24; ++ks) {
        if (ks + 1 < 24) xf[(ks + 1) & 1] = lds_r128(hb + (ks + 1) * 1024);
        SBAR();
#pragma unroll
        for (int f = 0; f < 2; ++f) {
            ac2[f] = MMA(192 + 2 * ks + f, xf[ks & 1], ac2[f]);
            SBAR();
            WLD(192 + 2 * ks + f + NB);
        }
        SBAR();
    }
    STAMPW(2, 9, 8);
#undef WLD
#undef SERVICE
    store_tile(ac2[0], 3); store_tile(ac2[1], 4);
    STAMPW(2, 10, 8);
}

__global__ __launch_bounds__(768, 3) void k2_main(const __bf16* __restrict__ x, const __bf16* __restrict__ b1, const __bf16* __restrict__ b2, char* ws) {
    extern __shared__ __attribute__((aligned(16))) char lds[];
    const int wid = __builtin_amdgcn_readfirstlane(threadIdx.x >> 6);
    const int b = blockIdx.x, xcd = b & 7, head = xcd * 4 + ((b >> 3) & 3), qb = b >> 5;
    STAMPW(1, 0, AW);
    STAMPW(2, 0, 8);
    if (wid < 8) k2_attn(ws, lds, wid, head, qb);
    else k2_mlp(x, b1, b2, ws, lds, wid - 8, head, xcd & 3, (xcd >> 2) * 32 + (b >> 3));
}

constexpr int K3_LDS = 40960 + 5 * 4096;

__global__ __launch_bounds__(640, 1) void k3_out(const char* ws, __bf16* __restrict__ out) {
    extern __shared__ __attribute__((aligned(16))) char lds[];
    const int tid = threadIdx.x, lane = tid & 63, r = lane & 31, hh = lane >> 5;
    const int wid = __builtin_amdgcn_readfirstlane(tid >> 6), f = wid % 5, kq = wid / 5;
    const int b = blockIdx.x, i = b >> 3, tt = (b & 7) * 8 + (i >> 2), ng = i & 3, ft = ng * 5 + f;
    const unsigned lds0 = (unsigned)(uintptr_t)lds;
    STAMP(4, 0);
    const char* op = ws + WS_O + ((size_t)tt * 40 + wid * 4) * 1024 + lane * 16;
#pragma unroll
    for (int k = 0; k < 4; ++k) glds16(op + k * 1024, lds0 + (wid * 4 + k) * 1024);
    const char* wp = ws + WS_PWO + (size_t)ft * 40 * 1024 + lane * 16;
    bf16x8 wf[20];
#pragma unroll
    for (int k = 0; k < 16; ++k) { wf[k] = *(const bf16x8*)(wp + (kq * 16 + k) * 1024); SBAR(); }
#pragma unroll
    for (int k = 0; k < 4; ++k) { wf[16 + k] = *(const bf16x8*)(wp + (32 + kq * 4 + k) * 1024); SBAR(); }
    u32x4 pv[4];
#pragma unroll
    for (int k = 0; k < 4; ++k) pv[k] = *(const u32x4*)(ws + WS_PART + (((size_t)k * 64 + tt) * 20 + ft) * 2048 + kq * 1024 + lane * 16);
    SBAR();
    WAIT_BAR(24);
    STAMP(4, 1);
    f32x16 acc = f32x16{};
    const lds_cptr ob = (lds_cptr)lds + lane * 16;
#pragma unroll
    for (int k = 0; k < 20; ++k) acc = mfma16(wf[k], lds_r128(ob + (k < 16 ? kq * 16 + k : 32 + kq * 4 + (k - 16)) * 1024), acc);
    STAMP(4, 2);
    f32x4* red = (f32x4*)(lds + 40960 + f * 4096);
    {
        const int c = 1 - kq;
        red[(2 * c) * 64 + lane] = kq ? f32x4{acc[0], acc[1], acc[2], acc[3]} : f32x4{acc[8], acc[9], acc[10], acc[11]};
        red[(2 * c + 1) * 64 + lane] = kq ? f32x4{acc[4], acc[5], acc[6], acc[7]} : f32x4{acc[12], acc[13], acc[14], acc[15]};
    }
    __syncthreads();
    {
        const f32x4 a = red[(2 * kq) * 64 + lane], bb = red[(2 * kq + 1) * 64 + lane];
        float m[8];
#pragma unroll
        for (int k = 0; k < 8; ++k) m[k] = kq ? acc[8 + k] : acc[k];
#pragma unroll
        for (int k = 0; k < 4; ++k) {
            m[2 * k] += (bf_lo(pv[0][k]) + bf_lo(pv[1][k])) + (bf_lo(pv[2][k]) + bf_lo(pv[3][k]));
            m[2 * k + 1] += (bf_hi(pv[0][k]) + bf_hi(pv[1][k])) + (bf_hi(pv[2][k]) + bf_hi(pv[3][k]));
        }
        u32x4 u;
        u[0] = pk2(a[0] + m[0], a[1] + m[1]); u[1] = pk2(a[2] + m[2], a[3] + m[3]);
        u[2] = pk2(bb[0] + m[4], bb[1] + m[5]); u[3] = pk2(bb[2] + m[6], bb[3] + m[7]);
        *(u32x4*)(out + (size_t)(tt * 32 + r) * D_ + ft * 32 + kq * 16 + hh * 8) = u;
    }
    if (b == 0 && tid == 0) (void)__hip_atomic_fetch_add((unsigned*)(ws + WS_EPOCH), 1u, __ATOMIC_RELAXED, __HIP_MEMORY_SCOPE_AGENT);
    STAMP(4, 3);
}

}

extern "C" void kernel_launch(void* const* d_in, const int* in_sizes, int n_in, void* d_out, int out_size, void* d_ws, size_t ws_size, hipStream_t stream) {
    const __bf16* x = (const __bf16*)d_in[0];
    const __bf16* wqkv = (const __bf16*)d_in[1];
    const __bf16* wo = (const __bf16*)d_in[2];
    const __bf16* w1 = (const __bf16*)d_in[4];
    const __bf16* b1 = (const __bf16*)d_in[5];
    const __bf16* w2 = (const __bf16*)d_in[6];
    const __bf16* b2 = (const __bf16*)d_in[7];
    char* ws = (char*)d_ws;
    (void)hipFuncSetAttribute((const void*)k1_qkv, hipFuncAttributeMaxDynamicSharedMemorySize, K1_LDS);
    (void)hipFuncSetAttribute((const void*)k2_main, hipFuncAttributeMaxDynamicSharedMemorySize, K2_LDS);
    (void)hipFuncSetAttribute((const void*)k3_out, hipFuncAttributeMaxDynamicSharedMemorySize, K3_LDS);
    k1_qkv<<<256, 768, K1_LDS, stream>>>(x, wqkv, wo, w1, w2, ws);
    k2_main<<<256, 768, K2_LDS, stream>>>(x, b1, b2, ws);
    k3_out<<<256, 640, K3_LDS, stream>>>(ws, (__bf16*)d_out);
}
```

```cpp
#include <hip/hip_runtime.h>
#include <hip/hip_bf16.h>
#include <stdint.h>

namespace {

typedef __attribute__((ext_vector_type(8))) __bf16 bf16x8;
typedef __attribute__((ext_vector_type(2))) __bf16 bf16x2;
typedef __attribute__((ext_vector_type(8))) short s16x8;
typedef __attribute__((ext_vector_type(4))) short s16x4;
typedef __attribute__((ext_vector_type(16))) float f32x16;
typedef __attribute__((ext_vector_type(4))) float f32x4;
typedef __attribute__((ext_vector_type(2))) float f32x2;
typedef __attribute__((ext_vector_type(4))) unsigned u32x4;
typedef __attribute__((ext_vector_type(2))) unsigned u32x2;
typedef __attribute__((address_space(3))) char* lds_ptr;
typedef __attribute__((address_space(3))) const char* lds_cptr;

constexpr int S_ = 2048, D_ = 640, H_ = 32, F_ = 1536;
constexpr float EPS = 1e-5f;
constexpr float QSCALE = 0.22360679774997896f * 1.4426950408889634f;

constexpr int QT_B = 1280;
constexpr int VROW_B = 352;
constexpr int KVT_B = 1280 + 4 * VROW_B;
constexpr size_t WS_Q = 0;
constexpr size_t WS_KV = WS_Q + (size_t)H_ * 64 * QT_B;
constexpr size_t WS_TOK = WS_KV + (size_t)H_ * 64 * KVT_B + 4096;
constexpr size_t WS_CS1 = WS_TOK + (size_t)S_ * 16;
constexpr size_t WS_PW = WS_CS1 + 8192;
constexpr size_t WS_PWO = WS_PW + (size_t)16 * 240 * 1024;
constexpr size_t WS_PART = WS_PWO + (size_t)20 * 40 * 1024;
constexpr size_t WS_O = WS_PART + (size_t)4 * 64 * 20 * 2048;
constexpr size_t WS_RT = WS_O + (size_t)64 * 40 * 1024;
constexpr size_t WS_RC = WS_RT + (size_t)S_ * 16;
constexpr size_t WS_END = WS_RC + (size_t)4 * 3 * D_ * 8;

#define STAMPW(kid, i, w)
#define STAMP(kid, i) STAMPW(kid, i, 0)
#define SBAR() __builtin_amdgcn_sched_barrier(0)
#define PIN(x) asm volatile("" : "+v"(x))
#define WAIT_BAR(N) asm volatile("s_waitcnt vmcnt(" #N ") lgkmcnt(0)\n\ts_barrier" ::: "memory")
#define WAIT_VM(N) asm volatile("s_waitcnt vmcnt(" #N ")" ::: "memory")

__device__ __forceinline__ f32x16 mfma16(bf16x8 a, bf16x8 b, f32x16 c) { return __builtin_amdgcn_mfma_f32_32x32x16_bf16(a, b, c, 0, 0, 0); }
__device__ __forceinline__ f32x16 mfma8(s16x4 a, s16x4 b, f32x16 c) { return __builtin_amdgcn_mfma_f32_32x32x8bf16_1k(a, b, c, 0, 0, 0); }
__device__ __forceinline__ unsigned pk2(float lo, float hi) { f32x2 v = {lo, hi}; return __builtin_bit_cast(unsigned, __builtin_convertvector(v, bf16x2)); }
__device__ __forceinline__ float bf_lo(unsigned u) { return __uint_as_float(u << 16); }
__device__ __forceinline__ float bf_hi(unsigned u) { return __uint_as_float(u & 0xffff0000u); }
__device__ __forceinline__ void glds16(const void* g, unsigned lds_base) {
    unsigned sv;
    asm volatile("s_mov_b32 %0, m0\n\ts_mov_b32 m0, %2\n\ts_nop 0\n\tglobal_load_lds_dwordx4 %1, off\n\ts_mov_b32 m0, %0" : "=&s"(sv) : "v"(g), "s"(lds_base) : "memory");
}
__device__ __forceinline__ void glds4(const void* g, unsigned lds_base) {
    unsigned sv;
    asm volatile("s_mov_b32 %0, m0\n\ts_mov_b32 m0, %2\n\ts_nop 0\n\tglobal_load_lds_dword %1, off\n\ts_mov_b32 m0, %0" : "=&s"(sv) : "v"(g), "s"(lds_base) : "memory");
}
__device__ __forceinline__ bf16x8 lds_r128(lds_cptr p) { return *(const __attribute__((address_space(3))) bf16x8*)p; }
__device__ __forceinline__ s16x4 lds_r64(lds_cptr p) { return *(const __attribute__((address_space(3))) s16x4*)p; }
__device__ __forceinline__ s16x4 lds_tr(lds_cptr p) { return __builtin_amdgcn_ds_read_tr16_b64_v4i16((__attribute__((address_space(3))) s16x4*)p); }
__device__ __forceinline__ bf16x8 cat8(s16x4 a, s16x4 b) { s16x8 v = {a[0], a[1], a[2], a[3], b[0], b[1], b[2], b[3]}; return __builtin_bit_cast(bf16x8, v); }
__device__ __forceinline__ float dot2(unsigned a, unsigned b, float c) { return __builtin_amdgcn_fdot2_f32_bf16(__builtin_bit_cast(bf16x2, a), __builtin_bit_cast(bf16x2, b), c, false); }
__device__ __forceinline__ float swap32_add(float v) { auto rr = __builtin_amdgcn_permlane32_swap(__float_as_uint(v), __float_as_uint(v), false, false); return __uint_as_float(rr[0]) + __uint_as_float(rr[1]); }
__device__ __forceinline__ float swap32_max(float v) { auto rr = __builtin_amdgcn_permlane32_swap(__float_as_uint(v), __float_as_uint(v), false, false); return __builtin_fmaxf(__uint_as_float(rr[0]), __uint_as_float(rr[1])); }
__device__ __forceinline__ void split3(float v, unsigned& a, unsigned& b, unsigned& c) {
    a = pk2(v, 0.f) & 0xffffu; float r = v - bf_lo(a);
    b = pk2(r, 0.f) & 0xffffu; r = r - bf_lo(b);
    c = pk2(r, 0.f) & 0xffffu;
}
__device__ __forceinline__ void st16(void* p, u32x4 v) {
    asm volatile("global_store_dwordx4 %0, %1, off sc1\n\ts_nop 1" :: "v"(p), "v"(v) : "memory");
}
__device__ __forceinline__ void st8(void* p, u32x2 v) { asm volatile("global_store_dwordx2 %0, %1, off sc1\n\ts_nop 1" :: "v"(p), "v"(v) : "memory"); }
#define QST16(p, v) (*(u32x4*)(p) = (v))
#define QST8(p, v) (*(u32x2*)(p) = (v))
#define PST16(p, v) st16((p), (v))
constexpr unsigned ONES2 = 0x3f803f80u;
typedef unsigned long long u64;
__device__ __forceinline__ u64 ld_agent(const u64* p) { return __hip_atomic_load(p, __ATOMIC_RELAXED, __HIP_MEMORY_SCOPE_AGENT); }
__device__ __forceinline__ void st_agent(u64* p, float v, unsigned tag) { __hip_atomic_store(p, (u64)__float_as_uint(v) | ((u64)tag << 32), __ATOMIC_RELAXED, __HIP_MEMORY_SCOPE_AGENT); }
__device__ __forceinline__ unsigned now_ticks() { return (unsigned)__builtin_amdgcn_s_memrealtime(); }
#define K1_SPIN 4096
__device__ __forceinline__ void lds_signal(unsigned addr, unsigned val = 1u) {
    asm volatile("s_mov_b64 exec, 1\n\tds_add_u32 %0, %1\n\ts_mov_b64 exec, -1" :: "v"(addr), "v"(val) : "memory");
}
__device__ __forceinline__ unsigned lds_peek(unsigned addr) { return *(const volatile __attribute__((address_space(3))) unsigned*)(uintptr_t)addr; }
__device__ __forceinline__ void lds_await(unsigned addr, unsigned first, unsigned target) {
    unsigned v = __builtin_amdgcn_readfirstlane(first);
    while (v < target) { __builtin_amdgcn_s_sleep(1); v = __builtin_amdgcn_readfirstlane(lds_peek(addr)); }
    asm volatile("" ::: "memory");
}

constexpr int K1_STAGE = 16384 + 20480, K1_AUX = 4 * K1_STAGE, K1_LDS = K1_AUX + 4096 + 5120 + 1024 + 256;

__device__ __forceinline__ constexpr unsigned k1_perm(int j) {
    return j == 0 ? 0x76453120u : (j == 3 ? 0x57064231u : (j == 4 ? 0x76254130u : 0x75643120u));
}
__device__ __forceinline__ int k1_piece_off(int P, int r, int& head) {
    head = P / 5; int rr = P - head * 5; const int h7 = head & 7;
    int role = rr;
    if (h7 == 1) role = (0x32410 >> (4 * rr)) & 7;
    if (h7 == 3) role = (0x32104 >> (4 * rr)) & 7;
    return role == 4 ? 1024 + r * 8 : (role >> 1) * 512 + r * 16 + (role & 1) * 8;
}

constexpr int K1_TF = K1_AUX, K1_FF = K1_AUX + 4096, K1_RS = K1_AUX + 9216;

template <bool VORI, int WN>
__device__ __forceinline__ void k1_body(char* __restrict__ ws, char* lds, int mt, int nt) {
    constexpr int NF = WN ? 2 : 3, F0 = WN ? 3 : 0;
    const int tid = threadIdx.x, lane = tid & 63, r = lane & 31, hh = lane >> 5;
    const int wid = __builtin_amdgcn_readfirstlane(tid >> 6), wm = wid & 3;
    const unsigned lds0 = (unsigned)(uintptr_t)lds;
    STAMP(0, 0);
    const int xrow = 32 * wm + r;
    const int q4 = (lane & 15) >> 2, p4 = lane & 3, gi = (lane >> 4) & 1;
    unsigned xa[4], wa[NF];
#pragma unroll
    for (int k = 0; k < 4; ++k) xa[k] = lds0 + xrow * 128 + (((2 * k + hh) ^ ((xrow >> 1) & 7)) * 16);
#pragma unroll
    for (int f = 0; f < NF; ++f) {
        int rel = 4 * gi + p4;
        if (!VORI) rel = (k1_perm(F0 + f) >> (4 * rel)) & 7;
        wa[f] = lds0 + 16384 + (8 * hh + q4) * 320 + (F0 + f) * 64 + rel * 8;
    }
    const int tt = mt * 4 + wm;
    const bool isq = nt < 4;
    char* const base = (!VORI && isq) ? ws + WS_Q + (size_t)tt * QT_B : ws + WS_KV + (size_t)tt * KVT_B;
    unsigned so[NF][3];
#pragma unroll
    for (int f = 0; f < NF; ++f) {
        if (!VORI) {
            const unsigned hstride = isq ? 64 * QT_B : 64 * KVT_B;
            const int T = (nt & 3) * 5 + F0 + f;
            const unsigned perm = k1_perm(F0 + f);
#pragma unroll
            for (int i = 0; i < 3; ++i) {
                int head;
                const int off = k1_piece_off(8 * T + ((perm >> (4 * ((i ? 2 + 2 * i : 0) + hh))) & 7), r, head);
                so[f][i] = head * hstride + off;
                PIN(so[f][i]);
            }
        } else {
            const int col = (nt - 8) * 160 + (F0 + f) * 32 + r;
            const int head = col / 20, d = col - head * 20;
            so[f][0] = head * (64 * KVT_B) + 1280 + hh * VROW_B + d * 16;
            so[f][1] = d;
            PIN(so[f][0]); PIN(so[f][1]);
        }
    }
    f32x16 acc[NF];
#pragma unroll
    for (int f = 0; f < NF; ++f) acc[f] = f32x16{};
    bf16x8 xf[4], wf[4][NF];
#define K1_RD(sl, ks) do { xf[ks] = lds_r128((lds_cptr)(uintptr_t)(xa[ks] + (sl))); \
        _Pragma("unroll") for (int f = 0; f < NF; ++f) { const lds_cptr p = (lds_cptr)(uintptr_t)(wa[f] + (sl)) + (ks) * 5120; wf[ks][f] = cat8(lds_tr(p), lds_tr(p + 1280)); } SBAR(); } while (0)
#define K1_MM(b) do { _Pragma("unroll") for (int f = 0; f < NF; ++f) acc[f] = VORI ? mfma16(xf[b], wf[b][f], acc[f]) : mfma16(wf[b][f], xf[b], acc[f]); SBAR(); } while (0)
    asm volatile("s_barrier" ::: "memory");
    STAMP(0, 1);
    __builtin_amdgcn_s_waitcnt(0xc07f);
    K1_RD(0u, 0); K1_RD(0u, 1);
#pragma unroll 1
    for (int st = 0; st < 10; ++st) {
        const unsigned sl = (st & 3) * K1_STAGE, sn = ((st + 1) & 3) * K1_STAGE;
        K1_RD(sl, 2); K1_MM(0);
        K1_RD(sl, 3); K1_MM(1);
        if (st < 9) asm volatile("s_waitcnt lgkmcnt(0)\n\ts_barrier" ::: "memory");
        K1_RD(sn, 0); K1_MM(2);
        K1_RD(sn, 1); K1_MM(3);
    }
#undef K1_RD
#undef K1_MM
    STAMP(0, 3);
    asm volatile("s_barrier" ::: "memory");
    const lds_cptr aux = (lds_cptr)lds;
    const bf16x8 tfb = lds_r128(aux + K1_TF + wm * 1024 + lane * 16);
    bf16x8 ffb[NF];
#pragma unroll
    for (int f = 0; f < NF; ++f) ffb[f] = lds_r128(aux + K1_FF + (F0 + f) * 1024 + lane * 16);
    const float* rsl = (const float*)(lds + K1_RS) + wm * 32;
    float rs[VORI ? 16 : 1];
    if (VORI) {
#pragma unroll
        for (int g = 0; g < 4; ++g) { const f32x4 t = *(const f32x4*)(rsl + 8 * g + 4 * hh); rs[4 * g] = t[0]; rs[4 * g + 1] = t[1]; rs[4 * g + 2] = t[2]; rs[4 * g + 3] = t[3]; }
    } else rs[0] = rsl[r] * (isq ? QSCALE : 1.f);
#pragma unroll
    for (int f = 0; f < NF; ++f) acc[f] = VORI ? mfma16(tfb, ffb[f], acc[f]) : mfma16(ffb[f], tfb, acc[f]);
    STAMP(0, 4);
#pragma unroll
    for (int f = 0; f < NF; ++f) {
        unsigned u[8];
#pragma unroll
        for (int i = 0; i < 8; ++i) u[i] = pk2(acc[f][2 * i] * rs[VORI ? 2 * i : 0], acc[f][2 * i + 1] * rs[VORI ? 2 * i + 1 : 0]);
        if (!VORI) {
            QST16(base + so[f][0], (u32x4{u[0], u[1], u[2], u[3]}));
            QST8(base + so[f][1], (u32x2{u[4], u[5]}));
            QST8(base + so[f][2], (u32x2{u[6], u[7]}));
        } else {
            char* p = base + so[f][0];
            QST16(p, (u32x4{u[0], u[1], u[2], u[3]}));
            QST16(p + 2 * VROW_B, (u32x4{u[4], u[5], u[6], u[7]}));
            if (so[f][1] == 19) {
                QST16(p + 16, (u32x4{ONES2, ONES2, ONES2, ONES2})); QST16(p + 2 * VROW_B + 16, (u32x4{ONES2, ONES2, ONES2, ONES2}));
                QST16(p + 32, (u32x4{0, 0, 0, 0})); QST16(p + 2 * VROW_B + 32, (u32x4{0, 0, 0, 0}));
            }
        }
    }
}

template <bool VORI>
__device__ __forceinline__ void k1_helper(const __bf16* __restrict__ x, const __bf16* __restrict__ w, char* __restrict__ ws, char* lds, int mt, int nt, int hw) {
    const int lane = threadIdx.x & 63, r = lane & 31, hh = lane >> 5;
    const unsigned lds0 = (unsigned)(uintptr_t)lds;
    const unsigned t_start = now_ticks();
    unsigned xo[4], wo[5];
#pragma unroll
    for (int i = 0; i < 4; ++i) {
        const int row = 8 * (4 * hw + i) + (lane >> 3);
        xo[i] = row * (D_ * 2) + (((lane & 7) ^ ((row >> 1) & 7)) * 16) - i * 1024;
    }
#pragma unroll
    for (int i = 0; i < 5; ++i) {
        const int idx = (5 * hw + i) * 64 + lane, krow = idx / 20, ch = idx - krow * 20;
        wo[i] = krow * (3 * D_ * 2) + ch * 16 - (i & 3) * 1024;
    }
    const char* xs = (const char*)x + (size_t)mt * 128 * (D_ * 2);
    const char* wsr = (const char*)w + nt * 320;
#define K1_M0(v) asm volatile("s_mov_b32 m0, %0\n\ts_nop 0" :: "s"(v) : "memory")
#define K1_DMA(vo, sp, imm) asm volatile("global_load_lds_dwordx4 %0, %1 offset:" #imm :: "v"(vo), "s"(sp) : "memory")
#define K1_PIECE(i, xp, wp, lb) do { \
        if ((i) == 0) { K1_M0((lb) + hw * 4096); K1_DMA(xo[0], xp, 0); } \
        if ((i) == 1) K1_DMA(xo[1], xp, 1024); \
        if ((i) == 2) K1_DMA(xo[2], xp, 2048); \
        if ((i) == 3) K1_DMA(xo[3], xp, 3072); \
        if ((i) == 4) { K1_M0((lb) + 16384 + hw * 5120); K1_DMA(wo[0], wp, 0); } \
        if ((i) == 5) K1_DMA(wo[1], wp, 1024); \
        if ((i) == 6) K1_DMA(wo[2], wp, 2048); \
        if ((i) == 7) K1_DMA(wo[3], wp, 3072); \
        if ((i) == 8) { K1_M0((lb) + 16384 + hw * 5120 + 4096); K1_DMA(wo[4], wp, 0); } \
        SBAR(); } while (0)
#pragma unroll 1
    for (int st = 0; st < 4; ++st) {
        const char* xp = xs + st * 128; const char* wp = wsr + (size_t)st * 64 * (3 * D_ * 2);
        const unsigned lb = lds0 + st * K1_STAGE;
#pragma unroll
        for (int i = 0; i < 9; ++i) K1_PIECE(i, xp, wp, lb);
    }
    WAIT_BAR(27);
#pragma unroll 1
    for (int st = 0; st < 7; ++st) {
        if (st) {
            const char* xp = xs + (st + 3) * 128; const char* wp = wsr + (size_t)(st + 3) * 64 * (3 * D_ * 2);
            const unsigned lb = lds0 + ((st + 3) & 3) * K1_STAGE;
#pragma unroll
            for (int i = 0; i < 9; ++i) K1_PIECE(i, xp, wp, lb);
        }
        WAIT_BAR(18);
    }
#undef K1_PIECE
#undef K1_DMA
#undef K1_M0
    const int tok = mt * 128 + 32 * hw + r;
    int col[2];
#pragma unroll
    for (int f = 0; f < 2; ++f) {
        const int t = f ? 0 : hw + 1;
        const int pc = VORI ? (r >> 2) : (int)((k1_perm(t) >> (4 * (r >> 2))) & 7);
        col[f] = nt * 160 + t * 32 + pc * 4 + (r & 3);
    }
    const u64* rt = (const u64*)(ws + WS_RT) + tok * 2 + hh;
    const u64* rc = (const u64*)(ws + WS_RC) + hh * (2 * 3 * D_);
    u64 rec[5];
#define K1_POLL() do { rec[0] = ld_agent(rt); rec[1] = ld_agent(rc + col[0]); rec[2] = ld_agent(rc + 3 * D_ + col[0]); rec[3] = ld_agent(rc + col[1]); rec[4] = ld_agent(rc + 3 * D_ + col[1]); } while (0)
    K1_POLL();
    WAIT_BAR(14);
    WAIT_BAR(5);
    bool ok = false;
#pragma unroll 1
    for (int it = 0; it < K1_SPIN; ++it) {
        WAIT_VM(0);
        const unsigned span = now_ticks() - t_start + 32u;
        bool good = true;
#pragma unroll
        for (int i = 0; i < 5; ++i) good = good && (unsigned)(rec[i] >> 32) - t_start <= span;
        if (__all(good)) { ok = true; break; }
        __builtin_amdgcn_s_sleep(8);
        K1_POLL();
    }
#undef K1_POLL
    float tv = __uint_as_float((unsigned)rec[0]);
    float cs0 = __uint_as_float((unsigned)rec[1]) + __uint_as_float((unsigned)rec[2]), cs1 = __uint_as_float((unsigned)rec[3]) + __uint_as_float((unsigned)rec[4]);
    if (__builtin_expect(!ok, 0)) {
        const u32x4* rowp = (const u32x4*)(x + (size_t)tok * D_) + hh * 8;
        float mu = 0.f;
#pragma unroll 1
        for (int pass = 0; pass < 2; ++pass) {
            float s1 = 0.f, s2 = 0.f, s3 = 0.f;
#pragma unroll 1
            for (int c = 0; c < 8; ++c) {
                float a0 = 0.f, a1 = 0.f;
#pragma unroll 1
                for (int i = 0; i < 5; ++i) {
                    const u32x4 u = rowp[c + 16 * i];
#pragma unroll
                    for (int j = 0; j < 4; ++j) {
                        if (pass == 0) a0 = dot2(u[j], ONES2, a0);
                        else { const float d0 = bf_lo(u[j]) - mu, d1 = bf_hi(u[j]) - mu; a0 = __builtin_fmaf(d0, d0, a0); a1 = __builtin_fmaf(d1, d1, a1); }
                    }
                }
                if (pass) a0 += a1;
                s1 = (c & 1) ? s1 + a0 : a0;
                if (c & 1) s2 = (c & 2) ? s2 + s1 : s1;
                if ((c & 3) == 3) s3 = (c & 4) ? s3 + s2 : s2;
            }
            float tot = swap32_add(s3) * (1.f / D_);
            PIN(tot);
            if (pass == 0) mu = tot; else tv = hh ? __builtin_amdgcn_rsqf(tot + EPS) : mu;
        }
        float q[2][2];
#pragma unroll
        for (int kq = 0; kq < 2; ++kq) {
            const unsigned short* wc = (const unsigned short*)w + (size_t)(2 * hh + kq) * 160 * (3 * D_);
            float t0[4] = {0.f, 0.f, 0.f, 0.f}, t1[4] = {0.f, 0.f, 0.f, 0.f};
#pragma unroll 1
            for (int wv = 0; wv < 8; ++wv) {
#pragma unroll
                for (int g = 0; g < 4; ++g) {
                    const unsigned short* rw = wc + (wv * 20 + g) * (3 * D_);
                    float c0 = bf_lo(rw[col[0]]), c1 = bf_lo(rw[col[1]]);
#pragma unroll
                    for (int i = 1; i < 5; ++i) { c0 += bf_lo(rw[4 * i * (3 * D_) + col[0]]); c1 += bf_lo(rw[4 * i * (3 * D_) + col[1]]); }
                    t0[g] += c0; t1[g] += c1;
                }
            }
            q[0][kq] = (t0[0] + t0[1]) + (t0[2] + t0[3]); q[1][kq] = (t1[0] + t1[1]) + (t1[2] + t1[3]);
        }
        cs0 = q[0][0] + q[0][1]; cs1 = q[1][0] + q[1][1];
    }
    {
        unsigned m1, m2, m3; split3(-tv, m1, m2, m3);
        u32x4 tf = {m1 | (m1 << 16), m2 | (m1 << 16), m2 | (m3 << 16), m2 | (m3 << 16)};
        if (hh) tf = u32x4{0, 0, 0, 0};
        *(u32x4*)(lds + K1_TF + hw * 1024 + lane * 16) = tf;
        unsigned a1, a2, a3; split3(swap32_add(cs0), a1, a2, a3);
        *(u32x4*)(lds + K1_FF + (hw + 1) * 1024 + lane * 16) = u32x4{a1 | (a2 << 16), a1 | (a3 << 16), a2 | (a1 << 16), a3 | (a2 << 16)};
        if (hw == 0) {
            split3(swap32_add(cs1), a1, a2, a3);
            *(u32x4*)(lds + K1_FF + lane * 16) = u32x4{a1 | (a2 << 16), a1 | (a3 << 16), a2 | (a1 << 16), a3 | (a2 << 16)};
        }
        if (hh) ((float*)(lds + K1_RS))[32 * hw + r] = tv;
    }
    asm volatile("s_waitcnt lgkmcnt(0)\n\ts_barrier" ::: "memory");
}

struct K1Stat { u32x4 xr[5], wr[5]; };
__device__ __forceinline__ void k1_stat_load(K1Stat& sv, const __bf16* __restrict__ x, const __bf16* __restrict__ w, char* __restrict__ ws, int job) {
    const int tid = threadIdx.x, lane = tid & 63, wid = tid >> 6;
    const u32x4* xp = (const u32x4*)(x + (size_t)(job * 32 + wid * 4 + (lane >> 4)) * D_) + (lane & 15);
    const int jc = job < 60 ? job : 59;
    const u32x4* wp = (const u32x4*)(w + (size_t)((jc / 15) * 160 + wid * 20 + (lane >> 4)) * (3 * D_) + (jc % 15) * 128) + (lane & 15);
#pragma unroll
    for (int i = 0; i < 5; ++i) sv.xr[i] = xp[16 * i];
#pragma unroll
    for (int i = 0; i < 5; ++i) sv.wr[i] = wp[(size_t)i * 4 * (3 * D_ / 8)];
}
__device__ __forceinline__ float red16(float v) {
    v += __shfl_xor(v, 1); v += __shfl_xor(v, 2); v += __shfl_xor(v, 4); v += __shfl_xor(v, 8);
    return v;
}
__device__ __forceinline__ void k1_stat(const K1Stat& sv, char* __restrict__ ws, char* lds, int job) {
    const int tid = threadIdx.x, lane = tid & 63, wid = tid >> 6;
    float a = 0.f;
#pragma unroll
    for (int i = 0; i < 5; ++i)
#pragma unroll
        for (int j = 0; j < 4; ++j) a = dot2(sv.xr[i][j], ONES2, a);
    float mu = red16(a) * (1.f / D_);
    PIN(mu);
    float a0 = 0.f, a1 = 0.f;
#pragma unroll
    for (int i = 0; i < 5; ++i)
#pragma unroll
        for (int j = 0; j < 4; ++j) { const float d0 = bf_lo(sv.xr[i][j]) - mu, d1 = bf_hi(sv.xr[i][j]) - mu; a0 = __builtin_fmaf(d0, d0, a0); a1 = __builtin_fmaf(d1, d1, a1); }
    float var = red16(a0 + a1) * (1.f / D_);
    PIN(var);
    const int tok = job * 32 + wid * 4 + (lane >> 4), c = lane & 15;
    float pub = c ? __builtin_amdgcn_rsqf(var + EPS) : mu;
    PIN(pub);
    if (c < 2) st_agent((u64*)(ws + WS_RT) + tok * 2 + c, pub, now_ticks());
    if (c == 2) {
        const float sig2 = __builtin_sqrtf(var * (1.f + EPS) + EPS * EPS);
        *(f32x4*)(ws + WS_TOK + (size_t)tok * 16) = f32x4{mu, 1.0f / sig2, sig2, 0.f};
    }
    float cs[8];
#pragma unroll
    for (int j = 0; j < 4; ++j) {
        cs[2 * j] = bf_lo(sv.wr[0][j]); cs[2 * j + 1] = bf_hi(sv.wr[0][j]);
#pragma unroll
        for (int i = 1; i < 5; ++i) { cs[2 * j] += bf_lo(sv.wr[i][j]); cs[2 * j + 1] += bf_hi(sv.wr[i][j]); }
    }
    float* red = (float*)(lds + 86016);
    *(f32x4*)(red + (wid * 4 + (lane >> 4)) * 128 + c * 8) = f32x4{cs[0], cs[1], cs[2], cs[3]};
    *(f32x4*)(red + (wid * 4 + (lane >> 4)) * 128 + c * 8 + 4) = f32x4{cs[4], cs[5], cs[6], cs[7]};
    asm volatile("s_waitcnt lgkmcnt(0)\n\ts_barrier" ::: "memory");
    if (tid < 128 && job < 60) {
        float t[4] = {0.f, 0.f, 0.f, 0.f};
#pragma unroll
        for (int i = 0; i < 32; ++i) t[i & 3] += red[i * 128 + tid];
        st_agent((u64*)(ws + WS_RC) + (job / 15) * (3 * D_) + (job % 15) * 128 + tid, (t[0] + t[1]) + (t[2] + t[3]), now_ticks());
    }
}

template <int KIND>
__device__ __forceinline__ void k1_pack(const __bf16* __restrict__ wsrc, char* __restrict__ ws, char* lds, int strip, int third, const K1Stat& sv, int job) {
    constexpr int NI = KIND == 1 ? 8 : 10, NKS = KIND == 1 ? 4 : 5, ROWB = (KIND == 0 ? F_ : D_) * 2;
    const int tid = threadIdx.x, lane = tid & 63, r = lane & 31, hh = lane >> 5;
    const int wid = __builtin_amdgcn_readfirstlane(tid >> 6);
    const unsigned lds0 = (unsigned)(uintptr_t)lds + wid * 10240;
    const int row0 = third * 512 + wid * NI * 8;
    const char* src = (const char*)wsrc + (size_t)(row0 + (lane >> 3)) * ROWB + strip * 128 + (((lane & 7) ^ (((lane >> 4) & 1) * 4)) * 16);
#pragma unroll
    for (int i = 0; i < NI; ++i) glds16(src + (size_t)i * 8 * ROWB, lds0 + i * 1024);
    const int q4 = (lane & 15) >> 2, p4 = lane & 3, gi = (lane >> 4) & 1;
    int pc = 4 * gi + p4;
    if (KIND != 0) pc = (0x75643120u >> (4 * pc)) & 7;
    const lds_cptr sb = (lds_cptr)lds + wid * 10240;
    float cs[2] = {0.f, 0.f};
    k1_stat(sv, ws, lds, job);
    WAIT_VM(0);
#pragma unroll
    for (int ks = 0; ks < NKS; ++ks) {
        int ra, rb;
        if (KIND == 0) { ra = 16 * ks + 8 * hh; rb = ra + 4; }
        else if (KIND == 1) { ra = 16 * ks + 4 * hh; rb = ra + 8; }
        else if (ks < 4) { ra = 20 * ks + 4 * hh; rb = ra + 8; }
        else { ra = 40 * hh + 16; rb = ra + 20; }
#pragma unroll
        for (int f = 0; f < 2; ++f) {
            const int co = ((((f * 8 + pc) >> 1) ^ ((q4 >> 1) * 4)) * 16) + (pc & 1) * 8;
            const bf16x8 fr = cat8(lds_tr(sb + (ra + q4) * 128 + co), lds_tr(sb + (rb + q4) * 128 + co));
            const int ft = strip * 2 + f;
            size_t dst;
            if (KIND == 0) dst = WS_PW + ((size_t)(ft / 3) * 240 + (wid * 5 + ks) * 3 + ft % 3) * 1024;
            else if (KIND == 1) { const int g = third * 32 + wid * 4 + ks; const int f5 = ft % 5; dst = WS_PW + ((size_t)((g / 24) * 4 + ft / 5) * 240 + (f5 < 3 ? 120 + (g % 24) * 3 + f5 : 192 + (g % 24) * 2 + f5 - 3)) * 1024; }
            else dst = WS_PWO + ((size_t)ft * 40 + (ks < 4 ? wid * 4 + ks : 32 + wid)) * 1024;
            PST16(ws + dst + lane * 16, __builtin_bit_cast(u32x4, fr));
            if (KIND == 0) {
                const u32x4 a = __builtin_bit_cast(u32x4, fr);
#pragma unroll
                for (int i = 0; i < 4; ++i) cs[f] = dot2(a[i], ONES2, cs[f]);
            }
        }
    }
    if (KIND == 0) {
        float* red = (float*)(lds + 81920);
        red[(wid * 2 + hh) * 64 + r] = cs[0];
        red[(wid * 2 + hh) * 64 + 32 + r] = cs[1];
        __syncthreads();
        if (tid < 64) {
            float s = 0.f;
#pragma unroll
            for (int i = 0; i < 16; ++i) s += red[i * 64 + tid];
            ((float*)(ws + WS_CS1))[strip * 64 + tid] = s;
        }
    }
}

__global__ __launch_bounds__(768, 3) void k1_qkv(const __bf16* __restrict__ x, const __bf16* __restrict__ wqkv, const __bf16* __restrict__ wo, const __bf16* __restrict__ w1,
                                                 const __bf16* __restrict__ w2, char* __restrict__ ws) {
    extern __shared__ __attribute__((aligned(16))) char lds[];
    const int b = blockIdx.x, xcd = b & 7, idx = b >> 3;
    const int wid = __builtin_amdgcn_readfirstlane(threadIdx.x >> 6);
    if (idx >= 24) {
        if (wid >= 8) return;
        const int job = (idx - 24) * 8 + xcd;
        K1Stat sv;
        k1_stat_load(sv, x, wqkv, ws, job);
        if (job < 24) k1_pack<0>(w1, ws, lds, job, 0, sv, job);
        else if (job < 54) k1_pack<1>(w2, ws, lds, (job - 24) % 10, (job - 24) / 10, sv, job);
        else k1_pack<2>(wo, ws, lds, job - 54, 0, sv, job);
        STAMP(3, 0);
        return;
    }
    const int mt = (xcd >> 1) * 4 + (idx & 3), nt = (xcd & 1) * 6 + (idx >> 2);
    if (wid >= 8) { if (nt < 8) k1_helper<false>(x, wqkv, ws, lds, mt, nt, wid - 8); else k1_helper<true>(x, wqkv, ws, lds, mt, nt, wid - 8); return; }
    if (nt < 8) { if (wid >= 4) k1_body<false, 1>(ws, lds, mt, nt); else k1_body<false, 0>(ws, lds, mt, nt); }
    else { if (wid >= 4) k1_body<true, 1>(ws, lds, mt, nt); else k1_body<true, 0>(ws, lds, mt, nt); }
    STAMP(0, 5);
}

#define MLP_NB 20
#define AP_HI 2
#define AP_MID 1
#define AP_LO 0
#define GELU_PRIO 1
#define MLP_SLEEP 0
#define AW 7
#define MLP_PRIO 0
template <int N> __device__ __forceinline__ void wait_vm() { asm volatile("s_waitcnt vmcnt(%0)" :: "n"(N) : "memory"); }
#define WAIT_VMX(N) wait_vm<(N)>()
constexpr int AT_SLOT = 8 * KVT_B;
constexpr int K2_X = 4 * AT_SLOT, K2_H = K2_X + 40960, K2_CNT = K2_H + 24576, K2_B2 = K2_CNT + 128, K2_LDS = K2_B2 + 1280;
constexpr int C_FULL = 0, C_FREE = 64, C_X = 112, C_H = 116;

__device__ __forceinline__ void attn_slow(const char* __restrict__ kv, bf16x8 qf, s16x4 qt, f32x16& o, int r, int hh) {
    float m = -INFINITY;
    o = f32x16{};
    if (hh) qt = s16x4{0, 0, 0, 0};
    for (int t = 0; t < 64; ++t) {
        const char* p = kv + (size_t)t * KVT_B;
        const bf16x8 kf = *(const bf16x8*)(p + hh * 512 + r * 16);
        const s16x4 kt = *(const s16x4*)(p + 1024 + r * 8);
        const int rv = r < 20 ? r : 20;
        const bf16x8 v0 = *(const bf16x8*)(p + 1280 + hh * VROW_B + rv * 16);
        const bf16x8 v1 = *(const bf16x8*)(p + 1280 + 2 * VROW_B + hh * VROW_B + rv * 16);
        f32x16 s = mfma16(kf, qf, f32x16{});
        s = mfma8(kt, qt, s);
        float tm = s[0];
#pragma unroll
        for (int i = 1; i < 16; ++i) tm = __builtin_fmaxf(tm, s[i]);
        tm = swap32_max(tm);
        const float mn = __builtin_fmaxf(m, tm);
        const float al = __builtin_amdgcn_exp2f(m - mn);
        m = mn;
        u32x4 p0, p1;
#pragma unroll
        for (int i = 0; i < 4; ++i) {
            p0[i] = pk2(__builtin_amdgcn_exp2f(s[2 * i] - m), __builtin_amdgcn_exp2f(s[2 * i + 1] - m));
            p1[i] = pk2(__builtin_amdgcn_exp2f(s[8 + 2 * i] - m), __builtin_amdgcn_exp2f(s[8 + 2 * i + 1] - m));
        }
#pragma unroll
        for (int i = 0; i < 16; ++i) o[i] *= al;
        o = mfma16(v0, __builtin_bit_cast(bf16x8, p0), o);
        o = mfma16(v1, __builtin_bit_cast(bf16x8, p1), o);
    }
}

__device__ __forceinline__ void attn_finish(char* __restrict__ ws, const char* kvh, bf16x8 qf, s16x4 qt, f32x16 o, int qt_idx, int head, int lane) {
    const int r = lane & 31, hh = lane >> 5;
    float l = swap32_add(hh ? o[8] : 0.f);
    bool bad = !(l < 1e30f);
#pragma unroll
    for (int i = 0; i < 8; ++i) bad |= !(__builtin_fabsf(o[i]) < 3e38f);
#pragma unroll
    for (int i = 8; i < 12; ++i) bad |= (hh == 0) && !(__builtin_fabsf(o[i]) < 3e38f);
    if (__builtin_expect(__any(bad), 0)) {
        attn_slow(kvh, qf, qt, o, r, hh);
        l = swap32_add(hh ? o[8] : 0.f);
    }
    const float inv = 1.0f / l;
    char* ob = ws + WS_O + (size_t)qt_idx * 40 * 1024;
    *(u32x4*)(ob + head * 1024 + lane * 16) = u32x4{pk2(o[0] * inv, o[1] * inv), pk2(o[2] * inv, o[3] * inv), pk2(o[4] * inv, o[5] * inv), pk2(o[6] * inv, o[7] * inv)};
    if (hh == 0) *(u32x2*)(ob + (32 + (head >> 2)) * 1024 + (((head >> 1) & 1) * 32 + r) * 16 + (head & 1) * 8) = u32x2{pk2(o[8] * inv, o[9] * inv), pk2(o[10] * inv, o[11] * inv)};
}

__device__ __forceinline__ void k2_attn(char* __restrict__ ws, char* lds, int wid, int head, int qb) {
    const int lane = threadIdx.x & 63, r = lane & 31, hh = lane >> 5;
    const unsigned lds0 = (unsigned)(uintptr_t)lds, cnt = lds0 + K2_CNT;
    const int qt_idx = qb * 8 + wid;
    const char* kvh = ws + WS_KV + (size_t)head * 64 * KVT_B;
    const char* src = kvh + wid * KVT_B + lane * 16;
    auto tile_issue = [&](int c) {
        const unsigned base = lds0 + (c & 3) * AT_SLOT + wid * KVT_B;
        glds16(src + c * AT_SLOT, base);
        glds16(src + c * AT_SLOT + 1024, base + 1024);
        if (lane < (KVT_B - 2048) / 16) glds16(src + c * AT_SLOT + 2048, base + 2048);
    };
    tile_issue(0);
    {
        const char* qp = ws + WS_Q + ((size_t)head * 64 + qt_idx) * QT_B + lane * 16;
        glds16(qp, lds0 + K2_H + wid * QT_B);
        if (lane < 16) glds16(qp + 1024, lds0 + K2_H + wid * QT_B + 1024);
    }
    if (wid == 0 && lane < 32) *(__attribute__((address_space(3))) unsigned*)(uintptr_t)(cnt + lane * 4) = lane == 8 ? 8u : 0u;
    asm volatile("s_waitcnt lgkmcnt(0)\n\ts_barrier" ::: "memory");
    const int koff = hh * 512 + r * 16, ktoff = hh ? 1600 : 1024 + r * 8, voff = 1280 + hh * VROW_B + (r < 21 ? r : 21) * 16;
    WAIT_VM(0);
    const lds_cptr qp = (lds_cptr)lds + K2_H + wid * QT_B;
    bf16x8 qf = lds_r128(qp + koff);
    s16x4 qt = lds_r64(qp + 1024 + r * 8);
    asm volatile("s_waitcnt lgkmcnt(0)" : "+v"(qf), "+v"(qt) :: "memory");
    lds_signal(cnt + C_FULL);
    tile_issue(1); tile_issue(2); tile_issue(3);
    lds_await(cnt + C_FULL, lds_peek(cnt + C_FULL), 8);
    STAMPW(1, 1, AW);
    f32x16 o = f32x16{}, sA, sB;
    u32x4 pA0, pA1, pB0 = {0, 0, 0, 0}, pB1 = {0, 0, 0, 0};
    bf16x8 kf, v0, v1; s16x4 kt;
    {
        const lds_cptr sb = (lds_cptr)lds;
        kf = lds_r128(sb + koff); kt = lds_r64(sb + ktoff);
        if (hh) qt = s16x4{0, 0, 0, 0};
        sA = mfma16(kf, qf, f32x16{});
        sA = mfma8(kt, qt, sA);
        float m0 = sA[0];
#pragma unroll
        for (int i = 1; i < 16; ++i) m0 = __builtin_fmaxf(m0, sA[i]);
        m0 = swap32_max(m0);
        const unsigned nm = pk2(-m0, 0.f) & 0xffffu;
        if (hh) qt[0] = (short)nm;
#pragma unroll
        for (int i = 0; i < 16; ++i) sA[i] += bf_lo(nm);
        kf = lds_r128(sb + KVT_B + koff); kt = lds_r64(sb + KVT_B + ktoff);
        v0 = lds_r128(sb + voff); v1 = v0;
    }
#define EX(v) __builtin_amdgcn_exp2f(v)
#define GRP(MF, SC, i, PW, j) do { MF; SBAR(); const float e0 = EX(SC[i]), e1 = EX(SC[i + 1]), e2 = EX(SC[i + 2]), e3 = EX(SC[i + 3]); PW[j] = pk2(e0, e1); PW[j + 1] = pk2(e2, e3); PIN(PW); SBAR(); } while (0)
#define STEP(SC, SN, PC0, PC1, PP0, PP1, KP, VP) do { \
        GRP(SN = mfma16(kf, qf, f32x16{}), SC, 0, PC0, 0); \
        kf = lds_r128((KP) + koff); SBAR(); \
        GRP(o = mfma16(v0, __builtin_bit_cast(bf16x8, PP0), o), SC, 4, PC0, 2); \
        v0 = lds_r128((VP) + voff); SBAR(); \
        GRP(SN = mfma8(kt, qt, SN), SC, 8, PC1, 0); \
        kt = lds_r64((KP) + ktoff); SBAR(); \
        GRP(o = mfma16(v1, __builtin_bit_cast(bf16x8, PP1), o), SC, 12, PC1, 2); \
        v1 = lds_r128((VP) + voff + 2 * VROW_B); SBAR(); \
    } while (0)
    if (wid >= 4) __builtin_amdgcn_s_setprio(AP_MID);
    for (int c = 0; c < 8; ++c) {
        const lds_cptr sb = (lds_cptr)lds + (c & 3) * AT_SLOT;
        const lds_cptr nb = (lds_cptr)lds + ((c + 1) & 3) * AT_SLOT;
        if (wid < 4) __builtin_amdgcn_s_setprio(AP_HI);
        STEP(sA, sB, pA0, pA1, pB0, pB1, sb + 2 * KVT_B, sb + 0 * KVT_B);
        STEP(sB, sA, pB0, pB1, pA0, pA1, sb + 3 * KVT_B, sb + 1 * KVT_B);
        STEP(sA, sB, pA0, pA1, pB0, pB1, sb + 4 * KVT_B, sb + 2 * KVT_B);
        if (c == 0) { WAIT_VM(6); lds_signal(cnt + C_FULL + 4); }
        STEP(sB, sA, pB0, pB1, pA0, pA1, sb + 5 * KVT_B, sb + 3 * KVT_B);
        STEP(sA, sB, pA0, pA1, pB0, pB1, sb + 6 * KVT_B, sb + 4 * KVT_B);
        if (wid < 4) __builtin_amdgcn_s_setprio(AP_LO);
        const unsigned pk = lds_peek(cnt + C_FULL + 4 * (c + 1)), pf = lds_peek(cnt + C_FREE + 4 * c - 4);
        SBAR();
        STEP(sB, sA, pB0, pB1, pA0, pA1, sb + 7 * KVT_B, sb + 5 * KVT_B);
        lds_await(cnt + C_FULL + 4 * (c + 1), pk, 8);
        if (c <= 4) {
            if (c >= 1) { lds_await(cnt + C_FREE + 4 * c - 4, pf, 8); tile_issue(c + 3); }
            WAIT_VM(3);
        } else WAIT_VM(0);
        if (c <= 5) lds_signal(cnt + C_FULL + 4 * (c + 2));
        SBAR();
        STEP(sA, sB, pA0, pA1, pB0, pB1, nb + 0 * KVT_B, sb + 6 * KVT_B);
        STEP(sB, sA, pB0, pB1, pA0, pA1, nb + 1 * KVT_B, sb + 7 * KVT_B);
        lds_signal(cnt + C_FREE + 4 * c);
        STAMPW(1, 2 + c, AW);
    }
    o = mfma16(v0, __builtin_bit_cast(bf16x8, pB0), o);
    o = mfma16(v1, __builtin_bit_cast(bf16x8, pB1), o);
#undef STEP
#undef GRP
#undef EX
    attn_finish(ws, kvh, qf, qt, o, qt_idx, head, lane);
    STAMPW(1, 10, AW);
}

constexpr int mlp_cnt(int i, int nb) {
    int n = 239 - i < nb - 1 ? 239 - i : nb - 1;
    if (192 <= i && i < 192 + nb) n += 6;
    return n;
}
__device__ __forceinline__ f32x2 pk_mul(f32x2 a, f32x2 b) { return a * b; }
__device__ __forceinline__ f32x2 pk_fma(f32x2 a, f32x2 b, f32x2 c) { return __builtin_elementwise_fma(a, b, c); }

__device__ __forceinline__ void k2_mlp(const __bf16* __restrict__ x, const __bf16* __restrict__ b1, const __bf16* __restrict__ b2, char* ws, char* lds, int w, int head, int j, int tt) {
    constexpr int NB = MLP_NB;
    const int lane = threadIdx.x & 63, r = lane & 31, hh = lane >> 5;
    const unsigned lds0 = (unsigned)(uintptr_t)lds, cnt = lds0 + K2_CNT;
    asm volatile("s_barrier" ::: "memory");
    __builtin_amdgcn_s_setprio(MLP_PRIO);
    lds_await(cnt + C_FULL, lds_peek(cnt + C_FULL), 8);
    const int l16 = lane * 16;
    f32x4 ts; float csv[3]; unsigned b1v[3];
    {
        const char* tp = ws + WS_TOK + (size_t)tt * 512;
        const char* cp = ws + WS_CS1 + (j * 384 + w * 96) * 4;
        const char* bp = (const char*)b1 + (j * 384 + w * 96) * 2;
        asm volatile("global_load_dwordx4 %0, %1, %2" : "=v"(ts) : "v"(r * 16), "s"(tp));
        asm volatile("global_load_dword %0, %3, %4\n\tglobal_load_dword %1, %3, %4 offset:128\n\tglobal_load_dword %2, %3, %4 offset:256" : "=&v"(csv[0]), "=&v"(csv[1]), "=&v"(csv[2]) : "v"(r * 4), "s"(cp));
        asm volatile("global_load_ushort %0, %3, %4\n\tglobal_load_ushort %1, %3, %4 offset:64\n\tglobal_load_ushort %2, %3, %4 offset:128" : "=&v"(b1v[0]), "=&v"(b1v[1]), "=&v"(b1v[2]) : "v"(r * 2), "s"(bp));
    }
    {
        const int row = 8 * w + (lane >> 3);
        const char* xs = (const char*)x + (size_t)(tt * 32 + row) * (D_ * 2) + (((lane & 7) ^ ((row >> 1) & 7)) * 16);
#pragma unroll
        for (int st = 0; st < 10; ++st) glds16(xs + st * 128, lds0 + K2_X + st * 4096 + w * 1024);
        if (lane < 20) glds16((const char*)b2 + w * 320 + lane * 16, lds0 + K2_B2 + w * 320);
    }
    const uint64_t wbase = (uint64_t)(ws + WS_PW + (size_t)(j * 4 + w) * 240 * 1024);
    const u32x4 wr = {(unsigned)wbase, (unsigned)(wbase >> 32) & 0xffffu, 240u * 1024u, 0x00020000u};
    bf16x8 wb[NB];
#define ABL_LIM 240
#define WLD(i) do { if ((i) < NB) asm volatile("buffer_load_dwordx4 %0, %1, %2, %3 offen offset:%4" : "=v"(wb[(i) % NB]) : "v"(l16), "s"(wr), "s"(((i) >> 2) * 4096), "n"(((i) & 3) * 1024)); \
        else if ((i) < ABL_LIM) asm volatile("buffer_load_dwordx4 %0, %1, %2, %3 offen offset:%4" : "+v"(wb[(i) % NB]) : "v"(l16), "s"(wr), "s"(((i) >> 2) * 4096), "n"(((i) & 3) * 1024)); SBAR(); } while (0)
#define WWAIT(i) asm volatile("s_waitcnt vmcnt(%1)" : "+v"(wb[(i) % NB]) : "n"(mlp_cnt(i, NB)))
#define MMA(i, b, c) ({ if (MLP_SLEEP) __builtin_amdgcn_s_sleep(MLP_SLEEP); WWAIT(i); mfma16(wb[(i) % NB], b, c); })
#pragma unroll
    for (int i = 0; i < NB; ++i) WLD(i);
    asm volatile("s_waitcnt vmcnt(%7)" : "+v"(ts), "+v"(csv[0]), "+v"(csv[1]), "+v"(csv[2]), "+v"(b1v[0]), "+v"(b1v[1]), "+v"(b1v[2]) : "n"(NB) : "memory");
    lds_signal(cnt + C_X);
    lds_await(cnt + C_X, lds_peek(cnt + C_X), 4);
    STAMPW(2, 1, 8);
#define SERVICE(NVM, cf, ci) STAMPW(2, (cf) - 1, 8)
    const lds_cptr xb = (lds_cptr)lds + K2_X;
    unsigned xa[4];
#pragma unroll
    for (int k = 0; k < 4; ++k) { xa[k] = lds0 + K2_X + r * 128 + (((2 * k + hh) ^ ((r >> 1) & 7)) * 16); PIN(xa[k]); }
    f32x16 acc[3]; acc[0] = f32x16{}; acc[1] = f32x16{}; acc[2] = f32x16{};
    bf16x8 xf[2];
    xf[0] = lds_r128((lds_cptr)(uintptr_t)xa[0]);
#pragma unroll
    for (int ks = 0; ks < 40; ++ks) {
        if (ks == 12) SERVICE(24, 3, 4);
        if (ks == 26) SERVICE(24, 4, 5);
        if (ks + 1 < 40) xf[(ks + 1) & 1] = lds_r128((lds_cptr)(uintptr_t)xa[(ks + 1) & 3] + ((ks + 1) >> 2) * 4096);
        SBAR();
#pragma unroll
        for (int f = 0; f < 3; ++f) {
            acc[f] = MMA(3 * ks + f, xf[ks & 1], acc[f]);
            SBAR();
            WLD(3 * ks + f + NB);
        }
        SBAR();
    }
    SERVICE(24, 5, 6);
    __builtin_amdgcn_s_setprio(GELU_PRIO);
    {
        unsigned m1, m2, m3; split3(hh ? ts[2] : -ts[0], m1, m2, m3);
        u32x4 tf = {m1 | (m1 << 16), m2 | (m1 << 16), m2 | (m3 << 16), m2 | (m3 << 16)};
        if (hh) tf = u32x4{m1 | (m2 << 16), m3, 0, 0};
#pragma unroll
        for (int f = 0; f < 3; ++f) {
            unsigned a1, a2, a3; split3(csv[f], a1, a2, a3);
            u32x4 ff = {a1 | (a2 << 16), a1 | (a3 << 16), a2 | (a1 << 16), a3 | (a2 << 16)};
            if (hh) ff = u32x4{b1v[f] | (b1v[f] << 16), b1v[f], 0, 0};
            acc[f] = mfma16(__builtin_bit_cast(bf16x8, ff), __builtin_bit_cast(bf16x8, tf), acc[f]);
            SBAR();
        }
    }
    {
        constexpr float C0 = -2.0f * 0.7978845608028654f * 1.4426950408889634f, C1 = C0 * 0.044715f;
        const float s2 = ts[1], sg = ts[2];
        const f32x2 k0 = {C0 * s2, C0 * s2}, k1 = {C1 * s2 * s2 * s2, C1 * s2 * s2 * s2}, sg2 = {sg, sg};
        __attribute__((address_space(3))) u32x4* hp = (__attribute__((address_space(3))) u32x4*)(uintptr_t)(lds0 + K2_H + w * 6144 + lane * 16);
#pragma unroll
        for (int f = 0; f < 3; ++f) {
#pragma unroll
            for (int s = 0; s < 2; ++s) {
                u32x4 u;
#pragma unroll
                for (int i = 0; i < 4; ++i) {
                    const f32x2 a = {acc[f][8 * s + 2 * i], acc[f][8 * s + 2 * i + 1]};
                    const f32x2 z = pk_mul(a, pk_fma(pk_mul(a, a), k1, k0));
                    const f32x2 e = {__builtin_amdgcn_exp2f(z[0]), __builtin_amdgcn_exp2f(z[1])};
                    const f32x2 d = pk_fma(e, sg2, sg2);
                    const f32x2 hv = pk_mul(a, f32x2{__builtin_amdgcn_rcpf(d[0]), __builtin_amdgcn_rcpf(d[1])});
                    u[i] = pk2(hv[0], hv[1]);
                }
                hp[(2 * f + s) * 64] = u;
                SBAR();
            }
        }
    }
    asm volatile("s_waitcnt lgkmcnt(0)" ::: "memory");
    lds_signal(cnt + C_H);
    __builtin_amdgcn_s_setprio(MLP_PRIO);
    lds_await(cnt + C_H, lds_peek(cnt + C_H), 4);
    STAMPW(2, 8, 8);
    unsigned ha = lds0 + K2_H + lane * 16;
    PIN(ha);
    const lds_cptr hb = (lds_cptr)(uintptr_t)ha;
    char* pp = ws + WS_PART + (((size_t)j * 64 + tt) * 20 + w * 5) * 2048 + lane * 16;
    auto store_tile = [&](const f32x16& t, int f) {
#pragma unroll
        for (int c = 0; c < 2; ++c) {
            float a[8];
#pragma unroll
            for (int i = 0; i < 8; ++i) a[i] = t[8 * c + i];
            if (j == 0) {
                const int col = (w * 5 + f) * 32 + c * 16 + hh * 8;
                const u32x4 xv = __builtin_bit_cast(u32x4, lds_r128(xb + (col >> 6) * 4096 + r * 128 + ((((col >> 3) & 7) ^ ((r >> 1) & 7)) * 16)));
                const u32x4 bv = __builtin_bit_cast(u32x4, lds_r128((lds_cptr)lds + K2_B2 + col * 2));
#pragma unroll
                for (int i = 0; i < 4; ++i) { a[2 * i] += bf_lo(xv[i]) + bf_lo(bv[i]); a[2 * i + 1] += bf_hi(xv[i]) + bf_hi(bv[i]); }
            }
            st16(pp + (f * 2 + c) * 1024, u32x4{pk2(a[0], a[1]), pk2(a[2], a[3]), pk2(a[4], a[5]), pk2(a[6], a[7])});
            SBAR();
        }
    };
    acc[0] = f32x16{}; acc[1] = f32x16{}; acc[2] = f32x16{};
    xf[0] = lds_r128(hb);
#pragma unroll
    for (int ks = 0; ks < 24; ++ks) {
        if (ks == 10) SERVICE(24, 6, 7);
        xf[(ks + 1) & 1] = lds_r128(hb + ((ks + 1) % 24) * 1024);
        SBAR();
#pragma unroll
        for (int f = 0; f < 3; ++f) {
            acc[f] = MMA(120 + 3 * ks + f, xf[ks & 1], acc[f]);
            SBAR();
            WLD(120 + 3 * ks + f + NB);
        }
        SBAR();
    }
    SERVICE(24, 7, 8);
    store_tile(acc[0], 0); store_tile(acc[1], 1); store_tile(acc[2], 2);
    SBAR();
    f32x16 ac2[2]; ac2[0] = f32x16{}; ac2[1] = f32x16{};
#pragma unroll
    for (int ks = 0; ks < 24; ++ks) {
        if (ks + 1 < 24) xf[(ks + 1) & 1] = lds_r128(hb + (ks + 1) * 1024);
        SBAR();
#pragma unroll
        for (int f = 0; f < 2; ++f) {
            ac2[f] = MMA(192 + 2 * ks + f, xf[ks & 1], ac2[f]);
            SBAR();
            WLD(192 + 2 * ks + f + NB);
        }
        SBAR();
    }
    STAMPW(2, 9, 8);
#undef WLD
#undef SERVICE
    store_tile(ac2[0], 3); store_tile(ac2[1], 4);
    STAMPW(2, 10, 8);
}

__global__ __launch_bounds__(768, 3) void k2_main(const __bf16* __restrict__ x, const __bf16* __restrict__ b1, const __bf16* __restrict__ b2, char* ws) {
    extern __shared__ __attribute__((aligned(16))) char lds[];
    const int wid = __builtin_amdgcn_readfirstlane(threadIdx.x >> 6);
    const int b = blockIdx.x, xcd = b & 7, head = xcd * 4 + ((b >> 3) & 3), qb = b >> 5;
    STAMPW(1, 0, AW);
    STAMPW(2, 0, 8);
    if (wid < 8) k2_attn(ws, lds, wid, head, qb);
    else k2_mlp(x, b1, b2, ws, lds, wid - 8, head, xcd & 3, (xcd >> 2) * 32 + (b >> 3));
}

constexpr int K3_LDS = 40960 + 5 * 4096;

__global__ __launch_bounds__(640, 1) void k3_out(const char* ws, __bf16* __restrict__ out) {
    extern __shared__ __attribute__((aligned(16))) char lds[];
    const int tid = threadIdx.x, lane = tid & 63, r = lane & 31, hh = lane >> 5;
    const int wid = __builtin_amdgcn_readfirstlane(tid >> 6), f = wid % 5, kq = wid / 5;
    const int b = blockIdx.x, i = b >> 3, tt = (b & 7) * 8 + (i >> 2), ng = i & 3, ft = ng * 5 + f;
    const unsigned lds0 = (unsigned)(uintptr_t)lds;
    STAMP(4, 0);
    const char* op = ws + WS_O + ((size_t)tt * 40 + wid * 4) * 1024 + lane * 16;
#pragma unroll
    for (int k = 0; k < 4; ++k) glds16(op + k * 1024, lds0 + (wid * 4 + k) * 1024);
    const char* wp = ws + WS_PWO + (size_t)ft * 40 * 1024 + lane * 16;
    bf16x8 wf[20];
#pragma unroll
    for (int k = 0; k < 16; ++k) { wf[k] = *(const bf16x8*)(wp + (kq * 16 + k) * 1024); SBAR(); }
#pragma unroll
    for (int k = 0; k < 4; ++k) { wf[16 + k] = *(const bf16x8*)(wp + (32 + kq * 4 + k) * 1024); SBAR(); }
    u32x4 pv[4];
#pragma unroll
    for (int k = 0; k < 4; ++k) pv[k] = *(const u32x4*)(ws + WS_PART + (((size_t)k * 64 + tt) * 20 + ft) * 2048 + kq * 1024 + lane * 16);
    SBAR();
    WAIT_BAR(24);
    STAMP(4, 1);
    f32x16 acc = f32x16{};
    const lds_cptr ob = (lds_cptr)lds + lane * 16;
#pragma unroll
    for (int k = 0; k < 20; ++k) acc = mfma16(wf[k], lds_r128(ob + (k < 16 ? kq * 16 + k : 32 + kq * 4 + (k - 16)) * 1024), acc);
    STAMP(4, 2);
    f32x4* red = (f32x4*)(lds + 40960 + f * 4096);
    {
        const int c = 1 - kq;
        red[(2 * c) * 64 + lane] = kq ? f32x4{acc[0], acc[1], acc[2], acc[3]} : f32x4{acc[8], acc[9], acc[10], acc[11]};
        red[(2 * c + 1) * 64 + lane] = kq ? f32x4{acc[4], acc[5], acc[6], acc[7]} : f32x4{acc[12], acc[13], acc[14], acc[15]};
    }
    __syncthreads();
    {
        const f32x4 a = red[(2 * kq) * 64 + lane], bb = red[(2 * kq + 1) * 64 + lane];
        float m[8];
#pragma unroll
        for (int k = 0; k < 8; ++k) m[k] = kq ? acc[8 + k] : acc[k];
#pragma unroll
        for (int k = 0; k < 4; ++k) {
            m[2 * k] += (bf_lo(pv[0][k]) + bf_lo(pv[1][k])) + (bf_lo(pv[2][k]) + bf_lo(pv[3][k]));
            m[2 * k + 1] += (bf_hi(pv[0][k]) + bf_hi(pv[1][k])) + (bf_hi(pv[2][k]) + bf_hi(pv[3][k]));
        }
        u32x4 u;
        u[0] = pk2(a[0] + m[0], a[1] + m[1]); u[1] = pk2(a[2] + m[2], a[3] + m[3]);
        u[2] = pk2(bb[0] + m[4], bb[1] + m[5]); u[3] = pk2(bb[2] + m[6], bb[3] + m[7]);
        *(u32x4*)(out + (size_t)(tt * 32 + r) * D_ + ft * 32 + kq * 16 + hh * 8) = u;
    }
    STAMP(4, 3);
}

}

extern "C" void kernel_launch(void* const* d_in, const int* in_sizes, int n_in, void* d_out, int out_size, void* d_ws, size_t ws_size, hipStream_t stream) {
    const __bf16* x = (const __bf16*)d_in[0];
    const __bf16* wqkv = (const __bf16*)d_in[1];
    const __bf16* wo = (const __bf16*)d_in[2];
    const __bf16* w1 = (const __bf16*)d_in[4];
    const __bf16* b1 = (const __bf16*)d_in[5];
    const __bf16* w2 = (const __bf16*)d_in[6];
    const __bf16* b2 = (const __bf16*)d_in[7];
    char* ws = (char*)d_ws;
    (void)hipFuncSetAttribute((const void*)k1_qkv, hipFuncAttributeMaxDynamicSharedMemorySize, K1_LDS);
    (void)hipFuncSetAttribute((const void*)k2_main, hipFuncAttributeMaxDynamicSharedMemorySize, K2_LDS);
    (void)hipFuncSetAttribute((const void*)k3_out, hipFuncAttributeMaxDynamicSharedMemorySize, K3_LDS);
    k1_qkv<<<256, 768, K1_LDS, stream>>>(x, wqkv, wo, w1, w2, ws);
    k2_main<<<256, 768, K2_LDS, stream>>>(x, b1, b2, ws);
    k3_out<<<256, 640, K3_LDS, stream>>>(ws, (__bf16*)d_out);
}
```

```cpp
#include <hip/hip_runtime.h>
#include <hip/hip_bf16.h>
#include <stdint.h>

namespace {

typedef __attribute__((ext_vector_type(8))) __bf16 bf16x8;
typedef __attribute__((ext_vector_type(2))) __bf16 bf16x2;
typedef __attribute__((ext_vector_type(8))) short s16x8;
typedef __attribute__((ext_vector_type(4))) short s16x4;
typedef __attribute__((ext_vector_type(16))) float f32x16;
typedef __attribute__((ext_vector_type(4))) float f32x4;
typedef __attribute__((ext_vector_type(2))) float f32x2;
typedef __attribute__((ext_vector_type(4))) unsigned u32x4;
typedef __attribute__((ext_vector_type(2))) unsigned u32x2;
typedef __attribute__((address_space(3))) char* lds_ptr;
typedef __attribute__((address_space(3))) const char* lds_cptr;

constexpr int S_ = 2048, D_ = 640, H_ = 32, F_ = 1536;
constexpr float EPS = 1e-5f;
constexpr float QSCALE = 0.22360679774997896f * 1.4426950408889634f;

constexpr int QT_B = 1280;
constexpr int VROW_B = 352;
constexpr int KVT_B = 1280 + 4 * VROW_B;
constexpr size_t WS_Q = 0;
constexpr size_t WS_KV = WS_Q + (size_t)H_ * 64 * QT_B;
constexpr size_t WS_TOK = WS_KV + (size_t)H_ * 64 * KVT_B + 4096;
constexpr size_t WS_CS1 = WS_TOK + (size_t)S_ * 16;
constexpr size_t WS_PW = WS_CS1 + 8192;
constexpr size_t WS_PWO = WS_PW + (size_t)16 * 240 * 1024;
constexpr size_t WS_PART = WS_PWO + (size_t)20 * 40 * 1024;
constexpr size_t WS_O = WS_PART + (size_t)4 * 64 * 20 * 2048;
constexpr size_t WS_RT = WS_O + (size_t)64 * 40 * 1024;
constexpr size_t WS_RC = WS_RT + (size_t)S_ * 16;
constexpr size_t WS_CNT = (WS_RC + (size_t)4 * 3 * D_ * 8 + 255) / 256 * 256;
constexpr size_t WS_END = WS_CNT + 64 * 256;

#define STAMPW(kid, i, w)
#define STAMP(kid, i) STAMPW(kid, i, 0)
#define SBAR() __builtin_amdgcn_sched_barrier(0)
#define PIN(x) asm volatile("" : "+v"(x))
#define WAIT_BAR(N) asm volatile("s_waitcnt vmcnt(" #N ") lgkmcnt(0)\n\ts_barrier" ::: "memory")
#define WAIT_VM(N) asm volatile("s_waitcnt vmcnt(" #N ")" ::: "memory")

__device__ __forceinline__ f32x16 mfma16(bf16x8 a, bf16x8 b, f32x16 c) { return __builtin_amdgcn_mfma_f32_32x32x16_bf16(a, b, c, 0, 0, 0); }
__device__ __forceinline__ f32x16 mfma8(s16x4 a, s16x4 b, f32x16 c) { return __builtin_amdgcn_mfma_f32_32x32x8bf16_1k(a, b, c, 0, 0, 0); }
__device__ __forceinline__ unsigned pk2(float lo, float hi) { f32x2 v = {lo, hi}; return __builtin_bit_cast(unsigned, __builtin_convertvector(v, bf16x2)); }
__device__ __forceinline__ float bf_lo(unsigned u) { return __uint_as_float(u << 16); }
__device__ __forceinline__ float bf_hi(unsigned u) { return __uint_as_float(u & 0xffff0000u); }
__device__ __forceinline__ void glds16(const void* g, unsigned lds_base) {
    unsigned sv;
    asm volatile("s_mov_b32 %0, m0\n\ts_mov_b32 m0, %2\n\ts_nop 0\n\tglobal_load_lds_dwordx4 %1, off\n\ts_mov_b32 m0, %0" : "=&s"(sv) : "v"(g), "s"(lds_base) : "memory");
}
__device__ __forceinline__ void glds4(const void* g, unsigned lds_base) {
    unsigned sv;
    asm volatile("s_mov_b32 %0, m0\n\ts_mov_b32 m0, %2\n\ts_nop 0\n\tglobal_load_lds_dword %1, off\n\ts_mov_b32 m0, %0" : "=&s"(sv) : "v"(g), "s"(lds_base) : "memory");
}
__device__ __forceinline__ bf16x8 lds_r128(lds_cptr p) { return *(const __attribute__((address_space(3))) bf16x8*)p; }
__device__ __forceinline__ s16x4 lds_r64(lds_cptr p) { return *(const __attribute__((address_space(3))) s16x4*)p; }
__device__ __forceinline__ s16x4 lds_tr(lds_cptr p) { return __builtin_amdgcn_ds_read_tr16_b64_v4i16((__attribute__((address_space(3))) s16x4*)p); }
__device__ __forceinline__ bf16x8 cat8(s16x4 a, s16x4 b) { s16x8 v = {a[0], a[1], a[2], a[3], b[0], b[1], b[2], b[3]}; return __builtin_bit_cast(bf16x8, v); }
__device__ __forceinline__ float dot2(unsigned a, unsigned b, float c) { return __builtin_amdgcn_fdot2_f32_bf16(__builtin_bit_cast(bf16x2, a), __builtin_bit_cast(bf16x2, b), c, false); }
__device__ __forceinline__ float swap32_add(float v) { auto rr = __builtin_amdgcn_permlane32_swap(__float_as_uint(v), __float_as_uint(v), false, false); return __uint_as_float(rr[0]) + __uint_as_float(rr[1]); }
__device__ __forceinline__ float swap32_max(float v) { auto rr = __builtin_amdgcn_permlane32_swap(__float_as_uint(v), __float_as_uint(v), false, false); return __builtin_fmaxf(__uint_as_float(rr[0]), __uint_as_float(rr[1])); }
__device__ __forceinline__ void split3(float v, unsigned& a, unsigned& b, unsigned& c) {
    a = pk2(v, 0.f) & 0xffffu; float r = v - bf_lo(a);
    b = pk2(r, 0.f) & 0xffffu; r = r - bf_lo(b);
    c = pk2(r, 0.f) & 0xffffu;
}
__device__ __forceinline__ void st16(void* p, u32x4 v) {
    asm volatile("global_store_dwordx4 %0, %1, off sc1\n\ts_nop 1" :: "v"(p), "v"(v) : "memory");
}
__device__ __forceinline__ void st8(void* p, u32x2 v) { asm volatile("global_store_dwordx2 %0, %1, off sc1\n\ts_nop 1" :: "v"(p), "v"(v) : "memory"); }
#define QST16(p, v) (*(u32x4*)(p) = (v))
#define QST8(p, v) (*(u32x2*)(p) = (v))
#define PST16(p, v) st16((p), (v))
constexpr unsigned ONES2 = 0x3f803f80u;
typedef unsigned long long u64;
__device__ __forceinline__ u64 ld_agent(const u64* p) { return __hip_atomic_load(p, __ATOMIC_RELAXED, __HIP_MEMORY_SCOPE_AGENT); }
__device__ __forceinline__ void st_agent(u64* p, float v, unsigned tag) { __hip_atomic_store(p, (u64)__float_as_uint(v) | ((u64)tag << 32), __ATOMIC_RELAXED, __HIP_MEMORY_SCOPE_AGENT); }
__device__ __forceinline__ unsigned now_ticks() { return (unsigned)__builtin_amdgcn_s_memrealtime(); }
#define K1_SPIN 4096
__device__ __forceinline__ void lds_signal(unsigned addr, unsigned val = 1u) {
    asm volatile("s_mov_b64 exec, 1\n\tds_add_u32 %0, %1\n\ts_mov_b64 exec, -1" :: "v"(addr), "v"(val) : "memory");
}
__device__ __forceinline__ unsigned lds_peek(unsigned addr) { return *(const volatile __attribute__((address_space(3))) unsigned*)(uintptr_t)addr; }
__device__ __forceinline__ void lds_await(unsigned addr, unsigned first, unsigned target) {
    unsigned v = __builtin_amdgcn_readfirstlane(first);
    while (v < target) { __builtin_amdgcn_s_sleep(1); v = __builtin_amdgcn_readfirstlane(lds_peek(addr)); }
    asm volatile("" ::: "memory");
}

constexpr int K1_STAGE = 16384 + 20480, K1_AUX = 4 * K1_STAGE, K1_LDS = K1_AUX + 4096 + 5120 + 1024 + 256;

__device__ __forceinline__ constexpr unsigned k1_perm(int j) {
    return j == 0 ? 0x76453120u : (j == 3 ? 0x57064231u : (j == 4 ? 0x76254130u : 0x75643120u));
}
__device__ __forceinline__ int k1_piece_off(int P, int r, int& head) {
    head = P / 5; int rr = P - head * 5; const int h7 = head & 7;
    int role = rr;
    if (h7 == 1) role = (0x32410 >> (4 * rr)) & 7;
    if (h7 == 3) role = (0x32104 >> (4 * rr)) & 7;
    return role == 4 ? 1024 + r * 8 : (role >> 1) * 512 + r * 16 + (role & 1) * 8;
}

constexpr int K1_TF = K1_AUX, K1_FF = K1_AUX + 4096, K1_RS = K1_AUX + 9216;

template <bool VORI, int WN>
__device__ __forceinline__ void k1_body(char* __restrict__ ws, char* lds, int mt, int nt) {
    constexpr int NF = WN ? 2 : 3, F0 = WN ? 3 : 0;
    const int tid = threadIdx.x, lane = tid & 63, r = lane & 31, hh = lane >> 5;
    const int wid = __builtin_amdgcn_readfirstlane(tid >> 6), wm = wid & 3;
    const unsigned lds0 = (unsigned)(uintptr_t)lds;
    STAMP(0, 0);
    const int xrow = 32 * wm + r;
    const int q4 = (lane & 15) >> 2, p4 = lane & 3, gi = (lane >> 4) & 1;
    unsigned xa[4], wa[NF];
#pragma unroll
    for (int k = 0; k < 4; ++k) xa[k] = lds0 + xrow * 128 + (((2 * k + hh) ^ ((xrow >> 1) & 7)) * 16);
#pragma unroll
    for (int f = 0; f < NF; ++f) {
        int rel = 4 * gi + p4;
        if (!VORI) rel = (k1_perm(F0 + f) >> (4 * rel)) & 7;
        wa[f] = lds0 + 16384 + (8 * hh + q4) * 320 + (F0 + f) * 64 + rel * 8;
    }
    const int tt = mt * 4 + wm;
    const bool isq = nt < 4;
    char* const base = (!VORI && isq) ? ws + WS_Q + (size_t)tt * QT_B : ws + WS_KV + (size_t)tt * KVT_B;
    unsigned so[NF][3];
#pragma unroll
    for (int f = 0; f < NF; ++f) {
        if (!VORI) {
            const unsigned hstride = isq ? 64 * QT_B : 64 * KVT_B;
            const int T = (nt & 3) * 5 + F0 + f;
            const unsigned perm = k1_perm(F0 + f);
#pragma unroll
            for (int i = 0; i < 3; ++i) {
                int head;
                const int off = k1_piece_off(8 * T + ((perm >> (4 * ((i ? 2 + 2 * i : 0) + hh))) & 7), r, head);
                so[f][i] = head * hstride + off;
                PIN(so[f][i]);
            }
        } else {
            const int col = (nt - 8) * 160 + (F0 + f) * 32 + r;
            const int head = col / 20, d = col - head * 20;
            so[f][0] = head * (64 * KVT_B) + 1280 + hh * VROW_B + d * 16;
            so[f][1] = d;
            PIN(so[f][0]); PIN(so[f][1]);
        }
    }
    f32x16 acc[NF];
#pragma unroll
    for (int f = 0; f < NF; ++f) acc[f] = f32x16{};
    bf16x8 xf[4], wf[4][NF];
#define K1_RD(sl, ks) do { xf[ks] = lds_r128((lds_cptr)(uintptr_t)(xa[ks] + (sl))); \
        _Pragma("unroll") for (int f = 0; f < NF; ++f) { const lds_cptr p = (lds_cptr)(uintptr_t)(wa[f] + (sl)) + (ks) * 5120; wf[ks][f] = cat8(lds_tr(p), lds_tr(p + 1280)); } SBAR(); } while (0)
#define K1_MM(b) do { _Pragma("unroll") for (int f = 0; f < NF; ++f) acc[f] = VORI ? mfma16(xf[b], wf[b][f], acc[f]) : mfma16(wf[b][f], xf[b], acc[f]); SBAR(); } while (0)
    asm volatile("s_barrier" ::: "memory");
    STAMP(0, 1);
    __builtin_amdgcn_s_waitcnt(0xc07f);
    K1_RD(0u, 0); K1_RD(0u, 1);
#pragma unroll 1
    for (int st = 0; st < 10; ++st) {
        const unsigned sl = (st & 3) * K1_STAGE, sn = ((st + 1) & 3) * K1_STAGE;
        K1_RD(sl, 2); K1_MM(0);
        K1_RD(sl, 3); K1_MM(1);
        if (st < 9) asm volatile("s_waitcnt lgkmcnt(0)\n\ts_barrier" ::: "memory");
        K1_RD(sn, 0); K1_MM(2);
        K1_RD(sn, 1); K1_MM(3);
    }
#undef K1_RD
#undef K1_MM
    STAMP(0, 3);
    asm volatile("s_barrier" ::: "memory");
    const lds_cptr aux = (lds_cptr)lds;
    const bf16x8 tfb = lds_r128(aux + K1_TF + wm * 1024 + lane * 16);
    bf16x8 ffb[NF];
#pragma unroll
    for (int f = 0; f < NF; ++f) ffb[f] = lds_r128(aux + K1_FF + (F0 + f) * 1024 + lane * 16);
    const float* rsl = (const float*)(lds + K1_RS) + wm * 32;
    float rs[VORI ? 16 : 1];
    if (VORI) {
#pragma unroll
        for (int g = 0; g < 4; ++g) { const f32x4 t = *(const f32x4*)(rsl + 8 * g + 4 * hh); rs[4 * g] = t[0]; rs[4 * g + 1] = t[1]; rs[4 * g + 2] = t[2]; rs[4 * g + 3] = t[3]; }
    } else rs[0] = rsl[r] * (isq ? QSCALE : 1.f);
#pragma unroll
    for (int f = 0; f < NF; ++f) acc[f] = VORI ? mfma16(tfb, ffb[f], acc[f]) : mfma16(ffb[f], tfb, acc[f]);
    STAMP(0, 4);
#pragma unroll
    for (int f = 0; f < NF; ++f) {
        unsigned u[8];
#pragma unroll
        for (int i = 0; i < 8; ++i) u[i] = pk2(acc[f][2 * i] * rs[VORI ? 2 * i : 0], acc[f][2 * i + 1] * rs[VORI ? 2 * i + 1 : 0]);
        if (!VORI) {
            QST16(base + so[f][0], (u32x4{u[0], u[1], u[2], u[3]}));
            QST8(base + so[f][1], (u32x2{u[4], u[5]}));
            QST8(base + so[f][2], (u32x2{u[6], u[7]}));
        } else {
            char* p = base + so[f][0];
            QST16(p, (u32x4{u[0], u[1], u[2], u[3]}));
            QST16(p + 2 * VROW_B, (u32x4{u[4], u[5], u[6], u[7]}));
            if (so[f][1] == 19) {
                QST16(p + 16, (u32x4{ONES2, ONES2, ONES2, ONES2})); QST16(p + 2 * VROW_B + 16, (u32x4{ONES2, ONES2, ONES2, ONES2}));
                QST16(p + 32, (u32x4{0, 0, 0, 0})); QST16(p + 2 * VROW_B + 32, (u32x4{0, 0, 0, 0}));
            }
        }
    }
}

template <bool VORI>
__device__ __forceinline__ void k1_helper(const __bf16* __restrict__ x, const __bf16* __restrict__ w, char* __restrict__ ws, char* lds, int mt, int nt, int hw) {
    const int lane = threadIdx.x & 63, r = lane & 31, hh = lane >> 5;
    const unsigned lds0 = (unsigned)(uintptr_t)lds;
    const unsigned t_start = now_ticks();
    unsigned xo[4], wo[5];
#pragma unroll
    for (int i = 0; i < 4; ++i) {
        const int row = 8 * (4 * hw + i) + (lane >> 3);
        xo[i] = row * (D_ * 2) + (((lane & 7) ^ ((row >> 1) & 7)) * 16) - i * 1024;
    }
#pragma unroll
    for (int i = 0; i < 5; ++i) {
        const int idx = (5 * hw + i) * 64 + lane, krow = idx / 20, ch = idx - krow * 20;
        wo[i] = krow * (3 * D_ * 2) + ch * 16 - (i & 3) * 1024;
    }
    const char* xs = (const char*)x + (size_t)mt * 128 * (D_ * 2);
    const char* wsr = (const char*)w + nt * 320;
#define K1_M0(v) asm volatile("s_mov_b32 m0, %0\n\ts_nop 0" :: "s"(v) : "memory")
#define K1_DMA(vo, sp, imm) asm volatile("global_load_lds_dwordx4 %0, %1 offset:" #imm :: "v"(vo), "s"(sp) : "memory")
#define K1_PIECE(i, xp, wp, lb) do { \
        if ((i) == 0) { K1_M0((lb) + hw * 4096); K1_DMA(xo[0], xp, 0); } \
        if ((i) == 1) K1_DMA(xo[1], xp, 1024); \
        if ((i) == 2) K1_DMA(xo[2], xp, 2048); \
        if ((i) == 3) K1_DMA(xo[3], xp, 3072); \
        if ((i) == 4) { K1_M0((lb) + 16384 + hw * 5120); K1_DMA(wo[0], wp, 0); } \
        if ((i) == 5) K1_DMA(wo[1], wp, 1024); \
        if ((i) == 6) K1_DMA(wo[2], wp, 2048); \
        if ((i) == 7) K1_DMA(wo[3], wp, 3072); \
        if ((i) == 8) { K1_M0((lb) + 16384 + hw * 5120 + 4096); K1_DMA(wo[4], wp, 0); } \
        SBAR(); } while (0)
#pragma unroll 1
    for (int st = 0; st < 4; ++st) {
        const char* xp = xs + st * 128; const char* wp = wsr + (size_t)st * 64 * (3 * D_ * 2);
        const unsigned lb = lds0 + st * K1_STAGE;
#pragma unroll
        for (int i = 0; i < 9; ++i) K1_PIECE(i, xp, wp, lb);
    }
    WAIT_BAR(27);
#pragma unroll 1
    for (int st = 0; st < 7; ++st) {
        if (st) {
            const char* xp = xs + (st + 3) * 128; const char* wp = wsr + (size_t)(st + 3) * 64 * (3 * D_ * 2);
            const unsigned lb = lds0 + ((st + 3) & 3) * K1_STAGE;
#pragma unroll
            for (int i = 0; i < 9; ++i) K1_PIECE(i, xp, wp, lb);
        }
        WAIT_BAR(18);
    }
#undef K1_PIECE
#undef K1_DMA
#undef K1_M0
    const int tok = mt * 128 + 32 * hw + r;
    int col[2];
#pragma unroll
    for (int f = 0; f < 2; ++f) {
        const int t = f ? 0 : hw + 1;
        const int pc = VORI ? (r >> 2) : (int)((k1_perm(t) >> (4 * (r >> 2))) & 7);
        col[f] = nt * 160 + t * 32 + pc * 4 + (r & 3);
    }
    const u64* rt = (const u64*)(ws + WS_RT) + tok * 2 + hh;
    const u64* rc = (const u64*)(ws + WS_RC) + hh * (2 * 3 * D_);
    u64 rec[5];
#define K1_POLL() do { rec[0] = ld_agent(rt); rec[1] = ld_agent(rc + col[0]); rec[2] = ld_agent(rc + 3 * D_ + col[0]); rec[3] = ld_agent(rc + col[1]); rec[4] = ld_agent(rc + 3 * D_ + col[1]); } while (0)
    K1_POLL();
    WAIT_BAR(14);
    WAIT_BAR(5);
    bool ok = false;
#pragma unroll 1
    for (int it = 0; it < K1_SPIN; ++it) {
        WAIT_VM(0);
        const unsigned span = now_ticks() - t_start + 32u;
        bool good = true;
#pragma unroll
        for (int i = 0; i < 5; ++i) good = good && (unsigned)(rec[i] >> 32) - t_start <= span;
        if (__all(good)) { ok = true; break; }
        __builtin_amdgcn_s_sleep(8);
        K1_POLL();
    }
#undef K1_POLL
    float tv = __uint_as_float((unsigned)rec[0]);
    float cs0 = __uint_as_float((unsigned)rec[1]) + __uint_as_float((unsigned)rec[2]), cs1 = __uint_as_float((unsigned)rec[3]) + __uint_as_float((unsigned)rec[4]);
    if (__builtin_expect(!ok, 0)) {
        const u32x4* rowp = (const u32x4*)(x + (size_t)tok * D_) + hh * 8;
        float mu = 0.f;
#pragma unroll 1
        for (int pass = 0; pass < 2; ++pass) {
            float s1 = 0.f, s2 = 0.f, s3 = 0.f;
#pragma unroll 1
            for (int c = 0; c < 8; ++c) {
                float a0 = 0.f, a1 = 0.f;
#pragma unroll 1
                for (int i = 0; i < 5; ++i) {
                    const u32x4 u = rowp[c + 16 * i];
#pragma unroll
                    for (int j = 0; j < 4; ++j) {
                        if (pass == 0) a0 = dot2(u[j], ONES2, a0);
                        else { const float d0 = bf_lo(u[j]) - mu, d1 = bf_hi(u[j]) - mu; a0 = __builtin_fmaf(d0, d0, a0); a1 = __builtin_fmaf(d1, d1, a1); }
                    }
                }
                if (pass) a0 += a1;
                s1 = (c & 1) ? s1 + a0 : a0;
                if (c & 1) s2 = (c & 2) ? s2 + s1 : s1;
                if ((c & 3) == 3) s3 = (c & 4) ? s3 + s2 : s2;
            }
            float tot = swap32_add(s3) * (1.f / D_);
            PIN(tot);
            if (pass == 0) mu = tot; else tv = hh ? __builtin_amdgcn_rsqf(tot + EPS) : mu;
        }
        float q[2][2];
#pragma unroll
        for (int kq = 0; kq < 2; ++kq) {
            const unsigned short* wc = (const unsigned short*)w + (size_t)(2 * hh + kq) * 160 * (3 * D_);
            float t0[4] = {0.f, 0.f, 0.f, 0.f}, t1[4] = {0.f, 0.f, 0.f, 0.f};
#pragma unroll 1
            for (int wv = 0; wv < 8; ++wv) {
#pragma unroll
                for (int g = 0; g < 4; ++g) {
                    const unsigned short* rw = wc + (wv * 20 + g) * (3 * D_);
                    float c0 = bf_lo(rw[col[0]]), c1 = bf_lo(rw[col[1]]);
#pragma unroll
                    for (int i = 1; i < 5; ++i) { c0 += bf_lo(rw[4 * i * (3 * D_) + col[0]]); c1 += bf_lo(rw[4 * i * (3 * D_) + col[1]]); }
                    t0[g] += c0; t1[g] += c1;
                }
            }
            q[0][kq] = (t0[0] + t0[1]) + (t0[2] + t0[3]); q[1][kq] = (t1[0] + t1[1]) + (t1[2] + t1[3]);
        }
        cs0 = q[0][0] + q[0][1]; cs1 = q[1][0] + q[1][1];
    }
    {
        unsigned m1, m2, m3; split3(-tv, m1, m2, m3);
        u32x4 tf = {m1 | (m1 << 16), m2 | (m1 << 16), m2 | (m3 << 16), m2 | (m3 << 16)};
        if (hh) tf = u32x4{0, 0, 0, 0};
        *(u32x4*)(lds + K1_TF + hw * 1024 + lane * 16) = tf;
        unsigned a1, a2, a3; split3(swap32_add(cs0), a1, a2, a3);
        *(u32x4*)(lds + K1_FF + (hw + 1) * 1024 + lane * 16) = u32x4{a1 | (a2 << 16), a1 | (a3 << 16), a2 | (a1 << 16), a3 | (a2 << 16)};
        if (hw == 0) {
            split3(swap32_add(cs1), a1, a2, a3);
            *(u32x4*)(lds + K1_FF + lane * 16) = u32x4{a1 | (a2 << 16), a1 | (a3 << 16), a2 | (a1 << 16), a3 | (a2 << 16)};
        }
        if (hh) ((float*)(lds + K1_RS))[32 * hw + r] = tv;
    }
    asm volatile("s_waitcnt lgkmcnt(0)\n\ts_barrier" ::: "memory");
}

struct K1Stat { u32x4 xr[5], wr[5]; };
__device__ __forceinline__ void k1_stat_load(K1Stat& sv, const __bf16* __restrict__ x, const __bf16* __restrict__ w, char* __restrict__ ws, int job) {
    const int tid = threadIdx.x, lane = tid & 63, wid = tid >> 6;
    const u32x4* xp = (const u32x4*)(x + (size_t)(job * 32 + wid * 4 + (lane >> 4)) * D_) + (lane & 15);
    const int jc = job < 60 ? job : 59;
    const u32x4* wp = (const u32x4*)(w + (size_t)((jc / 15) * 160 + wid * 20 + (lane >> 4)) * (3 * D_) + (jc % 15) * 128) + (lane & 15);
#pragma unroll
    for (int i = 0; i < 5; ++i) sv.xr[i] = xp[16 * i];
#pragma unroll
    for (int i = 0; i < 5; ++i) sv.wr[i] = wp[(size_t)i * 4 * (3 * D_ / 8)];
}
__device__ __forceinline__ float red16(float v) {
    v += __shfl_xor(v, 1); v += __shfl_xor(v, 2); v += __shfl_xor(v, 4); v += __shfl_xor(v, 8);
    return v;
}
__device__ __forceinline__ void k1_stat(const K1Stat& sv, char* __restrict__ ws, char* lds, int job) {
    const int tid = threadIdx.x, lane = tid & 63, wid = tid >> 6;
    float a = 0.f;
#pragma unroll
    for (int i = 0; i < 5; ++i)
#pragma unroll
        for (int j = 0; j < 4; ++j) a = dot2(sv.xr[i][j], ONES2, a);
    float mu = red16(a) * (1.f / D_);
    PIN(mu);
    float a0 = 0.f, a1 = 0.f;
#pragma unroll
    for (int i = 0; i < 5; ++i)
#pragma unroll
        for (int j = 0; j < 4; ++j) { const float d0 = bf_lo(sv.xr[i][j]) - mu, d1 = bf_hi(sv.xr[i][j]) - mu; a0 = __builtin_fmaf(d0, d0, a0); a1 = __builtin_fmaf(d1, d1, a1); }
    float var = red16(a0 + a1) * (1.f / D_);
    PIN(var);
    const int tok = job * 32 + wid * 4 + (lane >> 4), c = lane & 15;
    float pub = c ? __builtin_amdgcn_rsqf(var + EPS) : mu;
    PIN(pub);
    if (c < 2) st_agent((u64*)(ws + WS_RT) + tok * 2 + c, pub, now_ticks());
    if (c == 2) {
        const float sig2 = __builtin_sqrtf(var * (1.f + EPS) + EPS * EPS);
        *(f32x4*)(ws + WS_TOK + (size_t)tok * 16) = f32x4{mu, 1.0f / sig2, sig2, 0.f};
    }
    float cs[8];
#pragma unroll
    for (int j = 0; j < 4; ++j) {
        cs[2 * j] = bf_lo(sv.wr[0][j]); cs[2 * j + 1] = bf_hi(sv.wr[0][j]);
#pragma unroll
        for (int i = 1; i < 5; ++i) { cs[2 * j] += bf_lo(sv.wr[i][j]); cs[2 * j + 1] += bf_hi(sv.wr[i][j]); }
    }
    float* red = (float*)(lds + 86016);
    *(f32x4*)(red + (wid * 4 + (lane >> 4)) * 128 + c * 8) = f32x4{cs[0], cs[1], cs[2], cs[3]};
    *(f32x4*)(red + (wid * 4 + (lane >> 4)) * 128 + c * 8 + 4) = f32x4{cs[4], cs[5], cs[6], cs[7]};
    asm volatile("s_waitcnt lgkmcnt(0)\n\ts_barrier" ::: "memory");
    if (tid < 128 && job < 60) {
        float t[4] = {0.f, 0.f, 0.f, 0.f};
#pragma unroll
        for (int i = 0; i < 32; ++i) t[i & 3] += red[i * 128 + tid];
        st_agent((u64*)(ws + WS_RC) + (job / 15) * (3 * D_) + (job % 15) * 128 + tid, (t[0] + t[1]) + (t[2] + t[3]), now_ticks());
    }
}

template <int KIND>
__device__ __forceinline__ void k1_pack(const __bf16* __restrict__ wsrc, char* __restrict__ ws, char* lds, int strip, int third, const K1Stat& sv, int job) {
    constexpr int NI = KIND == 1 ? 8 : 10, NKS = KIND == 1 ? 4 : 5, ROWB = (KIND == 0 ? F_ : D_) * 2;
    const int tid = threadIdx.x, lane = tid & 63, r = lane & 31, hh = lane >> 5;
    const int wid = __builtin_amdgcn_readfirstlane(tid >> 6);
    const unsigned lds0 = (unsigned)(uintptr_t)lds + wid * 10240;
    const int row0 = third * 512 + wid * NI * 8;
    const char* src = (const char*)wsrc + (size_t)(row0 + (lane >> 3)) * ROWB + strip * 128 + (((lane & 7) ^ (((lane >> 4) & 1) * 4)) * 16);
#pragma unroll
    for (int i = 0; i < NI; ++i) glds16(src + (size_t)i * 8 * ROWB, lds0 + i * 1024);
    const int q4 = (lane & 15) >> 2, p4 = lane & 3, gi = (lane >> 4) & 1;
    int pc = 4 * gi + p4;
    if (KIND != 0) pc = (0x75643120u >> (4 * pc)) & 7;
    const lds_cptr sb = (lds_cptr)lds + wid * 10240;
    float cs[2] = {0.f, 0.f};
    k1_stat(sv, ws, lds, job);
    WAIT_VM(0);
#pragma unroll
    for (int ks = 0; ks < NKS; ++ks) {
        int ra, rb;
        if (KIND == 0) { ra = 16 * ks + 8 * hh; rb = ra + 4; }
        else if (KIND == 1) { ra = 16 * ks + 4 * hh; rb = ra + 8; }
        else if (ks < 4) { ra = 20 * ks + 4 * hh; rb = ra + 8; }
        else { ra = 40 * hh + 16; rb = ra + 20; }
#pragma unroll
        for (int f = 0; f < 2; ++f) {
            const int co = ((((f * 8 + pc) >> 1) ^ ((q4 >> 1) * 4)) * 16) + (pc & 1) * 8;
            const bf16x8 fr = cat8(lds_tr(sb + (ra + q4) * 128 + co), lds_tr(sb + (rb + q4) * 128 + co));
            const int ft = strip * 2 + f;
            size_t dst;
            if (KIND == 0) dst = WS_PW + ((size_t)(ft / 3) * 240 + (wid * 5 + ks) * 3 + ft % 3) * 1024;
            else if (KIND == 1) { const int g = third * 32 + wid * 4 + ks; const int f5 = ft % 5; dst = WS_PW + ((size_t)((g / 24) * 4 + ft / 5) * 240 + (f5 < 3 ? 120 + (g % 24) * 3 + f5 : 192 + (g % 24) * 2 + f5 - 3)) * 1024; }
            else dst = WS_PWO + ((size_t)ft * 40 + (ks < 4 ? wid * 4 + ks : 32 + wid)) * 1024;
            PST16(ws + dst + lane * 16, __builtin_bit_cast(u32x4, fr));
            if (KIND == 0) {
                const u32x4 a = __builtin_bit_cast(u32x4, fr);
#pragma unroll
                for (int i = 0; i < 4; ++i) cs[f] = dot2(a[i], ONES2, cs[f]);
            }
        }
    }
    if (KIND == 0) {
        float* red = (float*)(lds + 81920);
        red[(wid * 2 + hh) * 64 + r] = cs[0];
        red[(wid * 2 + hh) * 64 + 32 + r] = cs[1];
        __syncthreads();
        if (tid < 64) {
            float s = 0.f;
#pragma unroll
            for (int i = 0; i < 16; ++i) s += red[i * 64 + tid];
            ((float*)(ws + WS_CS1))[strip * 64 + tid] = s;
        }
    }
}

__global__ __launch_bounds__(768, 3) void k1_qkv(const __bf16* __restrict__ x, const __bf16* __restrict__ wqkv, const __bf16* __restrict__ wo, const __bf16* __restrict__ w1,
                                                 const __bf16* __restrict__ w2, char* __restrict__ ws) {
    extern __shared__ __attribute__((aligned(16))) char lds[];
    const int b = blockIdx.x, xcd = b & 7, idx = b >> 3;
    const int wid = __builtin_amdgcn_readfirstlane(threadIdx.x >> 6);
    if (idx >= 24) {
        if (wid >= 8) return;
        const int job = (idx - 24) * 8 + xcd;
        if (threadIdx.x == 0) __hip_atomic_store((unsigned*)(ws + WS_CNT + job * 256), 0u, __ATOMIC_RELAXED, __HIP_MEMORY_SCOPE_AGENT);
        K1Stat sv;
        k1_stat_load(sv, x, wqkv, ws, job);
        if (job < 24) k1_pack<0>(w1, ws, lds, job, 0, sv, job);
        else if (job < 54) k1_pack<1>(w2, ws, lds, (job - 24) % 10, (job - 24) / 10, sv, job);
        else k1_pack<2>(wo, ws, lds, job - 54, 0, sv, job);
        STAMP(3, 0);
        return;
    }
    const int mt = (xcd >> 1) * 4 + (idx & 3), nt = (xcd & 1) * 6 + (idx >> 2);
    if (wid >= 8) { if (nt < 8) k1_helper<false>(x, wqkv, ws, lds, mt, nt, wid - 8); else k1_helper<true>(x, wqkv, ws, lds, mt, nt, wid - 8); return; }
    if (nt < 8) { if (wid >= 4) k1_body<false, 1>(ws, lds, mt, nt); else k1_body<false, 0>(ws, lds, mt, nt); }
    else { if (wid >= 4) k1_body<true, 1>(ws, lds, mt, nt); else k1_body<true, 0>(ws, lds, mt, nt); }
    STAMP(0, 5);
}

#define MLP_NB 20
#define AP_HI 2
#define AP_MID 1
#define AP_LO 0
#define GELU_PRIO 1
#define MLP_SLEEP 0
#define AW 7
#define MLP_PRIO 0
template <int N> __device__ __forceinline__ void wait_vm() { asm volatile("s_waitcnt vmcnt(%0)" :: "n"(N) : "memory"); }
#define WAIT_VMX(N) wait_vm<(N)>()
constexpr int AT_SLOT = 8 * KVT_B;
constexpr int K2_X = 4 * AT_SLOT, K2_H = K2_X + 40960, K2_CNT = K2_H + 24576, K2_LDS = K2_CNT + 128;
constexpr int C_FULL = 0, C_FREE = 64, C_X = 112, C_H = 116, C_MODE = 120;

__device__ __forceinline__ void attn_slow(const char* __restrict__ kv, bf16x8 qf, s16x4 qt, f32x16& o, int r, int hh) {
    float m = -INFINITY;
    o = f32x16{};
    if (hh) qt = s16x4{0, 0, 0, 0};
    for (int t = 0; t < 64; ++t) {
        const char* p = kv + (size_t)t * KVT_B;
        const bf16x8 kf = *(const bf16x8*)(p + hh * 512 + r * 16);
        const s16x4 kt = *(const s16x4*)(p + 1024 + r * 8);
        const int rv = r < 20 ? r : 20;
        const bf16x8 v0 = *(const bf16x8*)(p + 1280 + hh * VROW_B + rv * 16);
        const bf16x8 v1 = *(const bf16x8*)(p + 1280 + 2 * VROW_B + hh * VROW_B + rv * 16);
        f32x16 s = mfma16(kf, qf, f32x16{});
        s = mfma8(kt, qt, s);
        float tm = s[0];
#pragma unroll
        for (int i = 1; i < 16; ++i) tm = __builtin_fmaxf(tm, s[i]);
        tm = swap32_max(tm);
        const float mn = __builtin_fmaxf(m, tm);
        const float al = __builtin_amdgcn_exp2f(m - mn);
        m = mn;
        u32x4 p0, p1;
#pragma unroll
        for (int i = 0; i < 4; ++i) {
            p0[i] = pk2(__builtin_amdgcn_exp2f(s[2 * i] - m), __builtin_amdgcn_exp2f(s[2 * i + 1] - m));
            p1[i] = pk2(__builtin_amdgcn_exp2f(s[8 + 2 * i] - m), __builtin_amdgcn_exp2f(s[8 + 2 * i + 1] - m));
        }
#pragma unroll
        for (int i = 0; i < 16; ++i) o[i] *= al;
        o = mfma16(v0, __builtin_bit_cast(bf16x8, p0), o);
        o = mfma16(v1, __builtin_bit_cast(bf16x8, p1), o);
    }
}

__device__ __forceinline__ void attn_finish(char* __restrict__ ws, const char* kvh, bf16x8 qf, s16x4 qt, f32x16 o, int qt_idx, int head, int lane) {
    const int r = lane & 31, hh = lane >> 5;
    float l = swap32_add(hh ? o[8] : 0.f);
    bool bad = !(l < 1e30f);
#pragma unroll
    for (int i = 0; i < 8; ++i) bad |= !(__builtin_fabsf(o[i]) < 3e38f);
#pragma unroll
    for (int i = 8; i < 12; ++i) bad |= (hh == 0) && !(__builtin_fabsf(o[i]) < 3e38f);
    if (__builtin_expect(__any(bad), 0)) {
        attn_slow(kvh, qf, qt, o, r, hh);
        l = swap32_add(hh ? o[8] : 0.f);
    }
    const float inv = 1.0f / l;
    char* ob = ws + WS_O + (size_t)qt_idx * 40 * 1024;
    st16(ob + head * 1024 + lane * 16, u32x4{pk2(o[0] * inv, o[1] * inv), pk2(o[2] * inv, o[3] * inv), pk2(o[4] * inv, o[5] * inv), pk2(o[6] * inv, o[7] * inv)});
    if (hh == 0) st8(ob + (32 + (head >> 2)) * 1024 + (head & 3) * 256 + r * 8, u32x2{pk2(o[8] * inv, o[9] * inv), pk2(o[10] * inv, o[11] * inv)});
}

#define K2_SPIN 400000
#define WO_PRE 12
__device__ __forceinline__ int fin_tt() { return (blockIdx.x & 7) * 8 + (blockIdx.x >> 5); }
__device__ __forceinline__ int fin_ft(int f) { return ((blockIdx.x >> 3) & 3) * 5 + f; }
__device__ __forceinline__ int wo_frag(int kq, int k) { return k < 16 ? kq * 16 + k : 32 + kq * 4 + (k - 16); }
#define BAR() asm volatile("s_waitcnt lgkmcnt(0)\n\ts_barrier" ::: "memory")
__device__ __forceinline__ void k2_arrive(char* ws, int tile, unsigned inc) {
    WAIT_VM(0);
    if ((threadIdx.x & 63) == 0) __hip_atomic_fetch_add((unsigned*)(ws + WS_CNT + tile * 256), inc, __ATOMIC_RELAXED, __HIP_MEMORY_SCOPE_AGENT);
}
__device__ __forceinline__ unsigned k2_wait(const unsigned* c, unsigned want) {
    unsigned got = 3;
#pragma unroll 1
    for (int it = 0; it < K2_SPIN; ++it) {
        const unsigned v = __builtin_amdgcn_readfirstlane(__hip_atomic_load(c, __ATOMIC_RELAXED, __HIP_MEMORY_SCOPE_AGENT));
        const unsigned g = ((v & 0xffffu) >= 32u ? 1u : 0u) | ((v >> 16) >= 16u ? 2u : 0u);
        if (g & want) { got = g; break; }
        __builtin_amdgcn_s_sleep(1);
    }
    return got;
}
__device__ __forceinline__ void k2_poll(char* ws, unsigned mode_addr) {
    const unsigned* c = (const unsigned*)(ws + WS_CNT + fin_tt() * 256);
    const unsigned got = k2_wait(c, 3u);
    *(volatile __attribute__((address_space(3))) unsigned*)(uintptr_t)mode_addr = got;
    BAR();
    if (got == 2u) { k2_wait(c, 1u); BAR(); }
    if (got == 1u) { BAR(); k2_wait(c, 2u); BAR(); }
}
template <int MODE>
__device__ __forceinline__ void k2_tail_m(char* ws, char* lds, __bf16* __restrict__ out, int f, int kq, const bf16x8 (&wf)[20], u32x4 xv, u32x4 bv) {
    const int lane = threadIdx.x & 63, r = lane & 31, hh = lane >> 5;
    const int tt = fin_tt(), ft = fin_ft(f), g = kq * 5 + f;
    const __amdgpu_buffer_rsrc_t ro = __builtin_amdgcn_make_buffer_rsrc(ws + WS_O + ((size_t)tt * 40 + g * 4) * 1024, 0, 4096, 0x00020000);
    const __amdgpu_buffer_rsrc_t rp = __builtin_amdgcn_make_buffer_rsrc(ws + WS_PART + ((size_t)tt * 20 + ft) * 2048 + kq * 1024, 0, 3 * 64 * 20 * 2048 + 1024, 0x00020000);
    u32x4 pv[4], ov[4];
#define PLOAD() do { _Pragma("unroll") for (int k = 0; k < 4; ++k) pv[k] = __builtin_amdgcn_raw_buffer_load_b128(rp, lane * 16, k * (64 * 20 * 2048), 16); SBAR(); } while (0)
    if (MODE == 2) { PLOAD(); BAR(); }
    STAMP(4, 1);
#pragma unroll
    for (int k = 0; k < 4; ++k) ov[k] = __builtin_amdgcn_raw_buffer_load_b128(ro, lane * 16 + k * 1024, 0, 16);
    SBAR();
    if (MODE == 3) PLOAD();
#pragma unroll
    for (int k = 0; k < 4; ++k) *(u32x4*)(lds + K2_X + (g * 4 + k) * 1024 + lane * 16) = ov[k];
    BAR();
    STAMP(4, 2);
    f32x16 acc = f32x16{};
    const lds_cptr ob = (lds_cptr)lds + K2_X;
#pragma unroll
    for (int k = 0; k < 16; ++k) acc = mfma16(wf[k], lds_r128(ob + (kq * 16 + k) * 1024 + lane * 16), acc);
#pragma unroll
    for (int k = 0; k < 4; ++k) {
        const lds_cptr p = ob + (32 + kq * 4 + k) * 1024 + hh * 512 + r * 8;
        acc = mfma16(wf[16 + k], cat8(lds_r64(p), lds_r64(p + 256)), acc);
    }
    SBAR();
    STAMP(4, 3);
    f32x4* red = (f32x4*)(lds + K2_H + f * 4096);
    {
        const int c = 1 - kq;
        red[(2 * c) * 64 + lane] = kq ? f32x4{acc[0], acc[1], acc[2], acc[3]} : f32x4{acc[8], acc[9], acc[10], acc[11]};
        red[(2 * c + 1) * 64 + lane] = kq ? f32x4{acc[4], acc[5], acc[6], acc[7]} : f32x4{acc[12], acc[13], acc[14], acc[15]};
    }
    BAR();
    if (MODE == 1) PLOAD();
#undef PLOAD
    {
        const f32x4 a = red[(2 * kq) * 64 + lane], bb = red[(2 * kq + 1) * 64 + lane];
        float m[8];
#pragma unroll
        for (int k = 0; k < 8; ++k) m[k] = kq ? acc[8 + k] : acc[k];
#pragma unroll
        for (int k = 0; k < 4; ++k) {
            m[2 * k] += bf_lo(xv[k]) + bf_lo(bv[k]); m[2 * k + 1] += bf_hi(xv[k]) + bf_hi(bv[k]);
            m[2 * k] += (bf_lo(pv[0][k]) + bf_lo(pv[1][k])) + (bf_lo(pv[2][k]) + bf_lo(pv[3][k]));
            m[2 * k + 1] += (bf_hi(pv[0][k]) + bf_hi(pv[1][k])) + (bf_hi(pv[2][k]) + bf_hi(pv[3][k]));
        }
        u32x4 u;
        u[0] = pk2(a[0] + m[0], a[1] + m[1]); u[1] = pk2(a[2] + m[2], a[3] + m[3]);
        u[2] = pk2(bb[0] + m[4], bb[1] + m[5]); u[3] = pk2(bb[2] + m[6], bb[3] + m[7]);
        *(u32x4*)(out + (size_t)(tt * 32 + r) * D_ + ft * 32 + kq * 16 + hh * 8) = u;
    }
    STAMP(4, 4);
}
__device__ __forceinline__ void k2_tail(const __bf16* __restrict__ x, const __bf16* __restrict__ b2, char* ws, char* lds, __bf16* __restrict__ out, int f, int kq, const bf16x8 (&wf)[20]) {
    const int col = fin_ft(f) * 32 + kq * 16 + ((threadIdx.x >> 5) & 1) * 8;
    const u32x4 xv = *(const u32x4*)(x + (size_t)(fin_tt() * 32 + (threadIdx.x & 31)) * D_ + col), bv = *(const u32x4*)(b2 + col);
    SBAR();
    STAMP(4, 0);
    BAR();
    const unsigned mode = __builtin_amdgcn_readfirstlane(lds_peek((unsigned)(uintptr_t)lds + K2_CNT + C_MODE));
    if (mode == 3u) k2_tail_m<3>(ws, lds, out, f, kq, wf, xv, bv);
    else if (mode == 2u) k2_tail_m<2>(ws, lds, out, f, kq, wf, xv, bv);
    else k2_tail_m<1>(ws, lds, out, f, kq, wf, xv, bv);
}

__device__ __forceinline__ void k2_attn(const __bf16* __restrict__ x, const __bf16* __restrict__ b2, char* ws, char* lds, __bf16* __restrict__ out, int wid, int head, int qb) {
    const int lane = threadIdx.x & 63, r = lane & 31, hh = lane >> 5;
    const unsigned lds0 = (unsigned)(uintptr_t)lds, cnt = lds0 + K2_CNT;
    const int qt_idx = qb * 8 + wid;
    const char* kvh = ws + WS_KV + (size_t)head * 64 * KVT_B;
    const char* src = kvh + wid * KVT_B + lane * 16;
    auto tile_issue = [&](int c) {
        const unsigned base = lds0 + (c & 3) * AT_SLOT + wid * KVT_B;
        glds16(src + c * AT_SLOT, base);
        glds16(src + c * AT_SLOT + 1024, base + 1024);
        if (lane < (KVT_B - 2048) / 16) glds16(src + c * AT_SLOT + 2048, base + 2048);
    };
    tile_issue(0);
    {
        const char* qp = ws + WS_Q + ((size_t)head * 64 + qt_idx) * QT_B + lane * 16;
        glds16(qp, lds0 + K2_H + wid * QT_B);
        if (lane < 16) glds16(qp + 1024, lds0 + K2_H + wid * QT_B + 1024);
    }
    if (wid == 0 && lane < 32) *(__attribute__((address_space(3))) unsigned*)(uintptr_t)(cnt + lane * 4) = lane == 8 ? 8u : 0u;
    asm volatile("s_waitcnt lgkmcnt(0)\n\ts_barrier" ::: "memory");
    const int koff = hh * 512 + r * 16, ktoff = hh ? 1600 : 1024 + r * 8, voff = 1280 + hh * VROW_B + (r < 21 ? r : 21) * 16;
    WAIT_VM(0);
    const lds_cptr qp = (lds_cptr)lds + K2_H + wid * QT_B;
    bf16x8 qf = lds_r128(qp + koff);
    s16x4 qt = lds_r64(qp + 1024 + r * 8);
    asm volatile("s_waitcnt lgkmcnt(0)" : "+v"(qf), "+v"(qt) :: "memory");
    lds_signal(cnt + C_FULL);
    tile_issue(1); tile_issue(2); tile_issue(3);
    lds_await(cnt + C_FULL, lds_peek(cnt + C_FULL), 8);
    STAMPW(1, 1, AW);
    f32x16 o = f32x16{}, sA, sB;
    u32x4 pA0, pA1, pB0 = {0, 0, 0, 0}, pB1 = {0, 0, 0, 0};
    bf16x8 kf, v0, v1; s16x4 kt;
    {
        const lds_cptr sb = (lds_cptr)lds;
        kf = lds_r128(sb + koff); kt = lds_r64(sb + ktoff);
        if (hh) qt = s16x4{0, 0, 0, 0};
        sA = mfma16(kf, qf, f32x16{});
        sA = mfma8(kt, qt, sA);
        float m0 = sA[0];
#pragma unroll
        for (int i = 1; i < 16; ++i) m0 = __builtin_fmaxf(m0, sA[i]);
        m0 = swap32_max(m0);
        const unsigned nm = pk2(-m0, 0.f) & 0xffffu;
        if (hh) qt[0] = (short)nm;
#pragma unroll
        for (int i = 0; i < 16; ++i) sA[i] += bf_lo(nm);
        kf = lds_r128(sb + KVT_B + koff); kt = lds_r64(sb + KVT_B + ktoff);
        v0 = lds_r128(sb + voff); v1 = v0;
    }
#define EX(v) __builtin_amdgcn_exp2f(v)
#define GRP(MF, SC, i, PW, j) do { MF; SBAR(); const float e0 = EX(SC[i]), e1 = EX(SC[i + 1]), e2 = EX(SC[i + 2]), e3 = EX(SC[i + 3]); PW[j] = pk2(e0, e1); PW[j + 1] = pk2(e2, e3); PIN(PW); SBAR(); } while (0)
#define STEP(SC, SN, PC0, PC1, PP0, PP1, KP, VP) do { \
        GRP(SN = mfma16(kf, qf, f32x16{}), SC, 0, PC0, 0); \
        kf = lds_r128((KP) + koff); SBAR(); \
        GRP(o = mfma16(v0, __builtin_bit_cast(bf16x8, PP0), o), SC, 4, PC0, 2); \
        v0 = lds_r128((VP) + voff); SBAR(); \
        GRP(SN = mfma8(kt, qt, SN), SC, 8, PC1, 0); \
        kt = lds_r64((KP) + ktoff); SBAR(); \
        GRP(o = mfma16(v1, __builtin_bit_cast(bf16x8, PP1), o), SC, 12, PC1, 2); \
        v1 = lds_r128((VP) + voff + 2 * VROW_B); SBAR(); \
    } while (0)
    if (wid >= 4) __builtin_amdgcn_s_setprio(AP_MID);
    for (int c = 0; c < 7; ++c) {
        const lds_cptr sb = (lds_cptr)lds + (c & 3) * AT_SLOT;
        const lds_cptr nb = (lds_cptr)lds + ((c + 1) & 3) * AT_SLOT;
        if (wid < 4) __builtin_amdgcn_s_setprio(AP_HI);
        STEP(sA, sB, pA0, pA1, pB0, pB1, sb + 2 * KVT_B, sb + 0 * KVT_B);
        STEP(sB, sA, pB0, pB1, pA0, pA1, sb + 3 * KVT_B, sb + 1 * KVT_B);
        STEP(sA, sB, pA0, pA1, pB0, pB1, sb + 4 * KVT_B, sb + 2 * KVT_B);
        if (c == 0) { WAIT_VM(6); lds_signal(cnt + C_FULL + 4); }
        STEP(sB, sA, pB0, pB1, pA0, pA1, sb + 5 * KVT_B, sb + 3 * KVT_B);
        STEP(sA, sB, pA0, pA1, pB0, pB1, sb + 6 * KVT_B, sb + 4 * KVT_B);
        if (wid < 4) __builtin_amdgcn_s_setprio(AP_LO);
        const unsigned pk = lds_peek(cnt + C_FULL + 4 * (c + 1)), pf = lds_peek(cnt + C_FREE + 4 * c - 4);
        SBAR();
        STEP(sB, sA, pB0, pB1, pA0, pA1, sb + 7 * KVT_B, sb + 5 * KVT_B);
        lds_await(cnt + C_FULL + 4 * (c + 1), pk, 8);
        if (c <= 4) {
            if (c >= 1) { lds_await(cnt + C_FREE + 4 * c - 4, pf, 8); tile_issue(c + 3); }
            WAIT_VM(3);
        } else WAIT_VM(0);
        if (c <= 5) lds_signal(cnt + C_FULL + 4 * (c + 2));
        SBAR();
        STEP(sA, sB, pA0, pA1, pB0, pB1, nb + 0 * KVT_B, sb + 6 * KVT_B);
        STEP(sB, sA, pB0, pB1, pA0, pA1, nb + 1 * KVT_B, sb + 7 * KVT_B);
        lds_signal(cnt + C_FREE + 4 * c);
        STAMPW(1, 2 + c, AW);
    }
    const int tf = 2 + (wid >> 1), tkq = wid & 1;
    const char* wop = ws + WS_PWO + (size_t)fin_ft(tf < 5 ? tf : 4) * 40 * 1024 + lane * 16;
    bf16x8 wf[20];
#pragma unroll
    for (int k = 0; k < WO_PRE; ++k) { wf[k] = *(const bf16x8*)(wop + wo_frag(tkq, k) * 1024); SBAR(); }
    {
        const lds_cptr sb = (lds_cptr)lds + 3 * AT_SLOT;
        if (wid < 4) __builtin_amdgcn_s_setprio(AP_HI);
        STEP(sA, sB, pA0, pA1, pB0, pB1, sb + 2 * KVT_B, sb + 0 * KVT_B);
        STEP(sB, sA, pB0, pB1, pA0, pA1, sb + 3 * KVT_B, sb + 1 * KVT_B);
        STEP(sA, sB, pA0, pA1, pB0, pB1, sb + 4 * KVT_B, sb + 2 * KVT_B);
        STEP(sB, sA, pB0, pB1, pA0, pA1, sb + 5 * KVT_B, sb + 3 * KVT_B);
        STEP(sA, sB, pA0, pA1, pB0, pB1, sb + 6 * KVT_B, sb + 4 * KVT_B);
        if (wid < 4) __builtin_amdgcn_s_setprio(AP_LO);
        STEP(sB, sA, pB0, pB1, pA0, pA1, sb + 7 * KVT_B, sb + 5 * KVT_B);
        STEP(sA, sB, pA0, pA1, pB0, pB1, sb + 7 * KVT_B, sb + 6 * KVT_B);
        STEP(sB, sA, pB0, pB1, pA0, pA1, sb + 7 * KVT_B, sb + 7 * KVT_B);
        STAMPW(1, 9, AW);
    }
    o = mfma16(v0, __builtin_bit_cast(bf16x8, pB0), o);
    o = mfma16(v1, __builtin_bit_cast(bf16x8, pB1), o);
#undef STEP
#undef GRP
#undef EX
    attn_finish(ws, kvh, qf, qt, o, qt_idx, head, lane);
    k2_arrive(ws, qt_idx, 1u);
    STAMPW(1, 10, AW);
    __builtin_amdgcn_s_setprio(0);
    if (wid >= 6) { if (wid == 7) k2_poll(ws, cnt + C_MODE); return; }
    SBAR();
#pragma unroll
    for (int k = WO_PRE; k < 20; ++k) { wf[k] = *(const bf16x8*)(wop + wo_frag(tkq, k) * 1024); SBAR(); }
    k2_tail(x, b2, ws, lds, out, tf, tkq, wf);
}

constexpr int mlp_cnt(int i, int nb) {
    int n = nb - 1;
    if (192 <= i && i < 192 + nb) n += 6;
    return n;
}
__device__ __forceinline__ f32x2 pk_mul(f32x2 a, f32x2 b) { return a * b; }
__device__ __forceinline__ f32x2 pk_fma(f32x2 a, f32x2 b, f32x2 c) { return __builtin_elementwise_fma(a, b, c); }

__device__ __forceinline__ void k2_mlp(const __bf16* __restrict__ x, const __bf16* __restrict__ b1, const __bf16* __restrict__ b2, char* ws, char* lds, __bf16* __restrict__ out, int w, int j, int tt) {
    constexpr int NB = 20;
    const int lane = threadIdx.x & 63, r = lane & 31, hh = lane >> 5;
    const unsigned lds0 = (unsigned)(uintptr_t)lds, cnt = lds0 + K2_CNT;
    asm volatile("s_barrier" ::: "memory");
    __builtin_amdgcn_s_setprio(MLP_PRIO);
    lds_await(cnt + C_FULL, lds_peek(cnt + C_FULL), 8);
    const int l16 = lane * 16;
    f32x4 ts; float csv[3]; unsigned b1v[3];
    {
        const char* tp = ws + WS_TOK + (size_t)tt * 512;
        const char* cp = ws + WS_CS1 + (j * 384 + w * 96) * 4;
        const char* bp = (const char*)b1 + (j * 384 + w * 96) * 2;
        asm volatile("global_load_dwordx4 %0, %1, %2" : "=v"(ts) : "v"(r * 16), "s"(tp));
        asm volatile("global_load_dword %0, %3, %4\n\tglobal_load_dword %1, %3, %4 offset:128\n\tglobal_load_dword %2, %3, %4 offset:256" : "=&v"(csv[0]), "=&v"(csv[1]), "=&v"(csv[2]) : "v"(r * 4), "s"(cp));
        asm volatile("global_load_ushort %0, %3, %4\n\tglobal_load_ushort %1, %3, %4 offset:64\n\tglobal_load_ushort %2, %3, %4 offset:128" : "=&v"(b1v[0]), "=&v"(b1v[1]), "=&v"(b1v[2]) : "v"(r * 2), "s"(bp));
    }
    {
        const int row = 8 * w + (lane >> 3);
        const char* xs = (const char*)x + (size_t)(tt * 32 + row) * (D_ * 2) + (((lane & 7) ^ ((row >> 1) & 7)) * 16);
#pragma unroll
        for (int st = 0; st < 10; ++st) glds16(xs + st * 128, lds0 + K2_X + st * 4096 + w * 1024);
    }
    const uint64_t wbase = (uint64_t)(ws + WS_PW + (size_t)(j * 4 + w) * 240 * 1024);
    const u32x4 wr = {(unsigned)wbase, (unsigned)(wbase >> 32) & 0xffffu, 240u * 1024u, 0x00020000u};
    const uint64_t obase = (uint64_t)(ws + WS_PWO + (size_t)fin_ft(w >> 1) * 40 * 1024);
    const u32x4 wr2 = {(unsigned)obase, (unsigned)(obase >> 32) & 0xffffu, 40u * 1024u, 0x00020000u};
    const int okq = w & 1;
    bf16x8 wb[NB];
#define ABL_LIM 240
#define WLD(i) do { if ((i) < NB) asm volatile("buffer_load_dwordx4 %0, %1, %2, %3 offen offset:%4" : "=v"(wb[(i) % NB]) : "v"(l16), "s"(wr), "s"(((i) >> 2) * 4096), "n"(((i) & 3) * 1024)); \
        else if ((i) < ABL_LIM) asm volatile("buffer_load_dwordx4 %0, %1, %2, %3 offen offset:%4" : "+v"(wb[(i) % NB]) : "v"(l16), "s"(wr), "s"(((i) >> 2) * 4096), "n"(((i) & 3) * 1024)); \
        else if ((i) >= 240) asm volatile("buffer_load_dwordx4 %0, %1, %2, %3 offen offset:%4" : "+v"(wb[(i) % NB]) : "v"(l16), "s"(wr2), "s"(wo_frag(okq, ((i) - 240) & ~3) * 1024), "n"(((i) & 3) * 1024)); \
        SBAR(); } while (0)
#define WWAIT(i) asm volatile("s_waitcnt vmcnt(%1)" : "+v"(wb[(i) % NB]) : "n"(mlp_cnt(i, NB)))
#define MMA(i, b, c) ({ if (MLP_SLEEP) __builtin_amdgcn_s_sleep(MLP_SLEEP); WWAIT(i); mfma16(wb[(i) % NB], b, c); })
#pragma unroll
    for (int i = 0; i < NB; ++i) WLD(i);
    asm volatile("s_waitcnt vmcnt(%7)" : "+v"(ts), "+v"(csv[0]), "+v"(csv[1]), "+v"(csv[2]), "+v"(b1v[0]), "+v"(b1v[1]), "+v"(b1v[2]) : "n"(NB) : "memory");
    lds_signal(cnt + C_X);
    lds_await(cnt + C_X, lds_peek(cnt + C_X), 4);
    STAMPW(2, 1, 8);
#define SERVICE(NVM, cf, ci) STAMPW(2, (cf) - 1, 8)
    unsigned xa[4];
#pragma unroll
    for (int k = 0; k < 4; ++k) { xa[k] = lds0 + K2_X + r * 128 + (((2 * k + hh) ^ ((r >> 1) & 7)) * 16); PIN(xa[k]); }
    f32x16 acc[3]; acc[0] = f32x16{}; acc[1] = f32x16{}; acc[2] = f32x16{};
    bf16x8 xf[2];
    xf[0] = lds_r128((lds_cptr)(uintptr_t)xa[0]);
#pragma unroll
    for (int ks = 0; ks < 40; ++ks) {
        if (ks == 12) SERVICE(24, 3, 4);
        if (ks == 26) SERVICE(24, 4, 5);
        if (ks + 1 < 40) xf[(ks + 1) & 1] = lds_r128((lds_cptr)(uintptr_t)xa[(ks + 1) & 3] + ((ks + 1) >> 2) * 4096);
        SBAR();
#pragma unroll
        for (int f = 0; f < 3; ++f) {
            acc[f] = MMA(3 * ks + f, xf[ks & 1], acc[f]);
            SBAR();
            WLD(3 * ks + f + NB);
        }
        SBAR();
    }
    SERVICE(24, 5, 6);
    __builtin_amdgcn_s_setprio(GELU_PRIO);
    {
        unsigned m1, m2, m3; split3(hh ? ts[2] : -ts[0], m1, m2, m3);
        u32x4 tf = {m1 | (m1 << 16), m2 | (m1 << 16), m2 | (m3 << 16), m2 | (m3 << 16)};
        if (hh) tf = u32x4{m1 | (m2 << 16), m3, 0, 0};
#pragma unroll
        for (int f = 0; f < 3; ++f) {
            unsigned a1, a2, a3; split3(csv[f], a1, a2, a3);
            u32x4 ff = {a1 | (a2 << 16), a1 | (a3 << 16), a2 | (a1 << 16), a3 | (a2 << 16)};
            if (hh) ff = u32x4{b1v[f] | (b1v[f] << 16), b1v[f], 0, 0};
            acc[f] = mfma16(__builtin_bit_cast(bf16x8, ff), __builtin_bit_cast(bf16x8, tf), acc[f]);
            SBAR();
        }
    }
    {
        constexpr float C0 = -2.0f * 0.7978845608028654f * 1.4426950408889634f, C1 = C0 * 0.044715f;
        const float s2 = ts[1], sg = ts[2];
        const f32x2 k0 = {C0 * s2, C0 * s2}, k1 = {C1 * s2 * s2 * s2, C1 * s2 * s2 * s2}, sg2 = {sg, sg};
        __attribute__((address_space(3))) u32x4* hp = (__attribute__((address_space(3))) u32x4*)(uintptr_t)(lds0 + K2_H + w * 6144 + lane * 16);
#pragma unroll
        for (int f = 0; f < 3; ++f) {
#pragma unroll
            for (int s = 0; s < 2; ++s) {
                u32x4 u;
#pragma unroll
                for (int i = 0; i < 4; ++i) {
                    const f32x2 a = {acc[f][8 * s + 2 * i], acc[f][8 * s + 2 * i + 1]};
                    const f32x2 z = pk_mul(a, pk_fma(pk_mul(a, a), k1, k0));
                    const f32x2 e = {__builtin_amdgcn_exp2f(z[0]), __builtin_amdgcn_exp2f(z[1])};
                    const f32x2 d = pk_fma(e, sg2, sg2);
                    const f32x2 hv = pk_mul(a, f32x2{__builtin_amdgcn_rcpf(d[0]), __builtin_amdgcn_rcpf(d[1])});
                    u[i] = pk2(hv[0], hv[1]);
                }
                hp[(2 * f + s) * 64] = u;
                SBAR();
            }
        }
    }
    asm volatile("s_waitcnt lgkmcnt(0)" ::: "memory");
    lds_signal(cnt + C_H);
    __builtin_amdgcn_s_setprio(MLP_PRIO);
    lds_await(cnt + C_H, lds_peek(cnt + C_H), 4);
    STAMPW(2, 8, 8);
    unsigned ha = lds0 + K2_H + lane * 16;
    PIN(ha);
    const lds_cptr hb = (lds_cptr)(uintptr_t)ha;
    char* pp = ws + WS_PART + (((size_t)j * 64 + tt) * 20 + w * 5) * 2048 + lane * 16;
    auto store_tile = [&](const f32x16& t, int f) {
#pragma unroll
        for (int c = 0; c < 2; ++c) {
            float a[8];
#pragma unroll
            for (int i = 0; i < 8; ++i) a[i] = t[8 * c + i];
            st16(pp + (f * 2 + c) * 1024, u32x4{pk2(a[0], a[1]), pk2(a[2], a[3]), pk2(a[4], a[5]), pk2(a[6], a[7])});
            SBAR();
        }
    };
    acc[0] = f32x16{}; acc[1] = f32x16{}; acc[2] = f32x16{};
    xf[0] = lds_r128(hb);
#pragma unroll
    for (int ks = 0; ks < 24; ++ks) {
        if (ks == 10) SERVICE(24, 6, 7);
        xf[(ks + 1) & 1] = lds_r128(hb + ((ks + 1) % 24) * 1024);
        SBAR();
#pragma unroll
        for (int f = 0; f < 3; ++f) {
            acc[f] = MMA(120 + 3 * ks + f, xf[ks & 1], acc[f]);
            SBAR();
            WLD(120 + 3 * ks + f + NB);
        }
        SBAR();
    }
    SERVICE(24, 7, 8);
    store_tile(acc[0], 0); store_tile(acc[1], 1); store_tile(acc[2], 2);
    SBAR();
    f32x16 ac2[2]; ac2[0] = f32x16{}; ac2[1] = f32x16{};
#pragma unroll
    for (int ks = 0; ks < 24; ++ks) {
        if (ks + 1 < 24) xf[(ks + 1) & 1] = lds_r128(hb + (ks + 1) * 1024);
        SBAR();
#pragma unroll
        for (int f = 0; f < 2; ++f) {
            ac2[f] = MMA(192 + 2 * ks + f, xf[ks & 1], ac2[f]);
            SBAR();
            WLD(192 + 2 * ks + f + NB);
        }
        SBAR();
    }
    STAMPW(2, 9, 8);
#undef WLD
#undef SERVICE
    store_tile(ac2[0], 3); store_tile(ac2[1], 4);
    asm volatile("s_waitcnt vmcnt(0)" : "+v"(wb[0]), "+v"(wb[1]), "+v"(wb[2]), "+v"(wb[3]), "+v"(wb[4]), "+v"(wb[5]), "+v"(wb[6]), "+v"(wb[7]), "+v"(wb[8]), "+v"(wb[9]) :: "memory");
    asm volatile("" : "+v"(wb[10]), "+v"(wb[11]), "+v"(wb[12]), "+v"(wb[13]), "+v"(wb[14]), "+v"(wb[15]), "+v"(wb[16]), "+v"(wb[17]), "+v"(wb[18]), "+v"(wb[19]) :: "memory");
    k2_arrive(ws, tt, 0x10000u);
    STAMPW(2, 10, 8);
    k2_tail(x, b2, ws, lds, out, w >> 1, okq, wb);
}

__global__ __launch_bounds__(768, 3) void k2_main(const __bf16* __restrict__ x, const __bf16* __restrict__ b1, const __bf16* __restrict__ b2, char* ws, __bf16* __restrict__ out) {
    extern __shared__ __attribute__((aligned(16))) char lds[];
    const int wid = __builtin_amdgcn_readfirstlane(threadIdx.x >> 6);
    const int b = blockIdx.x, xcd = b & 7, head = xcd * 4 + ((b >> 3) & 3), qb = b >> 5;
    STAMPW(1, 0, AW);
    STAMPW(2, 0, 8);
    if (wid < 8) k2_attn(x, b2, ws, lds, out, wid, head, qb);
    else k2_mlp(x, b1, b2, ws, lds, out, wid - 8, xcd & 3, (xcd >> 2) * 32 + (b >> 3));
}

}

extern "C" void kernel_launch(void* const* d_in, const int* in_sizes, int n_in, void* d_out, int out_size, void* d_ws, size_t ws_size, hipStream_t stream) {
    const __bf16* x = (const __bf16*)d_in[0];
    const __bf16* wqkv = (const __bf16*)d_in[1];
    const __bf16* wo = (const __bf16*)d_in[2];
    const __bf16* w1 = (const __bf16*)d_in[4];
    const __bf16* b1 = (const __bf16*)d_in[5];
    const __bf16* w2 = (const __bf16*)d_in[6];
    const __bf16* b2 = (const __bf16*)d_in[7];
    char* ws = (char*)d_ws;
    (void)hipFuncSetAttribute((const void*)k1_qkv, hipFuncAttributeMaxDynamicSharedMemorySize, K1_LDS);
    (void)hipFuncSetAttribute((const void*)k2_main, hipFuncAttributeMaxDynamicSharedMemorySize, K2_LDS);
    k1_qkv<<<256, 768, K1_LDS, stream>>>(x, wqkv, wo, w1, w2, ws);
    k2_main<<<256, 768, K2_LDS, stream>>>(x, b1, b2, ws, (__bf16*)d_out);
}
```

```cpp
#include <hip/hip_runtime.h>
#include <hip/hip_bf16.h>
#include <stdint.h>

namespace {

typedef __attribute__((ext_vector_type(8))) __bf16 bf16x8;
typedef __attribute__((ext_vector_type(2))) __bf16 bf16x2;
typedef __attribute__((ext_vector_type(8))) short s16x8;
typedef __attribute__((ext_vector_type(4))) short s16x4;
typedef __attribute__((ext_vector_type(16))) float f32x16;
typedef __attribute__((ext_vector_type(4))) float f32x4;
typedef __attribute__((ext_vector_type(2))) float f32x2;
typedef __attribute__((ext_vector_type(4))) unsigned u32x4;
typedef __attribute__((ext_vector_type(2))) unsigned u32x2;
typedef __attribute__((address_space(3))) char* lds_ptr;
typedef __attribute__((address_space(3))) const char* lds_cptr;

constexpr int S_ = 2048, D_ = 640, H_ = 32, F_ = 1536;
constexpr float EPS = 1e-5f;
constexpr float QSCALE = 0.22360679774997896f * 1.4426950408889634f;

constexpr int QT_B = 1280;
constexpr int VROW_B = 352;
constexpr int KVT_B = 1280 + 4 * VROW_B;
constexpr size_t WS_Q = 0;
constexpr size_t WS_KV = WS_Q + (size_t)H_ * 64 * QT_B;
constexpr size_t WS_TOK = WS_KV + (size_t)H_ * 64 * KVT_B + 4096;
constexpr size_t WS_CS1 = WS_TOK + (size_t)S_ * 16;
constexpr size_t WS_PW = WS_CS1 + 8192;
constexpr size_t WS_PWO = WS_PW + (size_t)16 * 240 * 1024;
constexpr size_t WS_PART = WS_PWO + (size_t)20 * 40 * 1024;
constexpr size_t WS_O = WS_PART + (size_t)4 * 64 * 20 * 2048;
constexpr size_t WS_RT = WS_O + (size_t)64 * 40 * 1024;
constexpr size_t WS_RC = WS_RT + (size_t)S_ * 16;
constexpr size_t WS_CNT = (WS_RC + (size_t)4 * 3 * D_ * 8 + 255) / 256 * 256;
constexpr size_t WS_END = WS_CNT + 64 * 256;

#define STAMPW(kid, i, w)
#define STAMP(kid, i) STAMPW(kid, i, 0)
#define SBAR() __builtin_amdgcn_sched_barrier(0)
#define PIN(x) asm volatile("" : "+v"(x))
#define WAIT_BAR(N) asm volatile("s_waitcnt vmcnt(" #N ") lgkmcnt(0)\n\ts_barrier" ::: "memory")
#define WAIT_VM(N) asm volatile("s_waitcnt vmcnt(" #N ")" ::: "memory")

__device__ __forceinline__ f32x16 mfma16(bf16x8 a, bf16x8 b, f32x16 c) { return __builtin_amdgcn_mfma_f32_32x32x16_bf16(a, b, c, 0, 0, 0); }
__device__ __forceinline__ f32x16 mfma8(s16x4 a, s16x4 b, f32x16 c) { return __builtin_amdgcn_mfma_f32_32x32x8bf16_1k(a, b, c, 0, 0, 0); }
__device__ __forceinline__ unsigned pk2(float lo, float hi) { f32x2 v = {lo, hi}; return __builtin_bit_cast(unsigned, __builtin_convertvector(v, bf16x2)); }
__device__ __forceinline__ float bf_lo(unsigned u) { return __uint_as_float(u << 16); }
__device__ __forceinline__ float bf_hi(unsigned u) { return __uint_as_float(u & 0xffff0000u); }
__device__ __forceinline__ void glds16(const void* g, unsigned lds_base) {
    unsigned sv;
    asm volatile("s_mov_b32 %0, m0\n\ts_mov_b32 m0, %2\n\ts_nop 0\n\tglobal_load_lds_dwordx4 %1, off\n\ts_mov_b32 m0, %0" : "=&s"(sv) : "v"(g), "s"(lds_base) : "memory");
}
__device__ __forceinline__ void glds4(const void* g, unsigned lds_base) {
    unsigned sv;
    asm volatile("s_mov_b32 %0, m0\n\ts_mov_b32 m0, %2\n\ts_nop 0\n\tglobal_load_lds_dword %1, off\n\ts_mov_b32 m0, %0" : "=&s"(sv) : "v"(g), "s"(lds_base) : "memory");
}
__device__ __forceinline__ bf16x8 lds_r128(lds_cptr p) { return *(const __attribute__((address_space(3))) bf16x8*)p; }
__device__ __forceinline__ s16x4 lds_r64(lds_cptr p) { return *(const __attribute__((address_space(3))) s16x4*)p; }
__device__ __forceinline__ s16x4 lds_tr(lds_cptr p) { return __builtin_amdgcn_ds_read_tr16_b64_v4i16((__attribute__((address_space(3))) s16x4*)p); }
__device__ __forceinline__ bf16x8 cat8(s16x4 a, s16x4 b) { s16x8 v = {a[0], a[1], a[2], a[3], b[0], b[1], b[2], b[3]}; return __builtin_bit_cast(bf16x8, v); }
__device__ __forceinline__ float dot2(unsigned a, unsigned b, float c) { return __builtin_amdgcn_fdot2_f32_bf16(__builtin_bit_cast(bf16x2, a), __builtin_bit_cast(bf16x2, b), c, false); }
__device__ __forceinline__ float swap32_add(float v) { auto rr = __builtin_amdgcn_permlane32_swap(__float_as_uint(v), __float_as_uint(v), false, false); return __uint_as_float(rr[0]) + __uint_as_float(rr[1]); }
__device__ __forceinline__ float swap32_max(float v) { auto rr = __builtin_amdgcn_permlane32_swap(__float_as_uint(v), __float_as_uint(v), false, false); return __builtin_fmaxf(__uint_as_float(rr[0]), __uint_as_float(rr[1])); }
__device__ __forceinline__ void split3(float v, unsigned& a, unsigned& b, unsigned& c) {
    a = pk2(v, 0.f) & 0xffffu; float r = v - bf_lo(a);
    b = pk2(r, 0.f) & 0xffffu; r = r - bf_lo(b);
    c = pk2(r, 0.f) & 0xffffu;
}
__device__ __forceinline__ void st16(void* p, u32x4 v) {
    asm volatile("global_store_dwordx4 %0, %1, off sc1\n\ts_nop 1" :: "v"(p), "v"(v) : "memory");
}
__device__ __forceinline__ void st8(void* p, u32x2 v) { asm volatile("global_store_dwordx2 %0, %1, off sc1\n\ts_nop 1" :: "v"(p), "v"(v) : "memory"); }
#define QST16(p, v) (*(u32x4*)(p) = (v))
#define QST8(p, v) (*(u32x2*)(p) = (v))
#define PST16(p, v) st16((p), (v))
constexpr unsigned ONES2 = 0x3f803f80u;
typedef unsigned long long u64;
__device__ __forceinline__ u64 ld_agent(const u64* p) { return __hip_atomic_load(p, __ATOMIC_RELAXED, __HIP_MEMORY_SCOPE_AGENT); }
__device__ __forceinline__ void st_agent(u64* p, float v, unsigned tag) { __hip_atomic_store(p, (u64)__float_as_uint(v) | ((u64)tag << 32), __ATOMIC_RELAXED, __HIP_MEMORY_SCOPE_AGENT); }
__device__ __forceinline__ unsigned now_ticks() { return (unsigned)__builtin_amdgcn_s_memrealtime(); }
#define K1_SPIN 4096
__device__ __forceinline__ void lds_signal(unsigned addr, unsigned val = 1u) {
    asm volatile("s_mov_b64 exec, 1\n\tds_add_u32 %0, %1\n\ts_mov_b64 exec, -1" :: "v"(addr), "v"(val) : "memory");
}
__device__ __forceinline__ unsigned lds_peek(unsigned addr) { return *(const volatile __attribute__((address_space(3))) unsigned*)(uintptr_t)addr; }
__device__ __forceinline__ void lds_await(unsigned addr, unsigned first, unsigned target) {
    unsigned v = __builtin_amdgcn_readfirstlane(first);
    while (v < target) { __builtin_amdgcn_s_sleep(1); v = __builtin_amdgcn_readfirstlane(lds_peek(addr)); }
    asm volatile("" ::: "memory");
}

constexpr int K1_STAGE = 16384 + 20480, K1_AUX = 4 * K1_STAGE, K1_LDS = K1_AUX + 4096 + 5120 + 1024 + 256;

__device__ __forceinline__ constexpr unsigned k1_perm(int j) {
    return j == 0 ? 0x76453120u : (j == 3 ? 0x57064231u : (j == 4 ? 0x76254130u : 0x75643120u));
}
__device__ __forceinline__ int k1_piece_off(int P, int r, int& head) {
    head = P / 5; int rr = P - head * 5; const int h7 = head & 7;
    int role = rr;
    if (h7 == 1) role = (0x32410 >> (4 * rr)) & 7;
    if (h7 == 3) role = (0x32104 >> (4 * rr)) & 7;
    return role == 4 ? 1024 + r * 8 : (role >> 1) * 512 + r * 16 + (role & 1) * 8;
}

constexpr int K1_TF = K1_AUX, K1_FF = K1_AUX + 4096, K1_RS = K1_AUX + 9216;

template <bool VORI, int WN>
__device__ __forceinline__ void k1_body(char* __restrict__ ws, char* lds, int mt, int nt) {
    constexpr int NF = WN ? 2 : 3, F0 = WN ? 3 : 0;
    const int tid = threadIdx.x, lane = tid & 63, r = lane & 31, hh = lane >> 5;
    const int wid = __builtin_amdgcn_readfirstlane(tid >> 6), wm = wid & 3;
    const unsigned lds0 = (unsigned)(uintptr_t)lds;
    STAMP(0, 0);
    const int xrow = 32 * wm + r;
    const int q4 = (lane & 15) >> 2, p4 = lane & 3, gi = (lane >> 4) & 1;
    unsigned xa[4], wa[NF];
#pragma unroll
    for (int k = 0; k < 4; ++k) xa[k] = lds0 + xrow * 128 + (((2 * k + hh) ^ ((xrow >> 1) & 7)) * 16);
#pragma unroll
    for (int f = 0; f < NF; ++f) {
        int rel = 4 * gi + p4;
        if (!VORI) rel = (k1_perm(F0 + f) >> (4 * rel)) & 7;
        wa[f] = lds0 + 16384 + (8 * hh + q4) * 320 + (F0 + f) * 64 + rel * 8;
    }
    const int tt = mt * 4 + wm;
    const bool isq = nt < 4;
    char* const base = (!VORI && isq) ? ws + WS_Q + (size_t)tt * QT_B : ws + WS_KV + (size_t)tt * KVT_B;
    unsigned so[NF][3];
#pragma unroll
    for (int f = 0; f < NF; ++f) {
        if (!VORI) {
            const unsigned hstride = isq ? 64 * QT_B : 64 * KVT_B;
            const int T = (nt & 3) * 5 + F0 + f;
            const unsigned perm = k1_perm(F0 + f);
#pragma unroll
            for (int i = 0; i < 3; ++i) {
                int head;
                const int off = k1_piece_off(8 * T + ((perm >> (4 * ((i ? 2 + 2 * i : 0) + hh))) & 7), r, head);
                so[f][i] = head * hstride + off;
                PIN(so[f][i]);
            }
        } else {
            const int col = (nt - 8) * 160 + (F0 + f) * 32 + r;
            const int head = col / 20, d = col - head * 20;
            so[f][0] = head * (64 * KVT_B) + 1280 + hh * VROW_B + d * 16;
            so[f][1] = d;
            PIN(so[f][0]); PIN(so[f][1]);
        }
    }
    f32x16 acc[NF];
#pragma unroll
    for (int f = 0; f < NF; ++f) acc[f] = f32x16{};
    bf16x8 xf[4], wf[4][NF];
#define K1_RD(sl, ks) do { xf[ks] = lds_r128((lds_cptr)(uintptr_t)(xa[ks] + (sl))); \
        _Pragma("unroll") for (int f = 0; f < NF; ++f) { const lds_cptr p = (lds_cptr)(uintptr_t)(wa[f] + (sl)) + (ks) * 5120; wf[ks][f] = cat8(lds_tr(p), lds_tr(p + 1280)); } SBAR(); } while (0)
#define K1_MM(b) do { _Pragma("unroll") for (int f = 0; f < NF; ++f) acc[f] = VORI ? mfma16(xf[b], wf[b][f], acc[f]) : mfma16(wf[b][f], xf[b], acc[f]); SBAR(); } while (0)
    asm volatile("s_barrier" ::: "memory");
    STAMP(0, 1);
    __builtin_amdgcn_s_waitcnt(0xc07f);
    K1_RD(0u, 0); K1_RD(0u, 1);
#pragma unroll 1
    for (int st = 0; st < 10; ++st) {
        const unsigned sl = (st & 3) * K1_STAGE, sn = ((st + 1) & 3) * K1_STAGE;
        K1_RD(sl, 2); K1_MM(0);
        K1_RD(sl, 3); K1_MM(1);
        if (st < 9) asm volatile("s_waitcnt lgkmcnt(0)\n\ts_barrier" ::: "memory");
        K1_RD(sn, 0); K1_MM(2);
        K1_RD(sn, 1); K1_MM(3);
    }
#undef K1_RD
#undef K1_MM
    STAMP(0, 3);
    asm volatile("s_barrier" ::: "memory");
    const lds_cptr aux = (lds_cptr)lds;
    const bf16x8 tfb = lds_r128(aux + K1_TF + wm * 1024 + lane * 16);
    bf16x8 ffb[NF];
#pragma unroll
    for (int f = 0; f < NF; ++f) ffb[f] = lds_r128(aux + K1_FF + (F0 + f) * 1024 + lane * 16);
    const float* rsl = (const float*)(lds + K1_RS) + wm * 32;
    float rs[VORI ? 16 : 1];
    if (VORI) {
#pragma unroll
        for (int g = 0; g < 4; ++g) { const f32x4 t = *(const f32x4*)(rsl + 8 * g + 4 * hh); rs[4 * g] = t[0]; rs[4 * g + 1] = t[1]; rs[4 * g + 2] = t[2]; rs[4 * g + 3] = t[3]; }
    } else rs[0] = rsl[r] * (isq ? QSCALE : 1.f);
#pragma unroll
    for (int f = 0; f < NF; ++f) acc[f] = VORI ? mfma16(tfb, ffb[f], acc[f]) : mfma16(ffb[f], tfb, acc[f]);
    STAMP(0, 4);
#pragma unroll
    for (int f = 0; f < NF; ++f) {
        unsigned u[8];
#pragma unroll
        for (int i = 0; i < 8; ++i) u[i] = pk2(acc[f][2 * i] * rs[VORI ? 2 * i : 0], acc[f][2 * i + 1] * rs[VORI ? 2 * i + 1 : 0]);
        if (!VORI) {
            QST16(base + so[f][0], (u32x4{u[0], u[1], u[2], u[3]}));
            QST8(base + so[f][1], (u32x2{u[4], u[5]}));
            QST8(base + so[f][2], (u32x2{u[6], u[7]}));
        } else {
            char* p = base + so[f][0];
            QST16(p, (u32x4{u[0], u[1], u[2], u[3]}));
            QST16(p + 2 * VROW_B, (u32x4{u[4], u[5], u[6], u[7]}));
            if (so[f][1] == 19) {
                QST16(p + 16, (u32x4{ONES2, ONES2, ONES2, ONES2})); QST16(p + 2 * VROW_B + 16, (u32x4{ONES2, ONES2, ONES2, ONES2}));
                QST16(p + 32, (u32x4{0, 0, 0, 0})); QST16(p + 2 * VROW_B + 32, (u32x4{0, 0, 0, 0}));
            }
        }
    }
}

template <bool VORI>
__device__ __forceinline__ void k1_helper(const __bf16* __restrict__ x, const __bf16* __restrict__ w, char* __restrict__ ws, char* lds, int mt, int nt, int hw) {
    const int lane = threadIdx.x & 63, r = lane & 31, hh = lane >> 5;
    const unsigned lds0 = (unsigned)(uintptr_t)lds;
    const unsigned t_start = now_ticks();
    unsigned xo[4], wo[5];
#pragma unroll
    for (int i = 0; i < 4; ++i) {
        const int row = 8 * (4 * hw + i) + (lane >> 3);
        xo[i] = row * (D_ * 2) + (((lane & 7) ^ ((row >> 1) & 7)) * 16) - i * 1024;
    }
#pragma unroll
    for (int i = 0; i < 5; ++i) {
        const int idx = (5 * hw + i) * 64 + lane, krow = idx / 20, ch = idx - krow * 20;
        wo[i] = krow * (3 * D_ * 2) + ch * 16 - (i & 3) * 1024;
    }
    const char* xs = (const char*)x + (size_t)mt * 128 * (D_ * 2);
    const char* wsr = (const char*)w + nt * 320;
#define K1_M0(v) asm volatile("s_mov_b32 m0, %0\n\ts_nop 0" :: "s"(v) : "memory")
#define K1_DMA(vo, sp, imm) asm volatile("global_load_lds_dwordx4 %0, %1 offset:" #imm :: "v"(vo), "s"(sp) : "memory")
#define K1_PIECE(i, xp, wp, lb) do { \
        if ((i) == 0) { K1_M0((lb) + hw * 4096); K1_DMA(xo[0], xp, 0); } \
        if ((i) == 1) K1_DMA(xo[1], xp, 1024); \
        if ((i) == 2) K1_DMA(xo[2], xp, 2048); \
        if ((i) == 3) K1_DMA(xo[3], xp, 3072); \
        if ((i) == 4) { K1_M0((lb) + 16384 + hw * 5120); K1_DMA(wo[0], wp, 0); } \
        if ((i) == 5) K1_DMA(wo[1], wp, 1024); \
        if ((i) == 6) K1_DMA(wo[2], wp, 2048); \
        if ((i) == 7) K1_DMA(wo[3], wp, 3072); \
        if ((i) == 8) { K1_M0((lb) + 16384 + hw * 5120 + 4096); K1_DMA(wo[4], wp, 0); } \
        SBAR(); } while (0)
#pragma unroll 1
    for (int st = 0; st < 4; ++st) {
        const char* xp = xs + st * 128; const char* wp = wsr + (size_t)st * 64 * (3 * D_ * 2);
        const unsigned lb = lds0 + st * K1_STAGE;
#pragma unroll
        for (int i = 0; i < 9; ++i) K1_PIECE(i, xp, wp, lb);
    }
    WAIT_BAR(27);
#pragma unroll 1
    for (int st = 0; st < 7; ++st) {
        if (st) {
            const char* xp = xs + (st + 3) * 128; const char* wp = wsr + (size_t)(st + 3) * 64 * (3 * D_ * 2);
            const unsigned lb = lds0 + ((st + 3) & 3) * K1_STAGE;
#pragma unroll
            for (int i = 0; i < 9; ++i) K1_PIECE(i, xp, wp, lb);
        }
        WAIT_BAR(18);
    }
#undef K1_PIECE
#undef K1_DMA
#undef K1_M0
    const int tok = mt * 128 + 32 * hw + r;
    int col[2];
#pragma unroll
    for (int f = 0; f < 2; ++f) {
        const int t = f ? 0 : hw + 1;
        const int pc = VORI ? (r >> 2) : (int)((k1_perm(t) >> (4 * (r >> 2))) & 7);
        col[f] = nt * 160 + t * 32 + pc * 4 + (r & 3);
    }
    const u64* rt = (const u64*)(ws + WS_RT) + tok * 2 + hh;
    const u64* rc = (const u64*)(ws + WS_RC) + hh * (2 * 3 * D_);
    u64 rec[5];
#define K1_POLL() do { rec[0] = ld_agent(rt); rec[1] = ld_agent(rc + col[0]); rec[2] = ld_agent(rc + 3 * D_ + col[0]); rec[3] = ld_agent(rc + col[1]); rec[4] = ld_agent(rc + 3 * D_ + col[1]); } while (0)
    K1_POLL();
    WAIT_BAR(14);
    WAIT_BAR(5);
    bool ok = false;
#pragma unroll 1
    for (int it = 0; it < K1_SPIN; ++it) {
        WAIT_VM(0);
        const unsigned span = now_ticks() - t_start + 32u;
        bool good = true;
#pragma unroll
        for (int i = 0; i < 5; ++i) good = good && (unsigned)(rec[i] >> 32) - t_start <= span;
        if (__all(good)) { ok = true; break; }
        __builtin_amdgcn_s_sleep(8);
        K1_POLL();
    }
#undef K1_POLL
    float tv = __uint_as_float((unsigned)rec[0]);
    float cs0 = __uint_as_float((unsigned)rec[1]) + __uint_as_float((unsigned)rec[2]), cs1 = __uint_as_float((unsigned)rec[3]) + __uint_as_float((unsigned)rec[4]);
    if (__builtin_expect(!ok, 0)) {
        const u32x4* rowp = (const u32x4*)(x + (size_t)tok * D_) + hh * 8;
        float mu = 0.f;
#pragma unroll 1
        for (int pass = 0; pass < 2; ++pass) {
            float s1 = 0.f, s2 = 0.f, s3 = 0.f;
#pragma unroll 1
            for (int c = 0; c < 8; ++c) {
                float a0 = 0.f, a1 = 0.f;
#pragma unroll 1
                for (int i = 0; i < 5; ++i) {
                    const u32x4 u = rowp[c + 16 * i];
#pragma unroll
                    for (int j = 0; j < 4; ++j) {
                        if (pass == 0) a0 = dot2(u[j], ONES2, a0);
                        else { const float d0 = bf_lo(u[j]) - mu, d1 = bf_hi(u[j]) - mu; a0 = __builtin_fmaf(d0, d0, a0); a1 = __builtin_fmaf(d1, d1, a1); }
                    }
                }
                if (pass) a0 += a1;
                s1 = (c & 1) ? s1 + a0 : a0;
                if (c & 1) s2 = (c & 2) ? s2 + s1 : s1;
                if ((c & 3) == 3) s3 = (c & 4) ? s3 + s2 : s2;
            }
            float tot = swap32_add(s3) * (1.f / D_);
            PIN(tot);
            if (pass == 0) mu = tot; else tv = hh ? __builtin_amdgcn_rsqf(tot + EPS) : mu;
        }
        float q[2][2];
#pragma unroll
        for (int kq = 0; kq < 2; ++kq) {
            const unsigned short* wc = (const unsigned short*)w + (size_t)(2 * hh + kq) * 160 * (3 * D_);
            float t0[4] = {0.f, 0.f, 0.f, 0.f}, t1[4] = {0.f, 0.f, 0.f, 0.f};
#pragma unroll 1
            for (int wv = 0; wv < 8; ++wv) {
#pragma unroll
                for (int g = 0; g < 4; ++g) {
                    const unsigned short* rw = wc + (wv * 20 + g) * (3 * D_);
                    float c0 = bf_lo(rw[col[0]]), c1 = bf_lo(rw[col[1]]);
#pragma unroll
                    for (int i = 1; i < 5; ++i) { c0 += bf_lo(rw[4 * i * (3 * D_) + col[0]]); c1 += bf_lo(rw[4 * i * (3 * D_) + col[1]]); }
                    t0[g] += c0; t1[g] += c1;
                }
            }
            q[0][kq] = (t0[0] + t0[1]) + (t0[2] + t0[3]); q[1][kq] = (t1[0] + t1[1]) + (t1[2] + t1[3]);
        }
        cs0 = q[0][0] + q[0][1]; cs1 = q[1][0] + q[1][1];
    }
    {
        unsigned m1, m2, m3; split3(-tv, m1, m2, m3);
        u32x4 tf = {m1 | (m1 << 16), m2 | (m1 << 16), m2 | (m3 << 16), m2 | (m3 << 16)};
        if (hh) tf = u32x4{0, 0, 0, 0};
        *(u32x4*)(lds + K1_TF + hw * 1024 + lane * 16) = tf;
        unsigned a1, a2, a3; split3(swap32_add(cs0), a1, a2, a3);
        *(u32x4*)(lds + K1_FF + (hw + 1) * 1024 + lane * 16) = u32x4{a1 | (a2 << 16), a1 | (a3 << 16), a2 | (a1 << 16), a3 | (a2 << 16)};
        if (hw == 0) {
            split3(swap32_add(cs1), a1, a2, a3);
            *(u32x4*)(lds + K1_FF + lane * 16) = u32x4{a1 | (a2 << 16), a1 | (a3 << 16), a2 | (a1 << 16), a3 | (a2 << 16)};
        }
        if (hh) ((float*)(lds + K1_RS))[32 * hw + r] = tv;
    }
    asm volatile("s_waitcnt lgkmcnt(0)\n\ts_barrier" ::: "memory");
}

struct K1Stat { u32x4 xr[5], wr[5]; };
__device__ __forceinline__ void k1_stat_load(K1Stat& sv, const __bf16* __restrict__ x, const __bf16* __restrict__ w, char* __restrict__ ws, int job) {
    const int tid = threadIdx.x, lane = tid & 63, wid = tid >> 6;
    const u32x4* xp = (const u32x4*)(x + (size_t)(job * 32 + wid * 4 + (lane >> 4)) * D_) + (lane & 15);
    const int jc = job < 60 ? job : 59;
    const u32x4* wp = (const u32x4*)(w + (size_t)((jc / 15) * 160 + wid * 20 + (lane >> 4)) * (3 * D_) + (jc % 15) * 128) + (lane & 15);
#pragma unroll
    for (int i = 0; i < 5; ++i) sv.xr[i] = xp[16 * i];
#pragma unroll
    for (int i = 0; i < 5; ++i) sv.wr[i] = wp[(size_t)i * 4 * (3 * D_ / 8)];
}
__device__ __forceinline__ float red16(float v) {
    v += __shfl_xor(v, 1); v += __shfl_xor(v, 2); v += __shfl_xor(v, 4); v += __shfl_xor(v, 8);
    return v;
}
__device__ __forceinline__ void k1_stat(const K1Stat& sv, char* __restrict__ ws, char* lds, int job) {
    const int tid = threadIdx.x, lane = tid & 63, wid = tid >> 6;
    float a = 0.f;
#pragma unroll
    for (int i = 0; i < 5; ++i)
#pragma unroll
        for (int j = 0; j < 4; ++j) a = dot2(sv.xr[i][j], ONES2, a);
    float mu = red16(a) * (1.f / D_);
    PIN(mu);
    float a0 = 0.f, a1 = 0.f;
#pragma unroll
    for (int i = 0; i < 5; ++i)
#pragma unroll
        for (int j = 0; j < 4; ++j) { const float d0 = bf_lo(sv.xr[i][j]) - mu, d1 = bf_hi(sv.xr[i][j]) - mu; a0 = __builtin_fmaf(d0, d0, a0); a1 = __builtin_fmaf(d1, d1, a1); }
    float var = red16(a0 + a1) * (1.f / D_);
    PIN(var);
    const int tok = job * 32 + wid * 4 + (lane >> 4), c = lane & 15;
    float pub = c ? __builtin_amdgcn_rsqf(var + EPS) : mu;
    PIN(pub);
    if (c < 2) st_agent((u64*)(ws + WS_RT) + tok * 2 + c, pub, now_ticks());
    if (c == 2) {
        const float sig2 = __builtin_sqrtf(var * (1.f + EPS) + EPS * EPS);
        *(f32x4*)(ws + WS_TOK + (size_t)tok * 16) = f32x4{mu, 1.0f / sig2, sig2, 0.f};
    }
    float cs[8];
#pragma unroll
    for (int j = 0; j < 4; ++j) {
        cs[2 * j] = bf_lo(sv.wr[0][j]); cs[2 * j + 1] = bf_hi(sv.wr[0][j]);
#pragma unroll
        for (int i = 1; i < 5; ++i) { cs[2 * j] += bf_lo(sv.wr[i][j]); cs[2 * j + 1] += bf_hi(sv.wr[i][j]); }
    }
    float* red = (float*)(lds + 86016);
    *(f32x4*)(red + (wid * 4 + (lane >> 4)) * 128 + c * 8) = f32x4{cs[0], cs[1], cs[2], cs[3]};
    *(f32x4*)(red + (wid * 4 + (lane >> 4)) * 128 + c * 8 + 4) = f32x4{cs[4], cs[5], cs[6], cs[7]};
    asm volatile("s_waitcnt lgkmcnt(0)\n\ts_barrier" ::: "memory");
    if (tid < 128 && job < 60) {
        float t[4] = {0.f, 0.f, 0.f, 0.f};
#pragma unroll
        for (int i = 0; i < 32; ++i) t[i & 3] += red[i * 128 + tid];
        st_agent((u64*)(ws + WS_RC) + (job / 15) * (3 * D_) + (job % 15) * 128 + tid, (t[0] + t[1]) + (t[2] + t[3]), now_ticks());
    }
}

template <int KIND>
__device__ __forceinline__ void k1_pack(const __bf16* __restrict__ wsrc, char* __restrict__ ws, char* lds, int strip, int third, const K1Stat& sv, int job) {
    constexpr int NI = KIND == 1 ? 8 : 10, NKS = KIND == 1 ? 4 : 5, ROWB = (KIND == 0 ? F_ : D_) * 2;
    const int tid = threadIdx.x, lane = tid & 63, r = lane & 31, hh = lane >> 5;
    const int wid = __builtin_amdgcn_readfirstlane(tid >> 6);
    const unsigned lds0 = (unsigned)(uintptr_t)lds + wid * 10240;
    const int row0 = third * 512 + wid * NI * 8;
    const char* src = (const char*)wsrc + (size_t)(row0 + (lane >> 3)) * ROWB + strip * 128 + (((lane & 7) ^ (((lane >> 4) & 1) * 4)) * 16);
#pragma unroll
    for (int i = 0; i < NI; ++i) glds16(src + (size_t)i * 8 * ROWB, lds0 + i * 1024);
    const int q4 = (lane & 15) >> 2, p4 = lane & 3, gi = (lane >> 4) & 1;
    int pc = 4 * gi + p4;
    if (KIND != 0) pc = (0x75643120u >> (4 * pc)) & 7;
    const lds_cptr sb = (lds_cptr)lds + wid * 10240;
    float cs[2] = {0.f, 0.f};
    k1_stat(sv, ws, lds, job);
    WAIT_VM(0);
#pragma unroll
    for (int ks = 0; ks < NKS; ++ks) {
        int ra, rb;
        if (KIND == 0) { ra = 16 * ks + 8 * hh; rb = ra + 4; }
        else if (KIND == 1) { ra = 16 * ks + 4 * hh; rb = ra + 8; }
        else if (ks < 4) { ra = 20 * ks + 4 * hh; rb = ra + 8; }
        else { ra = 40 * hh + 16; rb = ra + 20; }
#pragma unroll
        for (int f = 0; f < 2; ++f) {
            const int co = ((((f * 8 + pc) >> 1) ^ ((q4 >> 1) * 4)) * 16) + (pc & 1) * 8;
            const bf16x8 fr = cat8(lds_tr(sb + (ra + q4) * 128 + co), lds_tr(sb + (rb + q4) * 128 + co));
            const int ft = strip * 2 + f;
            size_t dst;
            if (KIND == 0) dst = WS_PW + ((size_t)(ft / 3) * 240 + (wid * 5 + ks) * 3 + ft % 3) * 1024;
            else if (KIND == 1) { const int g = third * 32 + wid * 4 + ks; const int f5 = ft % 5; dst = WS_PW + ((size_t)((g / 24) * 4 + ft / 5) * 240 + (f5 < 3 ? 120 + (g % 24) * 3 + f5 : 192 + (g % 24) * 2 + f5 - 3)) * 1024; }
            else dst = WS_PWO + ((size_t)ft * 40 + (ks < 4 ? wid * 4 + ks : 32 + wid)) * 1024;
            PST16(ws + dst + lane * 16, __builtin_bit_cast(u32x4, fr));
            if (KIND == 0) {
                const u32x4 a = __builtin_bit_cast(u32x4, fr);
#pragma unroll
                for (int i = 0; i < 4; ++i) cs[f] = dot2(a[i], ONES2, cs[f]);
            }
        }
    }
    if (KIND == 0) {
        float* red = (float*)(lds + 81920);
        red[(wid * 2 + hh) * 64 + r] = cs[0];
        red[(wid * 2 + hh) * 64 + 32 + r] = cs[1];
        __syncthreads();
        if (tid < 64) {
            float s = 0.f;
#pragma unroll
            for (int i = 0; i < 16; ++i) s += red[i * 64 + tid];
            ((float*)(ws + WS_CS1))[strip * 64 + tid] = s;
        }
    }
}

__global__ __launch_bounds__(768, 3) void k1_qkv(const __bf16* __restrict__ x, const __bf16* __restrict__ wqkv, const __bf16* __restrict__ wo, const __bf16* __restrict__ w1,
                                                 const __bf16* __restrict__ w2, char* __restrict__ ws) {
    extern __shared__ __attribute__((aligned(16))) char lds[];
    const int b = blockIdx.x, xcd = b & 7, idx = b >> 3;
    const int wid = __builtin_amdgcn_readfirstlane(threadIdx.x >> 6);
    if (idx >= 24) {
        if (wid >= 8) return;
        const int job = (idx - 24) * 8 + xcd;
        if (threadIdx.x < 2) __hip_atomic_store((unsigned*)(ws + WS_CNT + job * 256 + threadIdx.x * 128), 0u, __ATOMIC_RELAXED, __HIP_MEMORY_SCOPE_AGENT);
        K1Stat sv;
        k1_stat_load(sv, x, wqkv, ws, job);
        if (job < 24) k1_pack<0>(w1, ws, lds, job, 0, sv, job);
        else if (job < 54) k1_pack<1>(w2, ws, lds, (job - 24) % 10, (job - 24) / 10, sv, job);
        else k1_pack<2>(wo, ws, lds, job - 54, 0, sv, job);
        STAMP(3, 0);
        return;
    }
    const int mt = (xcd >> 1) * 4 + (idx & 3), nt = (xcd & 1) * 6 + (idx >> 2);
    if (wid >= 8) { if (nt < 8) k1_helper<false>(x, wqkv, ws, lds, mt, nt, wid - 8); else k1_helper<true>(x, wqkv, ws, lds, mt, nt, wid - 8); return; }
    if (nt < 8) { if (wid >= 4) k1_body<false, 1>(ws, lds, mt, nt); else k1_body<false, 0>(ws, lds, mt, nt); }
    else { if (wid >= 4) k1_body<true, 1>(ws, lds, mt, nt); else k1_body<true, 0>(ws, lds, mt, nt); }
    STAMP(0, 5);
}

#define MLP_NB 20
#define AP_HI 2
#define AP_MID 1
#define AP_LO 0
#define GELU_PRIO 1
#define MLP_SLEEP 0
#define AW 7
#define MLP_PRIO 0
template <int N> __device__ __forceinline__ void wait_vm() { asm volatile("s_waitcnt vmcnt(%0)" :: "n"(N) : "memory"); }
#define WAIT_VMX(N) wait_vm<(N)>()
constexpr int AT_SLOT = 8 * KVT_B;
constexpr int K2_X = 4 * AT_SLOT, K2_H = K2_X + 40960, K2_CNT = K2_H + 24576, K2_LDS = K2_CNT + 128;
constexpr int C_FULL = 0, C_FREE = 64, C_X = 112, C_H = 116, C_PRDY = 120, C_MDONE = 124;

__device__ __forceinline__ void attn_slow(const char* __restrict__ kv, bf16x8 qf, s16x4 qt, f32x16& o, int r, int hh) {
    float m = -INFINITY;
    o = f32x16{};
    if (hh) qt = s16x4{0, 0, 0, 0};
    for (int t = 0; t < 64; ++t) {
        const char* p = kv + (size_t)t * KVT_B;
        const bf16x8 kf = *(const bf16x8*)(p + hh * 512 + r * 16);
        const s16x4 kt = *(const s16x4*)(p + 1024 + r * 8);
        const int rv = r < 20 ? r : 20;
        const bf16x8 v0 = *(const bf16x8*)(p + 1280 + hh * VROW_B + rv * 16);
        const bf16x8 v1 = *(const bf16x8*)(p + 1280 + 2 * VROW_B + hh * VROW_B + rv * 16);
        f32x16 s = mfma16(kf, qf, f32x16{});
        s = mfma8(kt, qt, s);
        float tm = s[0];
#pragma unroll
        for (int i = 1; i < 16; ++i) tm = __builtin_fmaxf(tm, s[i]);
        tm = swap32_max(tm);
        const float mn = __builtin_fmaxf(m, tm);
        const float al = __builtin_amdgcn_exp2f(m - mn);
        m = mn;
        u32x4 p0, p1;
#pragma unroll
        for (int i = 0; i < 4; ++i) {
            p0[i] = pk2(__builtin_amdgcn_exp2f(s[2 * i] - m), __builtin_amdgcn_exp2f(s[2 * i + 1] - m));
            p1[i] = pk2(__builtin_amdgcn_exp2f(s[8 + 2 * i] - m), __builtin_amdgcn_exp2f(s[8 + 2 * i + 1] - m));
        }
#pragma unroll
        for (int i = 0; i < 16; ++i) o[i] *= al;
        o = mfma16(v0, __builtin_bit_cast(bf16x8, p0), o);
        o = mfma16(v1, __builtin_bit_cast(bf16x8, p1), o);
    }
}

__device__ __forceinline__ void attn_finish(char* __restrict__ ws, const char* kvh, bf16x8 qf, s16x4 qt, f32x16 o, int qt_idx, int head, int lane) {
    const int r = lane & 31, hh = lane >> 5;
    float l = swap32_add(hh ? o[8] : 0.f);
    bool bad = !(l < 1e30f);
#pragma unroll
    for (int i = 0; i < 8; ++i) bad |= !(__builtin_fabsf(o[i]) < 3e38f);
#pragma unroll
    for (int i = 8; i < 12; ++i) bad |= (hh == 0) && !(__builtin_fabsf(o[i]) < 3e38f);
    if (__builtin_expect(__any(bad), 0)) {
        attn_slow(kvh, qf, qt, o, r, hh);
        l = swap32_add(hh ? o[8] : 0.f);
    }
    const float inv = 1.0f / l;
    char* ob = ws + WS_O + (size_t)qt_idx * 40 * 1024;
    st16(ob + head * 1024 + lane * 16, u32x4{pk2(o[0] * inv, o[1] * inv), pk2(o[2] * inv, o[3] * inv), pk2(o[4] * inv, o[5] * inv), pk2(o[6] * inv, o[7] * inv)});
    if (hh == 0) st8(ob + (32 + (head >> 2)) * 1024 + (head & 3) * 256 + r * 8, u32x2{pk2(o[8] * inv, o[9] * inv), pk2(o[10] * inv, o[11] * inv)});
}

#define K2_SPIN 400000
#define AP_CUT 8
#define TAILP 0
#define TAIL_PD 6
#define WO_PRE 12
__device__ __forceinline__ int fin_tt() { return (blockIdx.x & 7) * 8 + (blockIdx.x >> 5); }
__device__ __forceinline__ int fin_ft(int f) { return ((blockIdx.x >> 3) & 3) * 5 + f; }
__device__ __forceinline__ int wo_frag(int kq, int k) { return k < 16 ? kq * 16 + k : 32 + kq * 4 + (k - 16); }
#define BAR() asm volatile("s_waitcnt lgkmcnt(0)\n\ts_barrier" ::: "memory")
__device__ __forceinline__ void k2_count(char* ws, int off) {
    if ((threadIdx.x & 63) == 0) __hip_atomic_fetch_add((unsigned*)(ws + WS_CNT + off), 1u, __ATOMIC_RELAXED, __HIP_MEMORY_SCOPE_AGENT);
}
__device__ __forceinline__ void k2_wait(const char* ws, int off, unsigned target) {
    const unsigned* c = (const unsigned*)(ws + WS_CNT + off);
#pragma unroll 1
    for (int it = 0; it < K2_SPIN; ++it) {
        if (__builtin_amdgcn_readfirstlane(__hip_atomic_load(c, __ATOMIC_RELAXED, __HIP_MEMORY_SCOPE_AGENT)) >= target) break;
        __builtin_amdgcn_s_sleep(1);
    }
    asm volatile("" ::: "memory");
}
constexpr int T_O = 0, T_RED = 40960, T_SUM = K2_X;
__device__ __forceinline__ void k2_gather(const __bf16* __restrict__ x, const __bf16* __restrict__ b2, char* ws, char* lds, int w) {
    const int lane = threadIdx.x & 63, r = lane & 31, hh = lane >> 5;
    const unsigned cnt = (unsigned)(uintptr_t)lds + K2_CNT;
    const int tt = fin_tt(), c0 = fin_ft(0) * 32 + hh * 8;
    u32x4 xv[3], bv[3], pv[3][4];
#pragma unroll
    for (int i = 0; i < 3; ++i) {
        const int u = w + 4 * i < 10 ? w + 4 * i : w;
        xv[i] = *(const u32x4*)(x + (size_t)(tt * 32 + r) * D_ + c0 + u * 16); bv[i] = *(const u32x4*)(b2 + c0 + u * 16);
    }
    SBAR();
    if (w == 0) { k2_wait(ws, tt * 256 + 128, 4u); lds_signal(cnt + C_PRDY); }
    else lds_await(cnt + C_PRDY, lds_peek(cnt + C_PRDY), 1);
    const __amdgpu_buffer_rsrc_t rp = __builtin_amdgcn_make_buffer_rsrc(ws + WS_PART + ((size_t)tt * 20 + fin_ft(0)) * 2048, 0, 3 * 64 * 20 * 2048 + 10240, 0x00020000);
#pragma unroll
    for (int i = 0; i < 3; ++i) {
        const int u = w + 4 * i < 10 ? w + 4 * i : w;
#pragma unroll
        for (int k = 0; k < 4; ++k) pv[i][k] = __builtin_amdgcn_raw_buffer_load_b128(rp, lane * 16, k * (64 * 20 * 2048) + u * 1024, 16);
    }
    SBAR();
#pragma unroll
    for (int i = 0; i < 3; ++i) {
        const int u = w + 4 * i < 10 ? w + 4 * i : w;
        float m[8];
#pragma unroll
        for (int k = 0; k < 4; ++k) {
            m[2 * k] = (bf_lo(xv[i][k]) + bf_lo(bv[i][k])) + ((bf_lo(pv[i][0][k]) + bf_lo(pv[i][1][k])) + (bf_lo(pv[i][2][k]) + bf_lo(pv[i][3][k])));
            m[2 * k + 1] = (bf_hi(xv[i][k]) + bf_hi(bv[i][k])) + ((bf_hi(pv[i][0][k]) + bf_hi(pv[i][1][k])) + (bf_hi(pv[i][2][k]) + bf_hi(pv[i][3][k])));
        }
        f32x4* d = (f32x4*)(lds + T_SUM + u * 2048) + lane;
        d[0] = f32x4{m[0], m[1], m[2], m[3]}; d[64] = f32x4{m[4], m[5], m[6], m[7]};
    }
    STAMPW(2, 11, 8);
}
__device__ __forceinline__ void k2_tail(char* ws, char* lds, __bf16* __restrict__ out, int f, int kq, const bf16x8 (&wf)[20]) {
    const int lane = threadIdx.x & 63, r = lane & 31, hh = lane >> 5;
    const int tt = fin_tt(), ft = fin_ft(f), g = kq * 5 + f;
    const __amdgpu_buffer_rsrc_t ro = __builtin_amdgcn_make_buffer_rsrc(ws + WS_O + ((size_t)tt * 40 + g * 4) * 1024, 0, 4096, 0x00020000);
    u32x4 ov[4];
    STAMP(4, 0);
    BAR();
    STAMP(4, 1);
#pragma unroll
    for (int k = 0; k < 4; ++k) ov[k] = __builtin_amdgcn_raw_buffer_load_b128(ro, lane * 16 + k * 1024, 0, 16);
    SBAR();
    const f32x4* sp = (const f32x4*)(lds + T_SUM + (f * 2 + kq) * 2048) + lane;
    const f32x4 s0 = sp[0], s1 = sp[64];
    SBAR();
#pragma unroll
    for (int k = 0; k < 4; ++k) *(u32x4*)(lds + T_O + (g * 4 + k) * 1024 + lane * 16) = ov[k];
    BAR();
    STAMP(4, 2);
    f32x16 acc = f32x16{};
    const lds_cptr ob = (lds_cptr)lds + T_O + kq * 16384 + lane * 16, ot = (lds_cptr)lds + T_O + (32 + kq * 4) * 1024 + hh * 512 + r * 8;
    constexpr int PD = TAIL_PD;
    bf16x8 of[PD];
#define OFRAG(k) ((k) < 16 ? lds_r128(ob + (k) * 1024) : cat8(lds_r64(ot + ((k) - 16) * 1024), lds_r64(ot + ((k) - 16) * 1024 + 256)))
#pragma unroll
    for (int k = 0; k < PD; ++k) of[k] = OFRAG(k);
    SBAR();
#pragma unroll
    for (int k = 0; k < 20; ++k) {
        acc = mfma16(wf[k], of[k % PD], acc);
        SBAR();
        if (k + PD < 20) of[k % PD] = OFRAG(k + PD);
        SBAR();
    }
#undef OFRAG
    STAMP(4, 3);
    f32x4* red = (f32x4*)(lds + T_RED + f * 4096);
    {
        const int c = 1 - kq;
        red[(2 * c) * 64 + lane] = kq ? f32x4{acc[0], acc[1], acc[2], acc[3]} : f32x4{acc[8], acc[9], acc[10], acc[11]};
        red[(2 * c + 1) * 64 + lane] = kq ? f32x4{acc[4], acc[5], acc[6], acc[7]} : f32x4{acc[12], acc[13], acc[14], acc[15]};
    }
    float m[8];
#pragma unroll
    for (int k = 0; k < 8; ++k) m[k] = (kq ? acc[8 + k] : acc[k]) + (k < 4 ? s0[k] : s1[k - 4]);
    BAR();
    {
        const f32x4 a = red[(2 * kq) * 64 + lane], bb = red[(2 * kq + 1) * 64 + lane];
        u32x4 u;
        u[0] = pk2(a[0] + m[0], a[1] + m[1]); u[1] = pk2(a[2] + m[2], a[3] + m[3]);
        u[2] = pk2(bb[0] + m[4], bb[1] + m[5]); u[3] = pk2(bb[2] + m[6], bb[3] + m[7]);
        *(u32x4*)(out + (size_t)(tt * 32 + r) * D_ + ft * 32 + kq * 16 + hh * 8) = u;
    }
    STAMP(4, 4);
}

__device__ __forceinline__ void k2_attn(char* ws, char* lds, __bf16* __restrict__ out, int wid, int head, int qb) {
    const int lane = threadIdx.x & 63, r = lane & 31, hh = lane >> 5;
    const unsigned lds0 = (unsigned)(uintptr_t)lds, cnt = lds0 + K2_CNT;
    const int qt_idx = qb * 8 + wid;
    const char* kvh = ws + WS_KV + (size_t)head * 64 * KVT_B;
    const char* src = kvh + wid * KVT_B + lane * 16;
    auto tile_issue = [&](int c) {
        const unsigned base = lds0 + (c & 3) * AT_SLOT + wid * KVT_B;
        glds16(src + c * AT_SLOT, base);
        glds16(src + c * AT_SLOT + 1024, base + 1024);
        if (lane < (KVT_B - 2048) / 16) glds16(src + c * AT_SLOT + 2048, base + 2048);
    };
    tile_issue(0);
    {
        const char* qp = ws + WS_Q + ((size_t)head * 64 + qt_idx) * QT_B + lane * 16;
        glds16(qp, lds0 + K2_H + wid * QT_B);
        if (lane < 16) glds16(qp + 1024, lds0 + K2_H + wid * QT_B + 1024);
    }
    if (wid == 0 && lane < 32) *(__attribute__((address_space(3))) unsigned*)(uintptr_t)(cnt + lane * 4) = lane == 8 ? 8u : 0u;
    asm volatile("s_waitcnt lgkmcnt(0)\n\ts_barrier" ::: "memory");
    const int koff = hh * 512 + r * 16, ktoff = hh ? 1600 : 1024 + r * 8, voff = 1280 + hh * VROW_B + (r < 21 ? r : 21) * 16;
    WAIT_VM(0);
    const lds_cptr qp = (lds_cptr)lds + K2_H + wid * QT_B;
    bf16x8 qf = lds_r128(qp + koff);
    s16x4 qt = lds_r64(qp + 1024 + r * 8);
    asm volatile("s_waitcnt lgkmcnt(0)" : "+v"(qf), "+v"(qt) :: "memory");
    lds_signal(cnt + C_FULL);
    tile_issue(1); tile_issue(2); tile_issue(3);
    lds_await(cnt + C_FULL, lds_peek(cnt + C_FULL), 8);
    STAMPW(1, 1, AW);
    f32x16 o = f32x16{}, sA, sB;
    u32x4 pA0, pA1, pB0 = {0, 0, 0, 0}, pB1 = {0, 0, 0, 0};
    bf16x8 kf, v0, v1; s16x4 kt;
    {
        const lds_cptr sb = (lds_cptr)lds;
        kf = lds_r128(sb + koff); kt = lds_r64(sb + ktoff);
        if (hh) qt = s16x4{0, 0, 0, 0};
        sA = mfma16(kf, qf, f32x16{});
        sA = mfma8(kt, qt, sA);
        float m0 = sA[0];
#pragma unroll
        for (int i = 1; i < 16; ++i) m0 = __builtin_fmaxf(m0, sA[i]);
        m0 = swap32_max(m0);
        const unsigned nm = pk2(-m0, 0.f) & 0xffffu;
        if (hh) qt[0] = (short)nm;
#pragma unroll
        for (int i = 0; i < 16; ++i) sA[i] += bf_lo(nm);
        kf = lds_r128(sb + KVT_B + koff); kt = lds_r64(sb + KVT_B + ktoff);
        v0 = lds_r128(sb + voff); v1 = v0;
    }
#define EX(v) __builtin_amdgcn_exp2f(v)
#define GRP(MF, SC, i, PW, j) do { MF; SBAR(); const float e0 = EX(SC[i]), e1 = EX(SC[i + 1]), e2 = EX(SC[i + 2]), e3 = EX(SC[i + 3]); PW[j] = pk2(e0, e1); PW[j + 1] = pk2(e2, e3); PIN(PW); SBAR(); } while (0)
#define STEP(SC, SN, PC0, PC1, PP0, PP1, KP, VP) do { \
        GRP(SN = mfma16(kf, qf, f32x16{}), SC, 0, PC0, 0); \
        kf = lds_r128((KP) + koff); SBAR(); \
        GRP(o = mfma16(v0, __builtin_bit_cast(bf16x8, PP0), o), SC, 4, PC0, 2); \
        v0 = lds_r128((VP) + voff); SBAR(); \
        GRP(SN = mfma8(kt, qt, SN), SC, 8, PC1, 0); \
        kt = lds_r64((KP) + ktoff); SBAR(); \
        GRP(o = mfma16(v1, __builtin_bit_cast(bf16x8, PP1), o), SC, 12, PC1, 2); \
        v1 = lds_r128((VP) + voff + 2 * VROW_B); SBAR(); \
    } while (0)
    if (wid >= 4) __builtin_amdgcn_s_setprio(AP_MID);
    for (int c = 0; c < 7; ++c) {
        const lds_cptr sb = (lds_cptr)lds + (c & 3) * AT_SLOT;
        const lds_cptr nb = (lds_cptr)lds + ((c + 1) & 3) * AT_SLOT;
        if (wid < 4) __builtin_amdgcn_s_setprio(AP_HI);
        STEP(sA, sB, pA0, pA1, pB0, pB1, sb + 2 * KVT_B, sb + 0 * KVT_B);
        STEP(sB, sA, pB0, pB1, pA0, pA1, sb + 3 * KVT_B, sb + 1 * KVT_B);
        STEP(sA, sB, pA0, pA1, pB0, pB1, sb + 4 * KVT_B, sb + 2 * KVT_B);
        if (c == 0) { WAIT_VM(6); lds_signal(cnt + C_FULL + 4); }
        STEP(sB, sA, pB0, pB1, pA0, pA1, sb + 5 * KVT_B, sb + 3 * KVT_B);
        STEP(sA, sB, pA0, pA1, pB0, pB1, sb + 6 * KVT_B, sb + 4 * KVT_B);
        if (wid < 4 && c < AP_CUT) __builtin_amdgcn_s_setprio(AP_LO);
        const unsigned pk = lds_peek(cnt + C_FULL + 4 * (c + 1)), pf = lds_peek(cnt + C_FREE + 4 * c - 4);
        SBAR();
        STEP(sB, sA, pB0, pB1, pA0, pA1, sb + 7 * KVT_B, sb + 5 * KVT_B);
        lds_await(cnt + C_FULL + 4 * (c + 1), pk, 8);
        if (c <= 4) {
            if (c >= 1) { lds_await(cnt + C_FREE + 4 * c - 4, pf, 8); tile_issue(c + 3); }
            WAIT_VM(3);
        } else WAIT_VM(0);
        if (c <= 5) lds_signal(cnt + C_FULL + 4 * (c + 2));
        SBAR();
        STEP(sA, sB, pA0, pA1, pB0, pB1, nb + 0 * KVT_B, sb + 6 * KVT_B);
        STEP(sB, sA, pB0, pB1, pA0, pA1, nb + 1 * KVT_B, sb + 7 * KVT_B);
        lds_signal(cnt + C_FREE + 4 * c);
        STAMPW(1, 2 + c, AW);
    }
    const int tf = 2 + (wid >> 1), tkq = wid & 1;
    const char* wop = ws + WS_PWO + (size_t)fin_ft(tf < 5 ? tf : 4) * 40 * 1024 + lane * 16;
    bf16x8 wf[20];
#pragma unroll
    for (int k = 0; k < WO_PRE; ++k) { wf[k] = *(const bf16x8*)(wop + (wid < 6 ? wo_frag(tkq, k) * 1024 : 0)); SBAR(); }
    {
        const lds_cptr sb = (lds_cptr)lds + 3 * AT_SLOT;
        if (wid < 4) __builtin_amdgcn_s_setprio(AP_HI);
        STEP(sA, sB, pA0, pA1, pB0, pB1, sb + 2 * KVT_B, sb + 0 * KVT_B);
        STEP(sB, sA, pB0, pB1, pA0, pA1, sb + 3 * KVT_B, sb + 1 * KVT_B);
        STEP(sA, sB, pA0, pA1, pB0, pB1, sb + 4 * KVT_B, sb + 2 * KVT_B);
        STEP(sB, sA, pB0, pB1, pA0, pA1, sb + 5 * KVT_B, sb + 3 * KVT_B);
        STEP(sA, sB, pA0, pA1, pB0, pB1, sb + 6 * KVT_B, sb + 4 * KVT_B);
        if (wid < 4 && 7 < AP_CUT) __builtin_amdgcn_s_setprio(AP_LO);
        STEP(sB, sA, pB0, pB1, pA0, pA1, sb + 7 * KVT_B, sb + 5 * KVT_B);
        STEP(sA, sB, pA0, pA1, pB0, pB1, sb + 7 * KVT_B, sb + 6 * KVT_B);
        STEP(sB, sA, pB0, pB1, pA0, pA1, sb + 7 * KVT_B, sb + 7 * KVT_B);
        STAMPW(1, 9, AW);
    }
    o = mfma16(v0, __builtin_bit_cast(bf16x8, pB0), o);
    o = mfma16(v1, __builtin_bit_cast(bf16x8, pB1), o);
#undef STEP
#undef GRP
#undef EX
    attn_finish(ws, kvh, qf, qt, o, qt_idx, head, lane);
    WAIT_VM(0);
    k2_count(ws, qt_idx * 256);
    STAMPW(1, 10, AW);
    __builtin_amdgcn_s_setprio(0);
    if (wid >= 6) {
        if (wid == 7) { k2_wait(ws, fin_tt() * 256, 32u); BAR(); }
        return;
    }
    SBAR();
#pragma unroll
    for (int k = WO_PRE; k < 20; ++k) { wf[k] = *(const bf16x8*)(wop + wo_frag(tkq, k) * 1024); SBAR(); }
    k2_tail(ws, lds, out, tf, tkq, wf);
}

constexpr int mlp_cnt(int i, int nb) {
    int n = nb - 1;
    if (192 <= i && i < 192 + nb) n += 6;
    return n;
}
__device__ __forceinline__ f32x2 pk_mul(f32x2 a, f32x2 b) { return a * b; }
__device__ __forceinline__ f32x2 pk_fma(f32x2 a, f32x2 b, f32x2 c) { return __builtin_elementwise_fma(a, b, c); }

__device__ __forceinline__ void k2_mlp(const __bf16* __restrict__ x, const __bf16* __restrict__ b1, const __bf16* __restrict__ b2, char* ws, char* lds, __bf16* __restrict__ out, int w, int j, int tt) {
    constexpr int NB = 20;
    const int lane = threadIdx.x & 63, r = lane & 31, hh = lane >> 5;
    const unsigned lds0 = (unsigned)(uintptr_t)lds, cnt = lds0 + K2_CNT;
    asm volatile("s_barrier" ::: "memory");
    __builtin_amdgcn_s_setprio(MLP_PRIO);
    lds_await(cnt + C_FULL, lds_peek(cnt + C_FULL), 8);
    const int l16 = lane * 16;
    f32x4 ts; float csv[3]; unsigned b1v[3];
    {
        const char* tp = ws + WS_TOK + (size_t)tt * 512;
        const char* cp = ws + WS_CS1 + (j * 384 + w * 96) * 4;
        const char* bp = (const char*)b1 + (j * 384 + w * 96) * 2;
        asm volatile("global_load_dwordx4 %0, %1, %2" : "=v"(ts) : "v"(r * 16), "s"(tp));
        asm volatile("global_load_dword %0, %3, %4\n\tglobal_load_dword %1, %3, %4 offset:128\n\tglobal_load_dword %2, %3, %4 offset:256" : "=&v"(csv[0]), "=&v"(csv[1]), "=&v"(csv[2]) : "v"(r * 4), "s"(cp));
        asm volatile("global_load_ushort %0, %3, %4\n\tglobal_load_ushort %1, %3, %4 offset:64\n\tglobal_load_ushort %2, %3, %4 offset:128" : "=&v"(b1v[0]), "=&v"(b1v[1]), "=&v"(b1v[2]) : "v"(r * 2), "s"(bp));
    }
    {
        const int row = 8 * w + (lane >> 3);
        const char* xs = (const char*)x + (size_t)(tt * 32 + row) * (D_ * 2) + (((lane & 7) ^ ((row >> 1) & 7)) * 16);
#pragma unroll
        for (int st = 0; st < 10; ++st) glds16(xs + st * 128, lds0 + K2_X + st * 4096 + w * 1024);
    }
    const uint64_t wbase = (uint64_t)(ws + WS_PW + (size_t)(j * 4 + w) * 240 * 1024);
    const u32x4 wr = {(unsigned)wbase, (unsigned)(wbase >> 32) & 0xffffu, 240u * 1024u, 0x00020000u};
    const uint64_t obase = (uint64_t)(ws + WS_PWO + (size_t)fin_ft(w >> 1) * 40 * 1024);
    const u32x4 wr2 = {(unsigned)obase, (unsigned)(obase >> 32) & 0xffffu, 40u * 1024u, 0x00020000u};
    const int okq = w & 1;
    bf16x8 wb[NB];
#define ABL_LIM 240
#define WLD(i) do { if ((i) < NB) asm volatile("buffer_load_dwordx4 %0, %1, %2, %3 offen offset:%4" : "=v"(wb[(i) % NB]) : "v"(l16), "s"(wr), "s"(((i) >> 2) * 4096), "n"(((i) & 3) * 1024)); \
        else if ((i) < ABL_LIM) asm volatile("buffer_load_dwordx4 %0, %1, %2, %3 offen offset:%4" : "+v"(wb[(i) % NB]) : "v"(l16), "s"(wr), "s"(((i) >> 2) * 4096), "n"(((i) & 3) * 1024)); \
        else if ((i) >= 240) asm volatile("buffer_load_dwordx4 %0, %1, %2, %3 offen offset:%4" : "+v"(wb[(i) % NB]) : "v"(l16), "s"(wr2), "s"(wo_frag(okq, ((i) - 240) & ~3) * 1024), "n"(((i) & 3) * 1024)); \
        SBAR(); } while (0)
#define WWAIT(i) asm volatile("s_waitcnt vmcnt(%1)" : "+v"(wb[(i) % NB]) : "n"(mlp_cnt(i, NB)))
#define MMA(i, b, c) ({ if (MLP_SLEEP) __builtin_amdgcn_s_sleep(MLP_SLEEP); WWAIT(i); mfma16(wb[(i) % NB], b, c); })
#pragma unroll
    for (int i = 0; i < NB; ++i) WLD(i);
    asm volatile("s_waitcnt vmcnt(%7)" : "+v"(ts), "+v"(csv[0]), "+v"(csv[1]), "+v"(csv[2]), "+v"(b1v[0]), "+v"(b1v[1]), "+v"(b1v[2]) : "n"(NB) : "memory");
    lds_signal(cnt + C_X);
    lds_await(cnt + C_X, lds_peek(cnt + C_X), 4);
    STAMPW(2, 1, 8);
#define SERVICE(NVM, cf, ci) STAMPW(2, (cf) - 1, 8)
    unsigned xa[4];
#pragma unroll
    for (int k = 0; k < 4; ++k) { xa[k] = lds0 + K2_X + r * 128 + (((2 * k + hh) ^ ((r >> 1) & 7)) * 16); PIN(xa[k]); }
    f32x16 acc[3]; acc[0] = f32x16{}; acc[1] = f32x16{}; acc[2] = f32x16{};
    bf16x8 xf[2];
    xf[0] = lds_r128((lds_cptr)(uintptr_t)xa[0]);
#pragma unroll
    for (int ks = 0; ks < 40; ++ks) {
        if (ks == 12) SERVICE(24, 3, 4);
        if (ks == 26) SERVICE(24, 4, 5);
        if (ks + 1 < 40) xf[(ks + 1) & 1] = lds_r128((lds_cptr)(uintptr_t)xa[(ks + 1) & 3] + ((ks + 1) >> 2) * 4096);
        SBAR();
#pragma unroll
        for (int f = 0; f < 3; ++f) {
            acc[f] = MMA(3 * ks + f, xf[ks & 1], acc[f]);
            SBAR();
            WLD(3 * ks + f + NB);
        }
        SBAR();
    }
    SERVICE(24, 5, 6);
    __builtin_amdgcn_s_setprio(GELU_PRIO);
    {
        unsigned m1, m2, m3; split3(hh ? ts[2] : -ts[0], m1, m2, m3);
        u32x4 tf = {m1 | (m1 << 16), m2 | (m1 << 16), m2 | (m3 << 16), m2 | (m3 << 16)};
        if (hh) tf = u32x4{m1 | (m2 << 16), m3, 0, 0};
#pragma unroll
        for (int f = 0; f < 3; ++f) {
            unsigned a1, a2, a3; split3(csv[f], a1, a2, a3);
            u32x4 ff = {a1 | (a2 << 16), a1 | (a3 << 16), a2 | (a1 << 16), a3 | (a2 << 16)};
            if (hh) ff = u32x4{b1v[f] | (b1v[f] << 16), b1v[f], 0, 0};
            acc[f] = mfma16(__builtin_bit_cast(bf16x8, ff), __builtin_bit_cast(bf16x8, tf), acc[f]);
            SBAR();
        }
    }
    {
        constexpr float C0 = -2.0f * 0.7978845608028654f * 1.4426950408889634f, C1 = C0 * 0.044715f;
        const float s2 = ts[1], sg = ts[2];
        const f32x2 k0 = {C0 * s2, C0 * s2}, k1 = {C1 * s2 * s2 * s2, C1 * s2 * s2 * s2}, sg2 = {sg, sg};
        __attribute__((address_space(3))) u32x4* hp = (__attribute__((address_space(3))) u32x4*)(uintptr_t)(lds0 + K2_H + w * 6144 + lane * 16);
#pragma unroll
        for (int f = 0; f < 3; ++f) {
#pragma unroll
            for (int s = 0; s < 2; ++s) {
                u32x4 u;
#pragma unroll
                for (int i = 0; i < 4; ++i) {
                    const f32x2 a = {acc[f][8 * s + 2 * i], acc[f][8 * s + 2 * i + 1]};
                    const f32x2 z = pk_mul(a, pk_fma(pk_mul(a, a), k1, k0));
                    const f32x2 e = {__builtin_amdgcn_exp2f(z[0]), __builtin_amdgcn_exp2f(z[1])};
                    const f32x2 d = pk_fma(e, sg2, sg2);
                    const f32x2 hv = pk_mul(a, f32x2{__builtin_amdgcn_rcpf(d[0]), __builtin_amdgcn_rcpf(d[1])});
                    u[i] = pk2(hv[0], hv[1]);
                }
                hp[(2 * f + s) * 64] = u;
                SBAR();
            }
        }
    }
    asm volatile("s_waitcnt lgkmcnt(0)" ::: "memory");
    lds_signal(cnt + C_H);
    __builtin_amdgcn_s_setprio(MLP_PRIO);
    lds_await(cnt + C_H, lds_peek(cnt + C_H), 4);
    STAMPW(2, 8, 8);
    unsigned ha = lds0 + K2_H + lane * 16;
    PIN(ha);
    const lds_cptr hb = (lds_cptr)(uintptr_t)ha;
    char* pp = ws + WS_PART + (((size_t)j * 64 + tt) * 20 + w * 5) * 2048 + lane * 16;
    auto store_tile = [&](const f32x16& t, int f) {
#pragma unroll
        for (int c = 0; c < 2; ++c) {
            float a[8];
#pragma unroll
            for (int i = 0; i < 8; ++i) a[i] = t[8 * c + i];
            st16(pp + (f * 2 + c) * 1024, u32x4{pk2(a[0], a[1]), pk2(a[2], a[3]), pk2(a[4], a[5]), pk2(a[6], a[7])});
            SBAR();
        }
    };
    acc[0] = f32x16{}; acc[1] = f32x16{}; acc[2] = f32x16{};
    xf[0] = lds_r128(hb);
#pragma unroll
    for (int ks = 0; ks < 24; ++ks) {
        if (ks == 10) SERVICE(24, 6, 7);
        xf[(ks + 1) & 1] = lds_r128(hb + ((ks + 1) % 24) * 1024);
        SBAR();
#pragma unroll
        for (int f = 0; f < 3; ++f) {
            acc[f] = MMA(120 + 3 * ks + f, xf[ks & 1], acc[f]);
            SBAR();
            WLD(120 + 3 * ks + f + NB);
        }
        SBAR();
    }
    SERVICE(24, 7, 8);
    store_tile(acc[0], 0); store_tile(acc[1], 1); store_tile(acc[2], 2);
    SBAR();
    f32x16 ac2[2]; ac2[0] = f32x16{}; ac2[1] = f32x16{};
#pragma unroll
    for (int ks = 0; ks < 24; ++ks) {
        if (ks + 1 < 24) xf[(ks + 1) & 1] = lds_r128(hb + (ks + 1) * 1024);
        SBAR();
#pragma unroll
        for (int f = 0; f < 2; ++f) {
            ac2[f] = MMA(192 + 2 * ks + f, xf[ks & 1], ac2[f]);
            SBAR();
            WLD(192 + 2 * ks + f + NB);
        }
        SBAR();
    }
    STAMPW(2, 9, 8);
#undef WLD
#undef SERVICE
    __builtin_amdgcn_s_setprio(TAILP);
    store_tile(ac2[0], 3); store_tile(ac2[1], 4);
    asm volatile("s_waitcnt vmcnt(0)" : "+v"(wb[0]), "+v"(wb[1]), "+v"(wb[2]), "+v"(wb[3]), "+v"(wb[4]), "+v"(wb[5]), "+v"(wb[6]), "+v"(wb[7]), "+v"(wb[8]), "+v"(wb[9]) :: "memory");
    asm volatile("" : "+v"(wb[10]), "+v"(wb[11]), "+v"(wb[12]), "+v"(wb[13]), "+v"(wb[14]), "+v"(wb[15]), "+v"(wb[16]), "+v"(wb[17]), "+v"(wb[18]), "+v"(wb[19]) :: "memory");
    {
        unsigned old = 0;
        if (lane == 0) old = __hip_atomic_fetch_add((__attribute__((address_space(3))) unsigned*)(uintptr_t)(cnt + C_MDONE), 1u, __ATOMIC_RELAXED, __HIP_MEMORY_SCOPE_WORKGROUP);
        if (__builtin_amdgcn_readfirstlane(old) == 3u) k2_count(ws, tt * 256 + 128);
    }
    STAMPW(2, 10, 8);
    k2_gather(x, b2, ws, lds, w);
    k2_tail(ws, lds, out, w >> 1, okq, wb);
}

__global__ __launch_bounds__(768, 3) void k2_main(const __bf16* __restrict__ x, const __bf16* __restrict__ b1, const __bf16* __restrict__ b2, char* ws, __bf16* __restrict__ out) {
    extern __shared__ __attribute__((aligned(16))) char lds[];
    const int wid = __builtin_amdgcn_readfirstlane(threadIdx.x >> 6);
    const int b = blockIdx.x, xcd = b & 7, head = xcd * 4 + ((b >> 3) & 3), qb = b >> 5;
    STAMPW(1, 0, AW);
    STAMPW(2, 0, 8);
    if (wid < 8) k2_attn(ws, lds, out, wid, head, qb);
    else k2_mlp(x, b1, b2, ws, lds, out, wid - 8, xcd & 3, (xcd >> 2) * 32 + (b >> 3));
}

}

extern "C" void kernel_launch(void* const* d_in, const int* in_sizes, int n_in, void* d_out, int out_size, void* d_ws, size_t ws_size, hipStream_t stream) {
    const __bf16* x = (const __bf16*)d_in[0];
    const __bf16* wqkv = (const __bf16*)d_in[1];
    const __bf16* wo = (const __bf16*)d_in[2];
    const __bf16* w1 = (const __bf16*)d_in[4];
    const __bf16* b1 = (const __bf16*)d_in[5];
    const __bf16* w2 = (const __bf16*)d_in[6];
    const __bf16* b2 = (const __bf16*)d_in[7];
    char* ws = (char*)d_ws;
    (void)hipFuncSetAttribute((const void*)k1_qkv, hipFuncAttributeMaxDynamicSharedMemorySize, K1_LDS);
    (void)hipFuncSetAttribute((const void*)k2_main, hipFuncAttributeMaxDynamicSharedMemorySize, K2_LDS);
    k1_qkv<<<256, 768, K1_LDS, stream>>>(x, wqkv, wo, w1, w2, ws);
    k2_main<<<256, 768, K2_LDS, stream>>>(x, b1, b2, ws, (__bf16*)d_out);
}
```

```cpp
#include <hip/hip_runtime.h>
#include <hip/hip_bf16.h>
#include <stdint.h>

namespace {

typedef __attribute__((ext_vector_type(8))) __bf16 bf16x8;
typedef __attribute__((ext_vector_type(2))) __bf16 bf16x2;
typedef __attribute__((ext_vector_type(8))) short s16x8;
typedef __attribute__((ext_vector_type(4))) short s16x4;
typedef __attribute__((ext_vector_type(16))) float f32x16;
typedef __attribute__((ext_vector_type(4))) float f32x4;
typedef __attribute__((ext_vector_type(2))) float f32x2;
typedef __attribute__((ext_vector_type(4))) unsigned u32x4;
typedef __attribute__((ext_vector_type(2))) unsigned u32x2;
typedef __attribute__((address_space(3))) char* lds_ptr;
typedef __attribute__((address_space(3))) const char* lds_cptr;

constexpr int S_ = 2048, D_ = 640, H_ = 32, F_ = 1536;
constexpr float EPS = 1e-5f;
constexpr float QSCALE = 0.22360679774997896f * 1.4426950408889634f;

constexpr int QT_B = 1280;
constexpr int VROW_B = 352;
constexpr int KVT_B = 1280 + 4 * VROW_B;
constexpr size_t WS_Q = 0;
constexpr size_t WS_KV = WS_Q + (size_t)H_ * 64 * QT_B;
constexpr size_t WS_TOK = WS_KV + (size_t)H_ * 64 * KVT_B + 4096;
constexpr size_t WS_CS1 = WS_TOK + (size_t)S_ * 16;
constexpr size_t WS_PW = WS_CS1 + 8192;
constexpr size_t WS_PWO = WS_PW + (size_t)16 * 240 * 1024;
constexpr size_t WS_PART = WS_PWO + (size_t)20 * 40 * 1024;
constexpr size_t WS_O = WS_PART + (size_t)4 * 64 * 20 * 2048;
constexpr size_t WS_RT = WS_O + (size_t)64 * 40 * 1024;
constexpr size_t WS_RC = WS_RT + (size_t)S_ * 16;
constexpr size_t WS_CNT = (WS_RC + (size_t)4 * 3 * D_ * 8 + 255) / 256 * 256;
constexpr size_t WS_END = WS_CNT + 64 * 256;

#define STAMPW(kid, i, w)
#define STAMP(kid, i) STAMPW(kid, i, 0)
#define SBAR() __builtin_amdgcn_sched_barrier(0)
#define PIN(x) asm volatile("" : "+v"(x))
#define WAIT_BAR(N) asm volatile("s_waitcnt vmcnt(" #N ") lgkmcnt(0)\n\ts_barrier" ::: "memory")
#define WAIT_VM(N) asm volatile("s_waitcnt vmcnt(" #N ")" ::: "memory")

__device__ __forceinline__ f32x16 mfma16(bf16x8 a, bf16x8 b, f32x16 c) { return __builtin_amdgcn_mfma_f32_32x32x16_bf16(a, b, c, 0, 0, 0); }
__device__ __forceinline__ f32x16 mfma8(s16x4 a, s16x4 b, f32x16 c) { return __builtin_amdgcn_mfma_f32_32x32x8bf16_1k(a, b, c, 0, 0, 0); }
__device__ __forceinline__ unsigned pk2(float lo, float hi) { f32x2 v = {lo, hi}; return __builtin_bit_cast(unsigned, __builtin_convertvector(v, bf16x2)); }
__device__ __forceinline__ float bf_lo(unsigned u) { return __uint_as_float(u << 16); }
__device__ __forceinline__ float bf_hi(unsigned u) { return __uint_as_float(u & 0xffff0000u); }
__device__ __forceinline__ void glds16(const void* g, unsigned lds_base) {
    unsigned sv;
    asm volatile("s_mov_b32 %0, m0\n\ts_mov_b32 m0, %2\n\ts_nop 0\n\tglobal_load_lds_dwordx4 %1, off\n\ts_mov_b32 m0, %0" : "=&s"(sv) : "v"(g), "s"(lds_base) : "memory");
}
__device__ __forceinline__ void glds4(const void* g, unsigned lds_base) {
    unsigned sv;
    asm volatile("s_mov_b32 %0, m0\n\ts_mov_b32 m0, %2\n\ts_nop 0\n\tglobal_load_lds_dword %1, off\n\ts_mov_b32 m0, %0" : "=&s"(sv) : "v"(g), "s"(lds_base) : "memory");
}
__device__ __forceinline__ bf16x8 lds_r128(lds_cptr p) { return *(const __attribute__((address_space(3))) bf16x8*)p; }
__device__ __forceinline__ s16x4 lds_r64(lds_cptr p) { return *(const __attribute__((address_space(3))) s16x4*)p; }
__device__ __forceinline__ s16x4 lds_tr(lds_cptr p) { return __builtin_amdgcn_ds_read_tr16_b64_v4i16((__attribute__((address_space(3))) s16x4*)p); }
__device__ __forceinline__ bf16x8 cat8(s16x4 a, s16x4 b) { s16x8 v = {a[0], a[1], a[2], a[3], b[0], b[1], b[2], b[3]}; return __builtin_bit_cast(bf16x8, v); }
__device__ __forceinline__ float dot2(unsigned a, unsigned b, float c) { return __builtin_amdgcn_fdot2_f32_bf16(__builtin_bit_cast(bf16x2, a), __builtin_bit_cast(bf16x2, b), c, false); }
__device__ __forceinline__ float swap32_add(float v) { auto rr = __builtin_amdgcn_permlane32_swap(__float_as_uint(v), __float_as_uint(v), false, false); return __uint_as_float(rr[0]) + __uint_as_float(rr[1]); }
__device__ __forceinline__ float swap32_max(float v) { auto rr = __builtin_amdgcn_permlane32_swap(__float_as_uint(v), __float_as_uint(v), false, false); return __builtin_fmaxf(__uint_as_float(rr[0]), __uint_as_float(rr[1])); }
__device__ __forceinline__ void split3(float v, unsigned& a, unsigned& b, unsigned& c) {
    a = pk2(v, 0.f) & 0xffffu; float r = v - bf_lo(a);
    b = pk2(r, 0.f) & 0xffffu; r = r - bf_lo(b);
    c = pk2(r, 0.f) & 0xffffu;
}
__device__ __forceinline__ void st16(void* p, u32x4 v) {
    asm volatile("global_store_dwordx4 %0, %1, off sc1\n\ts_nop 1" :: "v"(p), "v"(v) : "memory");
}
__device__ __forceinline__ void st8(void* p, u32x2 v) { asm volatile("global_store_dwordx2 %0, %1, off sc1\n\ts_nop 1" :: "v"(p), "v"(v) : "memory"); }
#define QST16(p, v) (*(u32x4*)(p) = (v))
#define QST8(p, v) (*(u32x2*)(p) = (v))
#define PST16(p, v) st16((p), (v))
constexpr unsigned ONES2 = 0x3f803f80u;
typedef unsigned long long u64;
__device__ __forceinline__ u64 ld_agent(const u64* p) { return __hip_atomic_load(p, __ATOMIC_RELAXED, __HIP_MEMORY_SCOPE_AGENT); }
__device__ __forceinline__ void st_agent(u64* p, float v, unsigned tag) { __hip_atomic_store(p, (u64)__float_as_uint(v) | ((u64)tag << 32), __ATOMIC_RELAXED, __HIP_MEMORY_SCOPE_AGENT); }
__device__ __forceinline__ unsigned now_ticks() { return (unsigned)__builtin_amdgcn_s_memrealtime(); }
#define K1_SPIN 4096
__device__ __forceinline__ void lds_signal(unsigned addr, unsigned val = 1u) {
    asm volatile("s_mov_b64 exec, 1\n\tds_add_u32 %0, %1\n\ts_mov_b64 exec, -1" :: "v"(addr), "v"(val) : "memory");
}
__device__ __forceinline__ unsigned lds_peek(unsigned addr) { return *(const volatile __attribute__((address_space(3))) unsigned*)(uintptr_t)addr; }
__device__ __forceinline__ void lds_await(unsigned addr, unsigned first, unsigned target) {
    unsigned v = __builtin_amdgcn_readfirstlane(first);
    while (v < target) { __builtin_amdgcn_s_sleep(1); v = __builtin_amdgcn_readfirstlane(lds_peek(addr)); }
    asm volatile("" ::: "memory");
}

#define K1_SYM "_ZN12_GLOBAL__N_16k1_qkvEPKDF16bS1_S1_S1_S1_Pc"
#define K2_SYM "_ZN12_GLOBAL__N_17k2_mainEPKDF16bS1_S1_PcPDF16b"
constexpr int K1_CODE = 32000, K2_CODE = 23000;
#define CODE_ADDR(a, SYM) asm volatile("s_getpc_b64 s[40:41]\n\ts_add_u32 s40, s40, " SYM "@rel32@lo+4\n\ts_addc_u32 s41, s41, " SYM "@rel32@hi+12\n\ts_mov_b64 %0, s[40:41]" : "=s"(a) :: "s40", "s41", "scc")
__device__ __forceinline__ unsigned code_touch(uint64_t base, int part, int size) {
    int off = part * 8192 + (int)(threadIdx.x & 63) * 128;
    off = off < size ? off : size;
    unsigned v;
    asm volatile("global_load_dword %0, %1, %2" : "=v"(v) : "v"(off), "s"(base) : "memory");
    return v;
}
#define CODE_DONE(v) asm volatile("s_waitcnt vmcnt(0)" : "+v"(v) :: "memory")
#define CPF 2

constexpr int K1_STAGE = 16384 + 20480, K1_AUX = 4 * K1_STAGE, K1_LDS = K1_AUX + 4096 + 5120 + 1024 + 256;

__device__ __forceinline__ constexpr unsigned k1_perm(int j) {
    return j == 0 ? 0x76453120u : (j == 3 ? 0x57064231u : (j == 4 ? 0x76254130u : 0x75643120u));
}
__device__ __forceinline__ int k1_piece_off(int P, int r, int& head) {
    head = P / 5; int rr = P - head * 5; const int h7 = head & 7;
    int role = rr;
    if (h7 == 1) role = (0x32410 >> (4 * rr)) & 7;
    if (h7 == 3) role = (0x32104 >> (4 * rr)) & 7;
    return role == 4 ? 1024 + r * 8 : (role >> 1) * 512 + r * 16 + (role & 1) * 8;
}

constexpr int K1_TF = K1_AUX, K1_FF = K1_AUX + 4096, K1_RS = K1_AUX + 9216;

template <bool VORI, int WN>
__device__ __forceinline__ void k1_body(char* __restrict__ ws, char* lds, int mt, int nt) {
    constexpr int NF = WN ? 2 : 3, F0 = WN ? 3 : 0;
    const int tid = threadIdx.x, lane = tid & 63, r = lane & 31, hh = lane >> 5;
    const int wid = __builtin_amdgcn_readfirstlane(tid >> 6), wm = wid & 3;
    const unsigned lds0 = (unsigned)(uintptr_t)lds;
    STAMP(0, 0);
    const int xrow = 32 * wm + r;
    const int q4 = (lane & 15) >> 2, p4 = lane & 3, gi = (lane >> 4) & 1;
    unsigned xa[4], wa[NF];
#pragma unroll
    for (int k = 0; k < 4; ++k) xa[k] = lds0 + xrow * 128 + (((2 * k + hh) ^ ((xrow >> 1) & 7)) * 16);
#pragma unroll
    for (int f = 0; f < NF; ++f) {
        int rel = 4 * gi + p4;
        if (!VORI) rel = (k1_perm(F0 + f) >> (4 * rel)) & 7;
        wa[f] = lds0 + 16384 + (8 * hh + q4) * 320 + (F0 + f) * 64 + rel * 8;
    }
    const int tt = mt * 4 + wm;
    const bool isq = nt < 4;
    char* const base = (!VORI && isq) ? ws + WS_Q + (size_t)tt * QT_B : ws + WS_KV + (size_t)tt * KVT_B;
    unsigned so[NF][3];
#pragma unroll
    for (int f = 0; f < NF; ++f) {
        if (!VORI) {
            const unsigned hstride = isq ? 64 * QT_B : 64 * KVT_B;
            const int T = (nt & 3) * 5 + F0 + f;
            const unsigned perm = k1_perm(F0 + f);
#pragma unroll
            for (int i = 0; i < 3; ++i) {
                int head;
                const int off = k1_piece_off(8 * T + ((perm >> (4 * ((i ? 2 + 2 * i : 0) + hh))) & 7), r, head);
                so[f][i] = head * hstride + off;
                PIN(so[f][i]);
            }
        } else {
            const int col = (nt - 8) * 160 + (F0 + f) * 32 + r;
            const int head = col / 20, d = col - head * 20;
            so[f][0] = head * (64 * KVT_B) + 1280 + hh * VROW_B + d * 16;
            so[f][1] = d;
            PIN(so[f][0]); PIN(so[f][1]);
        }
    }
    f32x16 acc[NF];
#pragma unroll
    for (int f = 0; f < NF; ++f) acc[f] = f32x16{};
    bf16x8 xf[4], wf[4][NF];
#define K1_RD(sl, ks) do { xf[ks] = lds_r128((lds_cptr)(uintptr_t)(xa[ks] + (sl))); \
        _Pragma("unroll") for (int f = 0; f < NF; ++f) { const lds_cptr p = (lds_cptr)(uintptr_t)(wa[f] + (sl)) + (ks) * 5120; wf[ks][f] = cat8(lds_tr(p), lds_tr(p + 1280)); } SBAR(); } while (0)
#define K1_MM(b) do { _Pragma("unroll") for (int f = 0; f < NF; ++f) acc[f] = VORI ? mfma16(xf[b], wf[b][f], acc[f]) : mfma16(wf[b][f], xf[b], acc[f]); SBAR(); } while (0)
    asm volatile("s_barrier" ::: "memory");
    STAMP(0, 1);
    __builtin_amdgcn_s_waitcnt(0xc07f);
    K1_RD(0u, 0); K1_RD(0u, 1);
#pragma unroll 1
    for (int st = 0; st < 10; ++st) {
        const unsigned sl = (st & 3) * K1_STAGE, sn = ((st + 1) & 3) * K1_STAGE;
        K1_RD(sl, 2); K1_MM(0);
        K1_RD(sl, 3); K1_MM(1);
        if (st < 9) asm volatile("s_waitcnt lgkmcnt(0)\n\ts_barrier" ::: "memory");
        K1_RD(sn, 0); K1_MM(2);
        K1_RD(sn, 1); K1_MM(3);
    }
#undef K1_RD
#undef K1_MM
    STAMP(0, 3);
    asm volatile("s_barrier" ::: "memory");
    const lds_cptr aux = (lds_cptr)lds;
    const bf16x8 tfb = lds_r128(aux + K1_TF + wm * 1024 + lane * 16);
    bf16x8 ffb[NF];
#pragma unroll
    for (int f = 0; f < NF; ++f) ffb[f] = lds_r128(aux + K1_FF + (F0 + f) * 1024 + lane * 16);
    const float* rsl = (const float*)(lds + K1_RS) + wm * 32;
    float rs[VORI ? 16 : 1];
    if (VORI) {
#pragma unroll
        for (int g = 0; g < 4; ++g) { const f32x4 t = *(const f32x4*)(rsl + 8 * g + 4 * hh); rs[4 * g] = t[0]; rs[4 * g + 1] = t[1]; rs[4 * g + 2] = t[2]; rs[4 * g + 3] = t[3]; }
    } else rs[0] = rsl[r] * (isq ? QSCALE : 1.f);
#pragma unroll
    for (int f = 0; f < NF; ++f) acc[f] = VORI ? mfma16(tfb, ffb[f], acc[f]) : mfma16(ffb[f], tfb, acc[f]);
    STAMP(0, 4);
#pragma unroll
    for (int f = 0; f < NF; ++f) {
        unsigned u[8];
#pragma unroll
        for (int i = 0; i < 8; ++i) u[i] = pk2(acc[f][2 * i] * rs[VORI ? 2 * i : 0], acc[f][2 * i + 1] * rs[VORI ? 2 * i + 1 : 0]);
        if (!VORI) {
            QST16(base + so[f][0], (u32x4{u[0], u[1], u[2], u[3]}));
            QST8(base + so[f][1], (u32x2{u[4], u[5]}));
            QST8(base + so[f][2], (u32x2{u[6], u[7]}));
        } else {
            char* p = base + so[f][0];
            QST16(p, (u32x4{u[0], u[1], u[2], u[3]}));
            QST16(p + 2 * VROW_B, (u32x4{u[4], u[5], u[6], u[7]}));
            if (so[f][1] == 19) {
                QST16(p + 16, (u32x4{ONES2, ONES2, ONES2, ONES2})); QST16(p + 2 * VROW_B + 16, (u32x4{ONES2, ONES2, ONES2, ONES2}));
                QST16(p + 32, (u32x4{0, 0, 0, 0})); QST16(p + 2 * VROW_B + 32, (u32x4{0, 0, 0, 0}));
            }
        }
    }
}

template <bool VORI>
__device__ __forceinline__ void k1_helper(const __bf16* __restrict__ x, const __bf16* __restrict__ w, char* __restrict__ ws, char* lds, int mt, int nt, int hw) {
    const int lane = threadIdx.x & 63, r = lane & 31, hh = lane >> 5;
    const unsigned lds0 = (unsigned)(uintptr_t)lds;
    const unsigned t_start = now_ticks();
    unsigned xo[4], wo[5];
#pragma unroll
    for (int i = 0; i < 4; ++i) {
        const int row = 8 * (4 * hw + i) + (lane >> 3);
        xo[i] = row * (D_ * 2) + (((lane & 7) ^ ((row >> 1) & 7)) * 16) - i * 1024;
    }
#pragma unroll
    for (int i = 0; i < 5; ++i) {
        const int idx = (5 * hw + i) * 64 + lane, krow = idx / 20, ch = idx - krow * 20;
        wo[i] = krow * (3 * D_ * 2) + ch * 16 - (i & 3) * 1024;
    }
    const char* xs = (const char*)x + (size_t)mt * 128 * (D_ * 2);
    const char* wsr = (const char*)w + nt * 320;
#define K1_M0(v) asm volatile("s_mov_b32 m0, %0\n\ts_nop 0" :: "s"(v) : "memory")
#define K1_DMA(vo, sp, imm) asm volatile("global_load_lds_dwordx4 %0, %1 offset:" #imm :: "v"(vo), "s"(sp) : "memory")
#define K1_PIECE(i, xp, wp, lb) do { \
        if ((i) == 0) { K1_M0((lb) + hw * 4096); K1_DMA(xo[0], xp, 0); } \
        if ((i) == 1) K1_DMA(xo[1], xp, 1024); \
        if ((i) == 2) K1_DMA(xo[2], xp, 2048); \
        if ((i) == 3) K1_DMA(xo[3], xp, 3072); \
        if ((i) == 4) { K1_M0((lb) + 16384 + hw * 5120); K1_DMA(wo[0], wp, 0); } \
        if ((i) == 5) K1_DMA(wo[1], wp, 1024); \
        if ((i) == 6) K1_DMA(wo[2], wp, 2048); \
        if ((i) == 7) K1_DMA(wo[3], wp, 3072); \
        if ((i) == 8) { K1_M0((lb) + 16384 + hw * 5120 + 4096); K1_DMA(wo[4], wp, 0); } \
        SBAR(); } while (0)
#pragma unroll 1
    for (int st = 0; st < 4; ++st) {
        const char* xp = xs + st * 128; const char* wp = wsr + (size_t)st * 64 * (3 * D_ * 2);
        const unsigned lb = lds0 + st * K1_STAGE;
#pragma unroll
        for (int i = 0; i < 9; ++i) K1_PIECE(i, xp, wp, lb);
    }
    WAIT_BAR(27);
#pragma unroll 1
    for (int st = 0; st < 7; ++st) {
        if (st) {
            const char* xp = xs + (st + 3) * 128; const char* wp = wsr + (size_t)(st + 3) * 64 * (3 * D_ * 2);
            const unsigned lb = lds0 + ((st + 3) & 3) * K1_STAGE;
#pragma unroll
            for (int i = 0; i < 9; ++i) K1_PIECE(i, xp, wp, lb);
        }
        WAIT_BAR(18);
    }
#undef K1_PIECE
#undef K1_DMA
#undef K1_M0
    const int tok = mt * 128 + 32 * hw + r;
    int col[2];
#pragma unroll
    for (int f = 0; f < 2; ++f) {
        const int t = f ? 0 : hw + 1;
        const int pc = VORI ? (r >> 2) : (int)((k1_perm(t) >> (4 * (r >> 2))) & 7);
        col[f] = nt * 160 + t * 32 + pc * 4 + (r & 3);
    }
    const u64* rt = (const u64*)(ws + WS_RT) + tok * 2 + hh;
    const u64* rc = (const u64*)(ws + WS_RC) + hh * (2 * 3 * D_);
    u64 rec[5];
#define K1_POLL() do { rec[0] = ld_agent(rt); rec[1] = ld_agent(rc + col[0]); rec[2] = ld_agent(rc + 3 * D_ + col[0]); rec[3] = ld_agent(rc + col[1]); rec[4] = ld_agent(rc + 3 * D_ + col[1]); } while (0)
    K1_POLL();
    WAIT_BAR(14);
    WAIT_BAR(5);
    bool ok = false;
#pragma unroll 1
    for (int it = 0; it < K1_SPIN; ++it) {
        WAIT_VM(0);
        const unsigned span = now_ticks() - t_start + 32u;
        bool good = true;
#pragma unroll
        for (int i = 0; i < 5; ++i) good = good && (unsigned)(rec[i] >> 32) - t_start <= span;
        if (__all(good)) { ok = true; break; }
        __builtin_amdgcn_s_sleep(8);
        K1_POLL();
    }
#undef K1_POLL
    float tv = __uint_as_float((unsigned)rec[0]);
    float cs0 = __uint_as_float((unsigned)rec[1]) + __uint_as_float((unsigned)rec[2]), cs1 = __uint_as_float((unsigned)rec[3]) + __uint_as_float((unsigned)rec[4]);
    if (__builtin_expect(!ok, 0)) {
        const u32x4* rowp = (const u32x4*)(x + (size_t)tok * D_) + hh * 8;
        float mu = 0.f;
#pragma unroll 1
        for (int pass = 0; pass < 2; ++pass) {
            float s1 = 0.f, s2 = 0.f, s3 = 0.f;
#pragma unroll 1
            for (int c = 0; c < 8; ++c) {
                float a0 = 0.f, a1 = 0.f;
#pragma unroll 1
                for (int i = 0; i < 5; ++i) {
                    const u32x4 u = rowp[c + 16 * i];
#pragma unroll
                    for (int j = 0; j < 4; ++j) {
                        if (pass == 0) a0 = dot2(u[j], ONES2, a0);
                        else { const float d0 = bf_lo(u[j]) - mu, d1 = bf_hi(u[j]) - mu; a0 = __builtin_fmaf(d0, d0, a0); a1 = __builtin_fmaf(d1, d1, a1); }
                    }
                }
                if (pass) a0 += a1;
                s1 = (c & 1) ? s1 + a0 : a0;
                if (c & 1) s2 = (c & 2) ? s2 + s1 : s1;
                if ((c & 3) == 3) s3 = (c & 4) ? s3 + s2 : s2;
            }
            float tot = swap32_add(s3) * (1.f / D_);
            PIN(tot);
            if (pass == 0) mu = tot; else tv = hh ? __builtin_amdgcn_rsqf(tot + EPS) : mu;
        }
        float q[2][2];
#pragma unroll
        for (int kq = 0; kq < 2; ++kq) {
            const unsigned short* wc = (const unsigned short*)w + (size_t)(2 * hh + kq) * 160 * (3 * D_);
            float t0[4] = {0.f, 0.f, 0.f, 0.f}, t1[4] = {0.f, 0.f, 0.f, 0.f};
#pragma unroll 1
            for (int wv = 0; wv < 8; ++wv) {
#pragma unroll
                for (int g = 0; g < 4; ++g) {
                    const unsigned short* rw = wc + (wv * 20 + g) * (3 * D_);
                    float c0 = bf_lo(rw[col[0]]), c1 = bf_lo(rw[col[1]]);
#pragma unroll
                    for (int i = 1; i < 5; ++i) { c0 += bf_lo(rw[4 * i * (3 * D_) + col[0]]); c1 += bf_lo(rw[4 * i * (3 * D_) + col[1]]); }
                    t0[g] += c0; t1[g] += c1;
                }
            }
            q[0][kq] = (t0[0] + t0[1]) + (t0[2] + t0[3]); q[1][kq] = (t1[0] + t1[1]) + (t1[2] + t1[3]);
        }
        cs0 = q[0][0] + q[0][1]; cs1 = q[1][0] + q[1][1];
    }
    {
        unsigned m1, m2, m3; split3(-tv, m1, m2, m3);
        u32x4 tf = {m1 | (m1 << 16), m2 | (m1 << 16), m2 | (m3 << 16), m2 | (m3 << 16)};
        if (hh) tf = u32x4{0, 0, 0, 0};
        *(u32x4*)(lds + K1_TF + hw * 1024 + lane * 16) = tf;
        unsigned a1, a2, a3; split3(swap32_add(cs0), a1, a2, a3);
        *(u32x4*)(lds + K1_FF + (hw + 1) * 1024 + lane * 16) = u32x4{a1 | (a2 << 16), a1 | (a3 << 16), a2 | (a1 << 16), a3 | (a2 << 16)};
        if (hw == 0) {
            split3(swap32_add(cs1), a1, a2, a3);
            *(u32x4*)(lds + K1_FF + lane * 16) = u32x4{a1 | (a2 << 16), a1 | (a3 << 16), a2 | (a1 << 16), a3 | (a2 << 16)};
        }
        if (hh) ((float*)(lds + K1_RS))[32 * hw + r] = tv;
    }
    asm volatile("s_waitcnt lgkmcnt(0)\n\ts_barrier" ::: "memory");
}

struct K1Stat { u32x4 xr[5], wr[5]; };
__device__ __forceinline__ void k1_stat_load(K1Stat& sv, const __bf16* __restrict__ x, const __bf16* __restrict__ w, char* __restrict__ ws, int job) {
    const int tid = threadIdx.x, lane = tid & 63, wid = tid >> 6;
    const u32x4* xp = (const u32x4*)(x + (size_t)(job * 32 + wid * 4 + (lane >> 4)) * D_) + (lane & 15);
    const int jc = job < 60 ? job : 59;
    const u32x4* wp = (const u32x4*)(w + (size_t)((jc / 15) * 160 + wid * 20 + (lane >> 4)) * (3 * D_) + (jc % 15) * 128) + (lane & 15);
#pragma unroll
    for (int i = 0; i < 5; ++i) sv.xr[i] = xp[16 * i];
#pragma unroll
    for (int i = 0; i < 5; ++i) sv.wr[i] = wp[(size_t)i * 4 * (3 * D_ / 8)];
}
__device__ __forceinline__ float red16(float v) {
    v += __shfl_xor(v, 1); v += __shfl_xor(v, 2); v += __shfl_xor(v, 4); v += __shfl_xor(v, 8);
    return v;
}
__device__ __forceinline__ void k1_stat(const K1Stat& sv, char* __restrict__ ws, char* lds, int job) {
    const int tid = threadIdx.x, lane = tid & 63, wid = tid >> 6;
    float a = 0.f;
#pragma unroll
    for (int i = 0; i < 5; ++i)
#pragma unroll
        for (int j = 0; j < 4; ++j) a = dot2(sv.xr[i][j], ONES2, a);
    float mu = red16(a) * (1.f / D_);
    PIN(mu);
    float a0 = 0.f, a1 = 0.f;
#pragma unroll
    for (int i = 0; i < 5; ++i)
#pragma unroll
        for (int j = 0; j < 4; ++j) { const float d0 = bf_lo(sv.xr[i][j]) - mu, d1 = bf_hi(sv.xr[i][j]) - mu; a0 = __builtin_fmaf(d0, d0, a0); a1 = __builtin_fmaf(d1, d1, a1); }
    float var = red16(a0 + a1) * (1.f / D_);
    PIN(var);
    const int tok = job * 32 + wid * 4 + (lane >> 4), c = lane & 15;
    float pub = c ? __builtin_amdgcn_rsqf(var + EPS) : mu;
    PIN(pub);
    if (c < 2) st_agent((u64*)(ws + WS_RT) + tok * 2 + c, pub, now_ticks());
    if (c == 2) {
        const float sig2 = __builtin_sqrtf(var * (1.f + EPS) + EPS * EPS);
        *(f32x4*)(ws + WS_TOK + (size_t)tok * 16) = f32x4{mu, 1.0f / sig2, sig2, 0.f};
    }
    float cs[8];
#pragma unroll
    for (int j = 0; j < 4; ++j) {
        cs[2 * j] = bf_lo(sv.wr[0][j]); cs[2 * j + 1] = bf_hi(sv.wr[0][j]);
#pragma unroll
        for (int i = 1; i < 5; ++i) { cs[2 * j] += bf_lo(sv.wr[i][j]); cs[2 * j + 1] += bf_hi(sv.wr[i][j]); }
    }
    float* red = (float*)(lds + 86016);
    *(f32x4*)(red + (wid * 4 + (lane >> 4)) * 128 + c * 8) = f32x4{cs[0], cs[1], cs[2], cs[3]};
    *(f32x4*)(red + (wid * 4 + (lane >> 4)) * 128 + c * 8 + 4) = f32x4{cs[4], cs[5], cs[6], cs[7]};
    asm volatile("s_waitcnt lgkmcnt(0)\n\ts_barrier" ::: "memory");
    if (tid < 128 && job < 60) {
        float t[4] = {0.f, 0.f, 0.f, 0.f};
#pragma unroll
        for (int i = 0; i < 32; ++i) t[i & 3] += red[i * 128 + tid];
        st_agent((u64*)(ws + WS_RC) + (job / 15) * (3 * D_) + (job % 15) * 128 + tid, (t[0] + t[1]) + (t[2] + t[3]), now_ticks());
    }
}

template <int KIND>
__device__ __forceinline__ void k1_pack(const __bf16* __restrict__ wsrc, char* __restrict__ ws, char* lds, int strip, int third, const K1Stat& sv, int job) {
    constexpr int NI = KIND == 1 ? 8 : 10, NKS = KIND == 1 ? 4 : 5, ROWB = (KIND == 0 ? F_ : D_) * 2;
    const int tid = threadIdx.x, lane = tid & 63, r = lane & 31, hh = lane >> 5;
    const int wid = __builtin_amdgcn_readfirstlane(tid >> 6);
    const unsigned lds0 = (unsigned)(uintptr_t)lds + wid * 10240;
    const int row0 = third * 512 + wid * NI * 8;
    const char* src = (const char*)wsrc + (size_t)(row0 + (lane >> 3)) * ROWB + strip * 128 + (((lane & 7) ^ (((lane >> 4) & 1) * 4)) * 16);
#pragma unroll
    for (int i = 0; i < NI; ++i) glds16(src + (size_t)i * 8 * ROWB, lds0 + i * 1024);
    const int q4 = (lane & 15) >> 2, p4 = lane & 3, gi = (lane >> 4) & 1;
    int pc = 4 * gi + p4;
    if (KIND != 0) pc = (0x75643120u >> (4 * pc)) & 7;
    const lds_cptr sb = (lds_cptr)lds + wid * 10240;
    float cs[2] = {0.f, 0.f};
    k1_stat(sv, ws, lds, job);
    WAIT_VM(0);
#pragma unroll
    for (int ks = 0; ks < NKS; ++ks) {
        int ra, rb;
        if (KIND == 0) { ra = 16 * ks + 8 * hh; rb = ra + 4; }
        else if (KIND == 1) { ra = 16 * ks + 4 * hh; rb = ra + 8; }
        else if (ks < 4) { ra = 20 * ks + 4 * hh; rb = ra + 8; }
        else { ra = 40 * hh + 16; rb = ra + 20; }
#pragma unroll
        for (int f = 0; f < 2; ++f) {
            const int co = ((((f * 8 + pc) >> 1) ^ ((q4 >> 1) * 4)) * 16) + (pc & 1) * 8;
            const bf16x8 fr = cat8(lds_tr(sb + (ra + q4) * 128 + co), lds_tr(sb + (rb + q4) * 128 + co));
            const int ft = strip * 2 + f;
            size_t dst;
            if (KIND == 0) dst = WS_PW + ((size_t)(ft / 3) * 240 + (wid * 5 + ks) * 3 + ft % 3) * 1024;
            else if (KIND == 1) { const int g = third * 32 + wid * 4 + ks; const int f5 = ft % 5; dst = WS_PW + ((size_t)((g / 24) * 4 + ft / 5) * 240 + (f5 < 3 ? 120 + (g % 24) * 3 + f5 : 192 + (g % 24) * 2 + f5 - 3)) * 1024; }
            else dst = WS_PWO + ((size_t)ft * 40 + (ks < 4 ? wid * 4 + ks : 32 + wid)) * 1024;
            PST16(ws + dst + lane * 16, __builtin_bit_cast(u32x4, fr));
            if (KIND == 0) {
                const u32x4 a = __builtin_bit_cast(u32x4, fr);
#pragma unroll
                for (int i = 0; i < 4; ++i) cs[f] = dot2(a[i], ONES2, cs[f]);
            }
        }
    }
    if (KIND == 0) {
        float* red = (float*)(lds + 81920);
        red[(wid * 2 + hh) * 64 + r] = cs[0];
        red[(wid * 2 + hh) * 64 + 32 + r] = cs[1];
        __syncthreads();
        if (tid < 64) {
            float s = 0.f;
#pragma unroll
            for (int i = 0; i < 16; ++i) s += red[i * 64 + tid];
            ((float*)(ws + WS_CS1))[strip * 64 + tid] = s;
        }
    }
}

__global__ __launch_bounds__(768, 3) void k1_qkv(const __bf16* __restrict__ x, const __bf16* __restrict__ wqkv, const __bf16* __restrict__ wo, const __bf16* __restrict__ w1,
                                                 const __bf16* __restrict__ w2, char* __restrict__ ws) {
    extern __shared__ __attribute__((aligned(16))) char lds[];
    const int b = blockIdx.x, xcd = b & 7, idx = b >> 3;
    const int wid = __builtin_amdgcn_readfirstlane(threadIdx.x >> 6);
    if (idx >= 24) {
        if (wid >= 8) {
            const int part = (idx - 24) * 4 + wid - 8;
            if ((CPF & 1) && part < 4) { uint64_t a; CODE_ADDR(a, K1_SYM); unsigned v = code_touch(a, part, K1_CODE); CODE_DONE(v); }
            else if ((CPF & 2) && part >= 4 && part < 7) { uint64_t a; CODE_ADDR(a, K2_SYM); unsigned v = code_touch(a, part - 4, K2_CODE); CODE_DONE(v); }
            return;
        }
        const int job = (idx - 24) * 8 + xcd;
        if (threadIdx.x < 2) __hip_atomic_store((unsigned*)(ws + WS_CNT + job * 256 + threadIdx.x * 128), 0u, __ATOMIC_RELAXED, __HIP_MEMORY_SCOPE_AGENT);
        K1Stat sv;
        k1_stat_load(sv, x, wqkv, ws, job);
        if (job < 24) k1_pack<0>(w1, ws, lds, job, 0, sv, job);
        else if (job < 54) k1_pack<1>(w2, ws, lds, (job - 24) % 10, (job - 24) / 10, sv, job);
        else k1_pack<2>(wo, ws, lds, job - 54, 0, sv, job);
        STAMP(3, 0);
        return;
    }
    const int mt = (xcd >> 1) * 4 + (idx & 3), nt = (xcd & 1) * 6 + (idx >> 2);
    if (wid >= 8) { if (nt < 8) k1_helper<false>(x, wqkv, ws, lds, mt, nt, wid - 8); else k1_helper<true>(x, wqkv, ws, lds, mt, nt, wid - 8); return; }
    if (nt < 8) { if (wid >= 4) k1_body<false, 1>(ws, lds, mt, nt); else k1_body<false, 0>(ws, lds, mt, nt); }
    else { if (wid >= 4) k1_body<true, 1>(ws, lds, mt, nt); else k1_body<true, 0>(ws, lds, mt, nt); }
    STAMP(0, 5);
}

#define MLP_NB 20
#define AP_HI 2
#define AP_MID 1
#define AP_LO 0
#define GELU_PRIO 1
#define MLP_SLEEP 0
#define AW 7
#define MLP_PRIO 0
template <int N> __device__ __forceinline__ void wait_vm() { asm volatile("s_waitcnt vmcnt(%0)" :: "n"(N) : "memory"); }
#define WAIT_VMX(N) wait_vm<(N)>()
constexpr int AT_SLOT = 8 * KVT_B;
constexpr int K2_X = 4 * AT_SLOT, K2_H = K2_X + 40960, K2_CNT = K2_H + 24576, K2_LDS = K2_CNT + 128;
constexpr int C_FULL = 0, C_FREE = 64, C_X = 112, C_H = 116, C_PRDY = 120, C_MDONE = 124;

__device__ __forceinline__ void attn_slow(const char* __restrict__ kv, bf16x8 qf, s16x4 qt, f32x16& o, int r, int hh) {
    float m = -INFINITY;
    o = f32x16{};
    if (hh) qt = s16x4{0, 0, 0, 0};
    for (int t = 0; t < 64; ++t) {
        const char* p = kv + (size_t)t * KVT_B;
        const bf16x8 kf = *(const bf16x8*)(p + hh * 512 + r * 16);
        const s16x4 kt = *(const s16x4*)(p + 1024 + r * 8);
        const int rv = r < 20 ? r : 20;
        const bf16x8 v0 = *(const bf16x8*)(p + 1280 + hh * VROW_B + rv * 16);
        const bf16x8 v1 = *(const bf16x8*)(p + 1280 + 2 * VROW_B + hh * VROW_B + rv * 16);
        f32x16 s = mfma16(kf, qf, f32x16{});
        s = mfma8(kt, qt, s);
        float tm = s[0];
#pragma unroll
        for (int i = 1; i < 16; ++i) tm = __builtin_fmaxf(tm, s[i]);
        tm = swap32_max(tm);
        const float mn = __builtin_fmaxf(m, tm);
        const float al = __builtin_amdgcn_exp2f(m - mn);
        m = mn;
        u32x4 p0, p1;
#pragma unroll
        for (int i = 0; i < 4; ++i) {
            p0[i] = pk2(__builtin_amdgcn_exp2f(s[2 * i] - m), __builtin_amdgcn_exp2f(s[2 * i + 1] - m));
            p1[i] = pk2(__builtin_amdgcn_exp2f(s[8 + 2 * i] - m), __builtin_amdgcn_exp2f(s[8 + 2 * i + 1] - m));
        }
#pragma unroll
        for (int i = 0; i < 16; ++i) o[i] *= al;
        o = mfma16(v0, __builtin_bit_cast(bf16x8, p0), o);
        o = mfma16(v1, __builtin_bit_cast(bf16x8, p1), o);
    }
}

__device__ __forceinline__ void attn_finish(char* __restrict__ ws, const char* kvh, bf16x8 qf, s16x4 qt, f32x16 o, int qt_idx, int head, int lane) {
    const int r = lane & 31, hh = lane >> 5;
    float l = swap32_add(hh ? o[8] : 0.f);
    bool bad = !(l < 1e30f);
#pragma unroll
    for (int i = 0; i < 8; ++i) bad |= !(__builtin_fabsf(o[i]) < 3e38f);
#pragma unroll
    for (int i = 8; i < 12; ++i) bad |= (hh == 0) && !(__builtin_fabsf(o[i]) < 3e38f);
    if (__builtin_expect(__any(bad), 0)) {
        attn_slow(kvh, qf, qt, o, r, hh);
        l = swap32_add(hh ? o[8] : 0.f);
    }
    const float inv = 1.0f / l;
    char* ob = ws + WS_O + (size_t)qt_idx * 40 * 1024;
    st16(ob + head * 1024 + lane * 16, u32x4{pk2(o[0] * inv, o[1] * inv), pk2(o[2] * inv, o[3] * inv), pk2(o[4] * inv, o[5] * inv), pk2(o[6] * inv, o[7] * inv)});
    if (hh == 0) st8(ob + (32 + (head >> 2)) * 1024 + (head & 3) * 256 + r * 8, u32x2{pk2(o[8] * inv, o[9] * inv), pk2(o[10] * inv, o[11] * inv)});
}

#define K2_SPIN 400000
#define AP_CUT 8
#define TAILP 0
#define TAIL_PD 6
#define WO_PRE 12
__device__ __forceinline__ int fin_tt() { return (blockIdx.x & 7) * 8 + (blockIdx.x >> 5); }
__device__ __forceinline__ int fin_ft(int f) { return ((blockIdx.x >> 3) & 3) * 5 + f; }
__device__ __forceinline__ int wo_frag(int kq, int k) { return k < 16 ? kq * 16 + k : 32 + kq * 4 + (k - 16); }
#define BAR() asm volatile("s_waitcnt lgkmcnt(0)\n\ts_barrier" ::: "memory")
__device__ __forceinline__ void k2_count(char* ws, int off) {
    if ((threadIdx.x & 63) == 0) __hip_atomic_fetch_add((unsigned*)(ws + WS_CNT + off), 1u, __ATOMIC_RELAXED, __HIP_MEMORY_SCOPE_AGENT);
}
__device__ __forceinline__ void k2_wait(const char* ws, int off, unsigned target) {
    const unsigned* c = (const unsigned*)(ws + WS_CNT + off);
#pragma unroll 1
    for (int it = 0; it < K2_SPIN; ++it) {
        if (__builtin_amdgcn_readfirstlane(__hip_atomic_load(c, __ATOMIC_RELAXED, __HIP_MEMORY_SCOPE_AGENT)) >= target) break;
        __builtin_amdgcn_s_sleep(1);
    }
    asm volatile("" ::: "memory");
}
constexpr int T_O = 0, T_RED = 40960, T_SUM = K2_X;
__device__ __forceinline__ void k2_gather(const __bf16* __restrict__ x, const __bf16* __restrict__ b2, char* ws, char* lds, int w) {
    const int lane = threadIdx.x & 63, r = lane & 31, hh = lane >> 5;
    const unsigned cnt = (unsigned)(uintptr_t)lds + K2_CNT;
    const int tt = fin_tt(), c0 = fin_ft(0) * 32 + hh * 8;
    u32x4 xv[3], bv[3], pv[3][4];
#pragma unroll
    for (int i = 0; i < 3; ++i) {
        const int u = w + 4 * i < 10 ? w + 4 * i : w;
        xv[i] = *(const u32x4*)(x + (size_t)(tt * 32 + r) * D_ + c0 + u * 16); bv[i] = *(const u32x4*)(b2 + c0 + u * 16);
    }
    SBAR();
    if (w == 0) { k2_wait(ws, tt * 256 + 128, 4u); lds_signal(cnt + C_PRDY); }
    else lds_await(cnt + C_PRDY, lds_peek(cnt + C_PRDY), 1);
    const __amdgpu_buffer_rsrc_t rp = __builtin_amdgcn_make_buffer_rsrc(ws + WS_PART + ((size_t)tt * 20 + fin_ft(0)) * 2048, 0, 3 * 64 * 20 * 2048 + 10240, 0x00020000);
#pragma unroll
    for (int i = 0; i < 3; ++i) {
        const int u = w + 4 * i < 10 ? w + 4 * i : w;
#pragma unroll
        for (int k = 0; k < 4; ++k) pv[i][k] = __builtin_amdgcn_raw_buffer_load_b128(rp, lane * 16, k * (64 * 20 * 2048) + u * 1024, 16);
    }
    SBAR();
#pragma unroll
    for (int i = 0; i < 3; ++i) {
        const int u = w + 4 * i < 10 ? w + 4 * i : w;
        float m[8];
#pragma unroll
        for (int k = 0; k < 4; ++k) {
            m[2 * k] = (bf_lo(xv[i][k]) + bf_lo(bv[i][k])) + ((bf_lo(pv[i][0][k]) + bf_lo(pv[i][1][k])) + (bf_lo(pv[i][2][k]) + bf_lo(pv[i][3][k])));
            m[2 * k + 1] = (bf_hi(xv[i][k]) + bf_hi(bv[i][k])) + ((bf_hi(pv[i][0][k]) + bf_hi(pv[i][1][k])) + (bf_hi(pv[i][2][k]) + bf_hi(pv[i][3][k])));
        }
        f32x4* d = (f32x4*)(lds + T_SUM + u * 2048) + lane;
        d[0] = f32x4{m[0], m[1], m[2], m[3]}; d[64] = f32x4{m[4], m[5], m[6], m[7]};
    }
    STAMPW(2, 11, 8);
}
__device__ __forceinline__ void k2_tail(char* ws, char* lds, __bf16* __restrict__ out, int f, int kq, const bf16x8 (&wf)[20]) {
    const int lane = threadIdx.x & 63, r = lane & 31, hh = lane >> 5;
    const int tt = fin_tt(), ft = fin_ft(f), g = kq * 5 + f;
    const __amdgpu_buffer_rsrc_t ro = __builtin_amdgcn_make_buffer_rsrc(ws + WS_O + ((size_t)tt * 40 + g * 4) * 1024, 0, 4096, 0x00020000);
    u32x4 ov[4];
    STAMP(4, 0);
    BAR();
    STAMP(4, 1);
#pragma unroll
    for (int k = 0; k < 4; ++k) ov[k] = __builtin_amdgcn_raw_buffer_load_b128(ro, lane * 16 + k * 1024, 0, 16);
    SBAR();
    const f32x4* sp = (const f32x4*)(lds + T_SUM + (f * 2 + kq) * 2048) + lane;
    const f32x4 s0 = sp[0], s1 = sp[64];
    SBAR();
#pragma unroll
    for (int k = 0; k < 4; ++k) *(u32x4*)(lds + T_O + (g * 4 + k) * 1024 + lane * 16) = ov[k];
    BAR();
    STAMP(4, 2);
    f32x16 acc = f32x16{};
    const lds_cptr ob = (lds_cptr)lds + T_O + kq * 16384 + lane * 16, ot = (lds_cptr)lds + T_O + (32 + kq * 4) * 1024 + hh * 512 + r * 8;
    constexpr int PD = TAIL_PD;
    bf16x8 of[PD];
#define OFRAG(k) ((k) < 16 ? lds_r128(ob + (k) * 1024) : cat8(lds_r64(ot + ((k) - 16) * 1024), lds_r64(ot + ((k) - 16) * 1024 + 256)))
#pragma unroll
    for (int k = 0; k < PD; ++k) of[k] = OFRAG(k);
    SBAR();
#pragma unroll
    for (int k = 0; k < 20; ++k) {
        acc = mfma16(wf[k], of[k % PD], acc);
        SBAR();
        if (k + PD < 20) of[k % PD] = OFRAG(k + PD);
        SBAR();
    }
#undef OFRAG
    STAMP(4, 3);
    f32x4* red = (f32x4*)(lds + T_RED + f * 4096);
    {
        const int c = 1 - kq;
        red[(2 * c) * 64 + lane] = kq ? f32x4{acc[0], acc[1], acc[2], acc[3]} : f32x4{acc[8], acc[9], acc[10], acc[11]};
        red[(2 * c + 1) * 64 + lane] = kq ? f32x4{acc[4], acc[5], acc[6], acc[7]} : f32x4{acc[12], acc[13], acc[14], acc[15]};
    }
    float m[8];
#pragma unroll
    for (int k = 0; k < 8; ++k) m[k] = (kq ? acc[8 + k] : acc[k]) + (k < 4 ? s0[k] : s1[k - 4]);
    BAR();
    {
        const f32x4 a = red[(2 * kq) * 64 + lane], bb = red[(2 * kq + 1) * 64 + lane];
        u32x4 u;
        u[0] = pk2(a[0] + m[0], a[1] + m[1]); u[1] = pk2(a[2] + m[2], a[3] + m[3]);
        u[2] = pk2(bb[0] + m[4], bb[1] + m[5]); u[3] = pk2(bb[2] + m[6], bb[3] + m[7]);
        *(u32x4*)(out + (size_t)(tt * 32 + r) * D_ + ft * 32 + kq * 16 + hh * 8) = u;
    }
    STAMP(4, 4);
}

__device__ __forceinline__ void k2_attn(char* ws, char* lds, __bf16* __restrict__ out, int wid, int head, int qb) {
    const int lane = threadIdx.x & 63, r = lane & 31, hh = lane >> 5;
    const unsigned lds0 = (unsigned)(uintptr_t)lds, cnt = lds0 + K2_CNT;
    const int qt_idx = qb * 8 + wid;
    const char* kvh = ws + WS_KV + (size_t)head * 64 * KVT_B;
    const char* src = kvh + wid * KVT_B + lane * 16;
    auto tile_issue = [&](int c) {
        const unsigned base = lds0 + (c & 3) * AT_SLOT + wid * KVT_B;
        glds16(src + c * AT_SLOT, base);
        glds16(src + c * AT_SLOT + 1024, base + 1024);
        if (lane < (KVT_B - 2048) / 16) glds16(src + c * AT_SLOT + 2048, base + 2048);
    };
    tile_issue(0);
    {
        const char* qp = ws + WS_Q + ((size_t)head * 64 + qt_idx) * QT_B + lane * 16;
        glds16(qp, lds0 + K2_H + wid * QT_B);
        if (lane < 16) glds16(qp + 1024, lds0 + K2_H + wid * QT_B + 1024);
    }
    if (wid == 0 && lane < 32) *(__attribute__((address_space(3))) unsigned*)(uintptr_t)(cnt + lane * 4) = lane == 8 ? 8u : 0u;
    asm volatile("s_waitcnt lgkmcnt(0)\n\ts_barrier" ::: "memory");
    const int koff = hh * 512 + r * 16, ktoff = hh ? 1600 : 1024 + r * 8, voff = 1280 + hh * VROW_B + (r < 21 ? r : 21) * 16;
    WAIT_VM(0);
    const lds_cptr qp = (lds_cptr)lds + K2_H + wid * QT_B;
    bf16x8 qf = lds_r128(qp + koff);
    s16x4 qt = lds_r64(qp + 1024 + r * 8);
    asm volatile("s_waitcnt lgkmcnt(0)" : "+v"(qf), "+v"(qt) :: "memory");
    lds_signal(cnt + C_FULL);
    tile_issue(1); tile_issue(2); tile_issue(3);
    lds_await(cnt + C_FULL, lds_peek(cnt + C_FULL), 8);
    STAMPW(1, 1, AW);
    f32x16 o = f32x16{}, sA, sB;
    u32x4 pA0, pA1, pB0 = {0, 0, 0, 0}, pB1 = {0, 0, 0, 0};
    bf16x8 kf, v0, v1; s16x4 kt;
    {
        const lds_cptr sb = (lds_cptr)lds;
        kf = lds_r128(sb + koff); kt = lds_r64(sb + ktoff);
        if (hh) qt = s16x4{0, 0, 0, 0};
        sA = mfma16(kf, qf, f32x16{});
        sA = mfma8(kt, qt, sA);
        float m0 = sA[0];
#pragma unroll
        for (int i = 1; i < 16; ++i) m0 = __builtin_fmaxf(m0, sA[i]);
        m0 = swap32_max(m0);
        const unsigned nm = pk2(-m0, 0.f) & 0xffffu;
        if (hh) qt[0] = (short)nm;
#pragma unroll
        for (int i = 0; i < 16; ++i) sA[i] += bf_lo(nm);
        kf = lds_r128(sb + KVT_B + koff); kt = lds_r64(sb + KVT_B + ktoff);
        v0 = lds_r128(sb + voff); v1 = v0;
    }
#define EX(v) __builtin_amdgcn_exp2f(v)
#define GRP(MF, SC, i, PW, j) do { MF; SBAR(); const float e0 = EX(SC[i]), e1 = EX(SC[i + 1]), e2 = EX(SC[i + 2]), e3 = EX(SC[i + 3]); PW[j] = pk2(e0, e1); PW[j + 1] = pk2(e2, e3); PIN(PW); SBAR(); } while (0)
#define STEP(SC, SN, PC0, PC1, PP0, PP1, KP, VP) do { \
        GRP(SN = mfma16(kf, qf, f32x16{}), SC, 0, PC0, 0); \
        kf = lds_r128((KP) + koff); SBAR(); \
        GRP(o = mfma16(v0, __builtin_bit_cast(bf16x8, PP0), o), SC, 4, PC0, 2); \
        v0 = lds_r128((VP) + voff); SBAR(); \
        GRP(SN = mfma8(kt, qt, SN), SC, 8, PC1, 0); \
        kt = lds_r64((KP) + ktoff); SBAR(); \
        GRP(o = mfma16(v1, __builtin_bit_cast(bf16x8, PP1), o), SC, 12, PC1, 2); \
        v1 = lds_r128((VP) + voff + 2 * VROW_B); SBAR(); \
    } while (0)
    if (wid >= 4) __builtin_amdgcn_s_setprio(AP_MID);
    for (int c = 0; c < 7; ++c) {
        const lds_cptr sb = (lds_cptr)lds + (c & 3) * AT_SLOT;
        const lds_cptr nb = (lds_cptr)lds + ((c + 1) & 3) * AT_SLOT;
        if (wid < 4) __builtin_amdgcn_s_setprio(AP_HI);
        STEP(sA, sB, pA0, pA1, pB0, pB1, sb + 2 * KVT_B, sb + 0 * KVT_B);
        STEP(sB, sA, pB0, pB1, pA0, pA1, sb + 3 * KVT_B, sb + 1 * KVT_B);
        STEP(sA, sB, pA0, pA1, pB0, pB1, sb + 4 * KVT_B, sb + 2 * KVT_B);
        if (c == 0) { WAIT_VM(6); lds_signal(cnt + C_FULL + 4); }
        STEP(sB, sA, pB0, pB1, pA0, pA1, sb + 5 * KVT_B, sb + 3 * KVT_B);
        STEP(sA, sB, pA0, pA1, pB0, pB1, sb + 6 * KVT_B, sb + 4 * KVT_B);
        if (wid < 4 && c < AP_CUT) __builtin_amdgcn_s_setprio(AP_LO);
        const unsigned pk = lds_peek(cnt + C_FULL + 4 * (c + 1)), pf = lds_peek(cnt + C_FREE + 4 * c - 4);
        SBAR();
        STEP(sB, sA, pB0, pB1, pA0, pA1, sb + 7 * KVT_B, sb + 5 * KVT_B);
        lds_await(cnt + C_FULL + 4 * (c + 1), pk, 8);
        if (c <= 4) {
            if (c >= 1) { lds_await(cnt + C_FREE + 4 * c - 4, pf, 8); tile_issue(c + 3); }
            WAIT_VM(3);
        } else WAIT_VM(0);
        if (c <= 5) lds_signal(cnt + C_FULL + 4 * (c + 2));
        SBAR();
        STEP(sA, sB, pA0, pA1, pB0, pB1, nb + 0 * KVT_B, sb + 6 * KVT_B);
        STEP(sB, sA, pB0, pB1, pA0, pA1, nb + 1 * KVT_B, sb + 7 * KVT_B);
        lds_signal(cnt + C_FREE + 4 * c);
        STAMPW(1, 2 + c, AW);
    }
    const int tf = 2 + (wid >> 1), tkq = wid & 1;
    const char* wop = ws + WS_PWO + (size_t)fin_ft(tf < 5 ? tf : 4) * 40 * 1024 + lane * 16;
    bf16x8 wf[20];
#pragma unroll
    for (int k = 0; k < WO_PRE; ++k) { wf[k] = *(const bf16x8*)(wop + (wid < 6 ? wo_frag(tkq, k) * 1024 : 0)); SBAR(); }
    {
        const lds_cptr sb = (lds_cptr)lds + 3 * AT_SLOT;
        if (wid < 4) __builtin_amdgcn_s_setprio(AP_HI);
        STEP(sA, sB, pA0, pA1, pB0, pB1, sb + 2 * KVT_B, sb + 0 * KVT_B);
        STEP(sB, sA, pB0, pB1, pA0, pA1, sb + 3 * KVT_B, sb + 1 * KVT_B);
        STEP(sA, sB, pA0, pA1, pB0, pB1, sb + 4 * KVT_B, sb + 2 * KVT_B);
        STEP(sB, sA, pB0, pB1, pA0, pA1, sb + 5 * KVT_B, sb + 3 * KVT_B);
        STEP(sA, sB, pA0, pA1, pB0, pB1, sb + 6 * KVT_B, sb + 4 * KVT_B);
        if (wid < 4 && 7 < AP_CUT) __builtin_amdgcn_s_setprio(AP_LO);
        STEP(sB, sA, pB0, pB1, pA0, pA1, sb + 7 * KVT_B, sb + 5 * KVT_B);
        STEP(sA, sB, pA0, pA1, pB0, pB1, sb + 7 * KVT_B, sb + 6 * KVT_B);
        STEP(sB, sA, pB0, pB1, pA0, pA1, sb + 7 * KVT_B, sb + 7 * KVT_B);
        STAMPW(1, 9, AW);
    }
    o = mfma16(v0, __builtin_bit_cast(bf16x8, pB0), o);
    o = mfma16(v1, __builtin_bit_cast(bf16x8, pB1), o);
#undef STEP
#undef GRP
#undef EX
    attn_finish(ws, kvh, qf, qt, o, qt_idx, head, lane);
    WAIT_VM(0);
    k2_count(ws, qt_idx * 256);
    STAMPW(1, 10, AW);
    __builtin_amdgcn_s_setprio(0);
    if (wid >= 6) {
        if (wid == 7) { k2_wait(ws, fin_tt() * 256, 32u); BAR(); }
        return;
    }
    SBAR();
#pragma unroll
    for (int k = WO_PRE; k < 20; ++k) { wf[k] = *(const bf16x8*)(wop + wo_frag(tkq, k) * 1024); SBAR(); }
    k2_tail(ws, lds, out, tf, tkq, wf);
}

constexpr int mlp_cnt(int i, int nb) {
    int n = nb - 1;
    if (192 <= i && i < 192 + nb) n += 6;
    return n;
}
__device__ __forceinline__ f32x2 pk_mul(f32x2 a, f32x2 b) { return a * b; }
__device__ __forceinline__ f32x2 pk_fma(f32x2 a, f32x2 b, f32x2 c) { return __builtin_elementwise_fma(a, b, c); }

__device__ __forceinline__ void k2_mlp(const __bf16* __restrict__ x, const __bf16* __restrict__ b1, const __bf16* __restrict__ b2, char* ws, char* lds, __bf16* __restrict__ out, int w, int j, int tt) {
    constexpr int NB = 20;
    const int lane = threadIdx.x & 63, r = lane & 31, hh = lane >> 5;
    const unsigned lds0 = (unsigned)(uintptr_t)lds, cnt = lds0 + K2_CNT;
    unsigned cv = 0;
    if ((CPF & 4) && blockIdx.x < 8 && w < 3) { uint64_t a; CODE_ADDR(a, K2_SYM); cv = code_touch(a, w, K2_CODE); }
    asm volatile("s_barrier" ::: "memory");
    __builtin_amdgcn_s_setprio(MLP_PRIO);
    lds_await(cnt + C_FULL, lds_peek(cnt + C_FULL), 8);
    if (CPF & 4) CODE_DONE(cv);
    const int l16 = lane * 16;
    f32x4 ts; float csv[3]; unsigned b1v[3];
    {
        const char* tp = ws + WS_TOK + (size_t)tt * 512;
        const char* cp = ws + WS_CS1 + (j * 384 + w * 96) * 4;
        const char* bp = (const char*)b1 + (j * 384 + w * 96) * 2;
        asm volatile("global_load_dwordx4 %0, %1, %2" : "=v"(ts) : "v"(r * 16), "s"(tp));
        asm volatile("global_load_dword %0, %3, %4\n\tglobal_load_dword %1, %3, %4 offset:128\n\tglobal_load_dword %2, %3, %4 offset:256" : "=&v"(csv[0]), "=&v"(csv[1]), "=&v"(csv[2]) : "v"(r * 4), "s"(cp));
        asm volatile("global_load_ushort %0, %3, %4\n\tglobal_load_ushort %1, %3, %4 offset:64\n\tglobal_load_ushort %2, %3, %4 offset:128" : "=&v"(b1v[0]), "=&v"(b1v[1]), "=&v"(b1v[2]) : "v"(r * 2), "s"(bp));
    }
    {
        const int row = 8 * w + (lane >> 3);
        const char* xs = (const char*)x + (size_t)(tt * 32 + row) * (D_ * 2) + (((lane & 7) ^ ((row >> 1) & 7)) * 16);
#pragma unroll
        for (int st = 0; st < 10; ++st) glds16(xs + st * 128, lds0 + K2_X + st * 4096 + w * 1024);
    }
    const uint64_t wbase = (uint64_t)(ws + WS_PW + (size_t)(j * 4 + w) * 240 * 1024);
    const u32x4 wr = {(unsigned)wbase, (unsigned)(wbase >> 32) & 0xffffu, 240u * 1024u, 0x00020000u};
    const uint64_t obase = (uint64_t)(ws + WS_PWO + (size_t)fin_ft(w >> 1) * 40 * 1024);
    const u32x4 wr2 = {(unsigned)obase, (unsigned)(obase >> 32) & 0xffffu, 40u * 1024u, 0x00020000u};
    const int okq = w & 1;
    bf16x8 wb[NB];
#define ABL_LIM 240
#define WLD(i) do { if ((i) < NB) asm volatile("buffer_load_dwordx4 %0, %1, %2, %3 offen offset:%4" : "=v"(wb[(i) % NB]) : "v"(l16), "s"(wr), "s"(((i) >> 2) * 4096), "n"(((i) & 3) * 1024)); \
        else if ((i) < ABL_LIM) asm volatile("buffer_load_dwordx4 %0, %1, %2, %3 offen offset:%4" : "+v"(wb[(i) % NB]) : "v"(l16), "s"(wr), "s"(((i) >> 2) * 4096), "n"(((i) & 3) * 1024)); \
        else if ((i) >= 240) asm volatile("buffer_load_dwordx4 %0, %1, %2, %3 offen offset:%4" : "+v"(wb[(i) % NB]) : "v"(l16), "s"(wr2), "s"(wo_frag(okq, ((i) - 240) & ~3) * 1024), "n"(((i) & 3) * 1024)); \
        SBAR(); } while (0)
#define WWAIT(i) asm volatile("s_waitcnt vmcnt(%1)" : "+v"(wb[(i) % NB]) : "n"(mlp_cnt(i, NB)))
#define MMA(i, b, c) ({ if (MLP_SLEEP) __builtin_amdgcn_s_sleep(MLP_SLEEP); WWAIT(i); mfma16(wb[(i) % NB], b, c); })
#pragma unroll
    for (int i = 0; i < NB; ++i) WLD(i);
    asm volatile("s_waitcnt vmcnt(%7)" : "+v"(ts), "+v"(csv[0]), "+v"(csv[1]), "+v"(csv[2]), "+v"(b1v[0]), "+v"(b1v[1]), "+v"(b1v[2]) : "n"(NB) : "memory");
    lds_signal(cnt + C_X);
    lds_await(cnt + C_X, lds_peek(cnt + C_X), 4);
    STAMPW(2, 1, 8);
#define SERVICE(NVM, cf, ci) STAMPW(2, (cf) - 1, 8)
    unsigned xa[4];
#pragma unroll
    for (int k = 0; k < 4; ++k) { xa[k] = lds0 + K2_X + r * 128 + (((2 * k + hh) ^ ((r >> 1) & 7)) * 16); PIN(xa[k]); }
    f32x16 acc[3]; acc[0] = f32x16{}; acc[1] = f32x16{}; acc[2] = f32x16{};
    bf16x8 xf[2];
    xf[0] = lds_r128((lds_cptr)(uintptr_t)xa[0]);
#pragma unroll
    for (int ks = 0; ks < 40; ++ks) {
        if (ks == 12) SERVICE(24, 3, 4);
        if (ks == 26) SERVICE(24, 4, 5);
        if (ks + 1 < 40) xf[(ks + 1) & 1] = lds_r128((lds_cptr)(uintptr_t)xa[(ks + 1) & 3] + ((ks + 1) >> 2) * 4096);
        SBAR();
#pragma unroll
        for (int f = 0; f < 3; ++f) {
            acc[f] = MMA(3 * ks + f, xf[ks & 1], acc[f]);
            SBAR();
            WLD(3 * ks + f + NB);
        }
        SBAR();
    }
    SERVICE(24, 5, 6);
    __builtin_amdgcn_s_setprio(GELU_PRIO);
    {
        unsigned m1, m2, m3; split3(hh ? ts[2] : -ts[0], m1, m2, m3);
        u32x4 tf = {m1 | (m1 << 16), m2 | (m1 << 16), m2 | (m3 << 16), m2 | (m3 << 16)};
        if (hh) tf = u32x4{m1 | (m2 << 16), m3, 0, 0};
#pragma unroll
        for (int f = 0; f < 3; ++f) {
            unsigned a1, a2, a3; split3(csv[f], a1, a2, a3);
            u32x4 ff = {a1 | (a2 << 16), a1 | (a3 << 16), a2 | (a1 << 16), a3 | (a2 << 16)};
            if (hh) ff = u32x4{b1v[f] | (b1v[f] << 16), b1v[f], 0, 0};
            acc[f] = mfma16(__builtin_bit_cast(bf16x8, ff), __builtin_bit_cast(bf16x8, tf), acc[f]);
            SBAR();
        }
    }
    {
        constexpr float C0 = -2.0f * 0.7978845608028654f * 1.4426950408889634f, C1 = C0 * 0.044715f;
        const float s2 = ts[1], sg = ts[2];
        const f32x2 k0 = {C0 * s2, C0 * s2}, k1 = {C1 * s2 * s2 * s2, C1 * s2 * s2 * s2}, sg2 = {sg, sg};
        __attribute__((address_space(3))) u32x4* hp = (__attribute__((address_space(3))) u32x4*)(uintptr_t)(lds0 + K2_H + w * 6144 + lane * 16);
#pragma unroll
        for (int f = 0; f < 3; ++f) {
#pragma unroll
            for (int s = 0; s < 2; ++s) {
                u32x4 u;
#pragma unroll
                for (int i = 0; i < 4; ++i) {
                    const f32x2 a = {acc[f][8 * s + 2 * i], acc[f][8 * s + 2 * i + 1]};
                    const f32x2 z = pk_mul(a, pk_fma(pk_mul(a, a), k1, k0));
                    const f32x2 e = {__builtin_amdgcn_exp2f(z[0]), __builtin_amdgcn_exp2f(z[1])};
                    const f32x2 d = pk_fma(e, sg2, sg2);
                    const f32x2 hv = pk_mul(a, f32x2{__builtin_amdgcn_rcpf(d[0]), __builtin_amdgcn_rcpf(d[1])});
                    u[i] = pk2(hv[0], hv[1]);
                }
                hp[(2 * f + s) * 64] = u;
                SBAR();
            }
        }
    }
    asm volatile("s_waitcnt lgkmcnt(0)" ::: "memory");
    lds_signal(cnt + C_H);
    __builtin_amdgcn_s_setprio(MLP_PRIO);
    lds_await(cnt + C_H, lds_peek(cnt + C_H), 4);
    STAMPW(2, 8, 8);
    unsigned ha = lds0 + K2_H + lane * 16;
    PIN(ha);
    const lds_cptr hb = (lds_cptr)(uintptr_t)ha;
    char* pp = ws + WS_PART + (((size_t)j * 64 + tt) * 20 + w * 5) * 2048 + lane * 16;
    auto store_tile = [&](const f32x16& t, int f) {
#pragma unroll
        for (int c = 0; c < 2; ++c) {
            float a[8];
#pragma unroll
            for (int i = 0; i < 8; ++i) a[i] = t[8 * c + i];
            st16(pp + (f * 2 + c) * 1024, u32x4{pk2(a[0], a[1]), pk2(a[2], a[3]), pk2(a[4], a[5]), pk2(a[6], a[7])});
            SBAR();
        }
    };
    acc[0] = f32x16{}; acc[1] = f32x16{}; acc[2] = f32x16{};
    xf[0] = lds_r128(hb);
#pragma unroll
    for (int ks = 0; ks < 24; ++ks) {
        if (ks == 10) SERVICE(24, 6, 7);
        xf[(ks + 1) & 1] = lds_r128(hb + ((ks + 1) % 24) * 1024);
        SBAR();
#pragma unroll
        for (int f = 0; f < 3; ++f) {
            acc[f] = MMA(120 + 3 * ks + f, xf[ks & 1], acc[f]);
            SBAR();
            WLD(120 + 3 * ks + f + NB);
        }
        SBAR();
    }
    SERVICE(24, 7, 8);
    store_tile(acc[0], 0); store_tile(acc[1], 1); store_tile(acc[2], 2);
    SBAR();
    f32x16 ac2[2]; ac2[0] = f32x16{}; ac2[1] = f32x16{};
#pragma unroll
    for (int ks = 0; ks < 24; ++ks) {
        if (ks + 1 < 24) xf[(ks + 1) & 1] = lds_r128(hb + (ks + 1) * 1024);
        SBAR();
#pragma unroll
        for (int f = 0; f < 2; ++f) {
            ac2[f] = MMA(192 + 2 * ks + f, xf[ks & 1], ac2[f]);
            SBAR();
            WLD(192 + 2 * ks + f + NB);
        }
        SBAR();
    }
    STAMPW(2, 9, 8);
#undef WLD
#undef SERVICE
    __builtin_amdgcn_s_setprio(TAILP);
    store_tile(ac2[0], 3); store_tile(ac2[1], 4);
    asm volatile("s_waitcnt vmcnt(0)" : "+v"(wb[0]), "+v"(wb[1]), "+v"(wb[2]), "+v"(wb[3]), "+v"(wb[4]), "+v"(wb[5]), "+v"(wb[6]), "+v"(wb[7]), "+v"(wb[8]), "+v"(wb[9]) :: "memory");
    asm volatile("" : "+v"(wb[10]), "+v"(wb[11]), "+v"(wb[12]), "+v"(wb[13]), "+v"(wb[14]), "+v"(wb[15]), "+v"(wb[16]), "+v"(wb[17]), "+v"(wb[18]), "+v"(wb[19]) :: "memory");
    {
        unsigned old = 0;
        if (lane == 0) old = __hip_atomic_fetch_add((__attribute__((address_space(3))) unsigned*)(uintptr_t)(cnt + C_MDONE), 1u, __ATOMIC_RELAXED, __HIP_MEMORY_SCOPE_WORKGROUP);
        if (__builtin_amdgcn_readfirstlane(old) == 3u) k2_count(ws, tt * 256 + 128);
    }
    STAMPW(2, 10, 8);
    k2_gather(x, b2, ws, lds, w);
    k2_tail(ws, lds, out, w >> 1, okq, wb);
}

__global__ __launch_bounds__(768, 3) void k2_main(const __bf16* __restrict__ x, const __bf16* __restrict__ b1, const __bf16* __restrict__ b2, char* ws, __bf16* __restrict__ out) {
    extern __shared__ __attribute__((aligned(16))) char lds[];
    const int wid = __builtin_amdgcn_readfirstlane(threadIdx.x >> 6);
    const int b = blockIdx.x, xcd = b & 7, head = xcd * 4 + ((b >> 3) & 3), qb = b >> 5;
    STAMPW(1, 0, AW);
    STAMPW(2, 0, 8);
    if (wid < 8) k2_attn(ws, lds, out, wid, head, qb);
    else k2_mlp(x, b1, b2, ws, lds, out, wid - 8, xcd & 3, (xcd >> 2) * 32 + (b >> 3));
}

}

extern "C" void kernel_launch(void* const* d_in, const int* in_sizes, int n_in, void* d_out, int out_size, void* d_ws, size_t ws_size, hipStream_t stream) {
    const __bf16* x = (const __bf16*)d_in[0];
    const __bf16* wqkv = (const __bf16*)d_in[1];
    const __bf16* wo = (const __bf16*)d_in[2];
    const __bf16* w1 = (const __bf16*)d_in[4];
    const __bf16* b1 = (const __bf16*)d_in[5];
    const __bf16* w2 = (const __bf16*)d_in[6];
    const __bf16* b2 = (const __bf16*)d_in[7];
    char* ws = (char*)d_ws;
    (void)hipFuncSetAttribute((const void*)k1_qkv, hipFuncAttributeMaxDynamicSharedMemorySize, K1_LDS);
    (void)hipFuncSetAttribute((const void*)k2_main, hipFuncAttributeMaxDynamicSharedMemorySize, K2_LDS);
    k1_qkv<<<256, 768, K1_LDS, stream>>>(x, wqkv, wo, w1, w2, ws);
    k2_main<<<256, 768, K2_LDS, stream>>>(x, b1, b2, ws, (__bf16*)d_out);
}
```

```cpp
#include <hip/hip_runtime.h>
#include <hip/hip_bf16.h>
#include <stdint.h>

namespace {

typedef __attribute__((ext_vector_type(8))) __bf16 bf16x8;
typedef __attribute__((ext_vector_type(2))) __bf16 bf16x2;
typedef __attribute__((ext_vector_type(8))) short s16x8;
typedef __attribute__((ext_vector_type(4))) short s16x4;
typedef __attribute__((ext_vector_type(16))) float f32x16;
typedef __attribute__((ext_vector_type(4))) float f32x4;
typedef __attribute__((ext_vector_type(2))) float f32x2;
typedef __attribute__((ext_vector_type(4))) unsigned u32x4;
typedef __attribute__((ext_vector_type(2))) unsigned u32x2;
typedef __attribute__((address_space(3))) char* lds_ptr;
typedef __attribute__((address_space(3))) const char* lds_cptr;

constexpr int S_ = 2048, D_ = 640, H_ = 32, F_ = 1536;
constexpr float EPS = 1e-5f;
constexpr float QSCALE = 0.22360679774997896f * 1.4426950408889634f;

constexpr int QT_B = 1280;
constexpr int VROW_B = 352;
constexpr int KVT_B = 1280 + 4 * VROW_B;
constexpr size_t WS_Q = 0;
constexpr size_t WS_KV = WS_Q + (size_t)H_ * 64 * QT_B;
constexpr size_t WS_TOK = WS_KV + (size_t)H_ * 64 * KVT_B + 4096;
constexpr size_t WS_CS1 = WS_TOK + (size_t)S_ * 16;
constexpr size_t WS_PW = WS_CS1 + 8192;
constexpr size_t WS_PWO = WS_PW + (size_t)16 * 240 * 1024;
constexpr size_t WS_PART = WS_PWO + (size_t)20 * 40 * 1024;
constexpr size_t WS_O = WS_PART + (size_t)4 * 64 * 20 * 2048;
constexpr size_t WS_RT = WS_O + (size_t)64 * 40 * 1024;
constexpr size_t WS_RC = WS_RT + (size_t)S_ * 16;
constexpr size_t WS_CNT = (WS_RC + (size_t)4 * 3 * D_ * 8 + 255) / 256 * 256;
constexpr size_t WS_END = WS_CNT + 64 * 256;

#define STAMPW(kid, i, w)
#define STAMP(kid, i) STAMPW(kid, i, 0)
#define SBAR() __builtin_amdgcn_sched_barrier(0)
#define PIN(x) asm volatile("" : "+v"(x))
#define WAIT_BAR(N) asm volatile("s_waitcnt vmcnt(" #N ") lgkmcnt(0)\n\ts_barrier" ::: "memory")
#define WAIT_VM(N) asm volatile("s_waitcnt vmcnt(" #N ")" ::: "memory")

__device__ __forceinline__ f32x16 mfma16(bf16x8 a, bf16x8 b, f32x16 c) { return __builtin_amdgcn_mfma_f32_32x32x16_bf16(a, b, c, 0, 0, 0); }
__device__ __forceinline__ f32x16 mfma8(s16x4 a, s16x4 b, f32x16 c) { return __builtin_amdgcn_mfma_f32_32x32x8bf16_1k(a, b, c, 0, 0, 0); }
__device__ __forceinline__ unsigned pk2(float lo, float hi) { f32x2 v = {lo, hi}; return __builtin_bit_cast(unsigned, __builtin_convertvector(v, bf16x2)); }
__device__ __forceinline__ float bf_lo(unsigned u) { return __uint_as_float(u << 16); }
__device__ __forceinline__ float bf_hi(unsigned u) { return __uint_as_float(u & 0xffff0000u); }
__device__ __forceinline__ void glds16(const void* g, unsigned lds_base) {
    unsigned sv;
    asm volatile("s_mov_b32 %0, m0\n\ts_mov_b32 m0, %2\n\ts_nop 0\n\tglobal_load_lds_dwordx4 %1, off\n\ts_mov_b32 m0, %0" : "=&s"(sv) : "v"(g), "s"(lds_base) : "memory");
}
__device__ __forceinline__ void glds4(const void* g, unsigned lds_base) {
    unsigned sv;
    asm volatile("s_mov_b32 %0, m0\n\ts_mov_b32 m0, %2\n\ts_nop 0\n\tglobal_load_lds_dword %1, off\n\ts_mov_b32 m0, %0" : "=&s"(sv) : "v"(g), "s"(lds_base) : "memory");
}
__device__ __forceinline__ bf16x8 lds_r128(lds_cptr p) { return *(const __attribute__((address_space(3))) bf16x8*)p; }
__device__ __forceinline__ s16x4 lds_r64(lds_cptr p) { return *(const __attribute__((address_space(3))) s16x4*)p; }
__device__ __forceinline__ s16x4 lds_tr(lds_cptr p) { return __builtin_amdgcn_ds_read_tr16_b64_v4i16((__attribute__((address_space(3))) s16x4*)p); }
__device__ __forceinline__ bf16x8 cat8(s16x4 a, s16x4 b) { s16x8 v = {a[0], a[1], a[2], a[3], b[0], b[1], b[2], b[3]}; return __builtin_bit_cast(bf16x8, v); }
__device__ __forceinline__ float dot2(unsigned a, unsigned b, float c) { return __builtin_amdgcn_fdot2_f32_bf16(__builtin_bit_cast(bf16x2, a), __builtin_bit_cast(bf16x2, b), c, false); }
__device__ __forceinline__ float swap32_add(float v) { auto rr = __builtin_amdgcn_permlane32_swap(__float_as_uint(v), __float_as_uint(v), false, false); return __uint_as_float(rr[0]) + __uint_as_float(rr[1]); }
__device__ __forceinline__ float swap32_max(float v) { auto rr = __builtin_amdgcn_permlane32_swap(__float_as_uint(v), __float_as_uint(v), false, false); return __builtin_fmaxf(__uint_as_float(rr[0]), __uint_as_float(rr[1])); }
__device__ __forceinline__ void split3(float v, unsigned& a, unsigned& b, unsigned& c) {
    a = pk2(v, 0.f) & 0xffffu; float r = v - bf_lo(a);
    b = pk2(r, 0.f) & 0xffffu; r = r - bf_lo(b);
    c = pk2(r, 0.f) & 0xffffu;
}
__device__ __forceinline__ void st16(void* p, u32x4 v) {
    asm volatile("global_store_dwordx4 %0, %1, off sc1\n\ts_nop 1" :: "v"(p), "v"(v) : "memory");
}
__device__ __forceinline__ void st8(void* p, u32x2 v) { asm volatile("global_store_dwordx2 %0, %1, off sc1\n\ts_nop 1" :: "v"(p), "v"(v) : "memory"); }
#define QST16(p, v) (*(u32x4*)(p) = (v))
#define QST8(p, v) (*(u32x2*)(p) = (v))
#define PST16(p, v) st16((p), (v))
constexpr unsigned ONES2 = 0x3f803f80u;
typedef unsigned long long u64;
__device__ __forceinline__ u64 ld_agent(const u64* p) { return __hip_atomic_load(p, __ATOMIC_RELAXED, __HIP_MEMORY_SCOPE_AGENT); }
__device__ __forceinline__ void st_agent(u64* p, float v, unsigned tag) { __hip_atomic_store(p, (u64)__float_as_uint(v) | ((u64)tag << 32), __ATOMIC_RELAXED, __HIP_MEMORY_SCOPE_AGENT); }
__device__ __forceinline__ unsigned now_ticks() { return (unsigned)__builtin_amdgcn_s_memrealtime(); }
#define K1_SPIN 4096
__device__ __forceinline__ void lds_signal(unsigned addr, unsigned val = 1u) {
    asm volatile("s_mov_b64 exec, 1\n\tds_add_u32 %0, %1\n\ts_mov_b64 exec, -1" :: "v"(addr), "v"(val) : "memory");
}
__device__ __forceinline__ unsigned lds_peek(unsigned addr) { return *(const volatile __attribute__((address_space(3))) unsigned*)(uintptr_t)addr; }
__device__ __forceinline__ void lds_await(unsigned addr, unsigned first, unsigned target) {
    unsigned v = __builtin_amdgcn_readfirstlane(first);
    while (v < target) { __builtin_amdgcn_s_sleep(1); v = __builtin_amdgcn_readfirstlane(lds_peek(addr)); }
    asm volatile("" ::: "memory");
}

#define K1_SYM "_ZN12_GLOBAL__N_16k1_qkvEPKDF16bS1_S1_S1_S1_Pc"
#define K2_SYM "_ZN12_GLOBAL__N_17k2_mainEPKDF16bS1_S1_PcPDF16b"
constexpr int K1_CODE = 32000, K2_CODE = 23000;
#define CODE_ADDR(a, SYM) asm volatile("s_getpc_b64 s[40:41]\n\ts_add_u32 s40, s40, " SYM "@rel32@lo+4\n\ts_addc_u32 s41, s41, " SYM "@rel32@hi+12\n\ts_mov_b64 %0, s[40:41]" : "=s"(a) :: "s40", "s41", "scc")
__device__ __forceinline__ unsigned code_touch(uint64_t base, int part, int size) {
    int off = part * 8192 + (int)(threadIdx.x & 63) * 128;
    off = off < size ? off : size;
    unsigned v;
    asm volatile("global_load_dword %0, %1, %2" : "=v"(v) : "v"(off), "s"(base) : "memory");
    return v;
}
#define CODE_DONE(v) asm volatile("s_waitcnt vmcnt(0)" : "+v"(v) :: "memory")
#define CPF 18

constexpr int K1_STAGE = 16384 + 20480, K1_AUX = 4 * K1_STAGE, K1_LDS = K1_AUX + 4096 + 5120 + 1024 + 256;

__device__ __forceinline__ constexpr unsigned k1_perm(int j) {
    return j == 0 ? 0x76453120u : (j == 3 ? 0x57064231u : (j == 4 ? 0x76254130u : 0x75643120u));
}
__device__ __forceinline__ int k1_piece_off(int P, int r, int& head) {
    head = P / 5; int rr = P - head * 5; const int h7 = head & 7;
    int role = rr;
    if (h7 == 1) role = (0x32410 >> (4 * rr)) & 7;
    if (h7 == 3) role = (0x32104 >> (4 * rr)) & 7;
    return role == 4 ? 1024 + r * 8 : (role >> 1) * 512 + r * 16 + (role & 1) * 8;
}

constexpr int K1_TF = K1_AUX, K1_FF = K1_AUX + 4096, K1_RS = K1_AUX + 9216;

template <bool VORI, int WN>
__device__ __forceinline__ void k1_body(char* __restrict__ ws, char* lds, int mt, int nt) {
    constexpr int NF = WN ? 2 : 3, F0 = WN ? 3 : 0;
    const int tid = threadIdx.x, lane = tid & 63, r = lane & 31, hh = lane >> 5;
    const int wid = __builtin_amdgcn_readfirstlane(tid >> 6), wm = wid & 3;
    const unsigned lds0 = (unsigned)(uintptr_t)lds;
    STAMP(0, 0);
    const int xrow = 32 * wm + r;
    const int q4 = (lane & 15) >> 2, p4 = lane & 3, gi = (lane >> 4) & 1;
    unsigned xa[4], wa[NF];
#pragma unroll
    for (int k = 0; k < 4; ++k) xa[k] = lds0 + xrow * 128 + (((2 * k + hh) ^ ((xrow >> 1) & 7)) * 16);
#pragma unroll
    for (int f = 0; f < NF; ++f) {
        int rel = 4 * gi + p4;
        if (!VORI) rel = (k1_perm(F0 + f) >> (4 * rel)) & 7;
        wa[f] = lds0 + 16384 + (8 * hh + q4) * 320 + (F0 + f) * 64 + rel * 8;
    }
    const int tt = mt * 4 + wm;
    const bool isq = nt < 4;
    char* const base = (!VORI && isq) ? ws + WS_Q + (size_t)tt * QT_B : ws + WS_KV + (size_t)tt * KVT_B;
    unsigned so[NF][3];
#pragma unroll
    for (int f = 0; f < NF; ++f) {
        if (!VORI) {
            const unsigned hstride = isq ? 64 * QT_B : 64 * KVT_B;
            const int T = (nt & 3) * 5 + F0 + f;
            const unsigned perm = k1_perm(F0 + f);
#pragma unroll
            for (int i = 0; i < 3; ++i) {
                int head;
                const int off = k1_piece_off(8 * T + ((perm >> (4 * ((i ? 2 + 2 * i : 0) + hh))) & 7), r, head);
                so[f][i] = head * hstride + off;
                PIN(so[f][i]);
            }
        } else {
            const int col = (nt - 8) * 160 + (F0 + f) * 32 + r;
            const int head = col / 20, d = col - head * 20;
            so[f][0] = head * (64 * KVT_B) + 1280 + hh * VROW_B + d * 16;
            so[f][1] = d;
            PIN(so[f][0]); PIN(so[f][1]);
        }
    }
    f32x16 acc[NF];
#pragma unroll
    for (int f = 0; f < NF; ++f) acc[f] = f32x16{};
    bf16x8 xf[4], wf[4][NF];
#define K1_RD(sl, ks) do { xf[ks] = lds_r128((lds_cptr)(uintptr_t)(xa[ks] + (sl))); \
        _Pragma("unroll") for (int f = 0; f < NF; ++f) { const lds_cptr p = (lds_cptr)(uintptr_t)(wa[f] + (sl)) + (ks) * 5120; wf[ks][f] = cat8(lds_tr(p), lds_tr(p + 1280)); } SBAR(); } while (0)
#define K1_MM(b) do { _Pragma("unroll") for (int f = 0; f < NF; ++f) acc[f] = VORI ? mfma16(xf[b], wf[b][f], acc[f]) : mfma16(wf[b][f], xf[b], acc[f]); SBAR(); } while (0)
    asm volatile("s_barrier" ::: "memory");
    STAMP(0, 1);
    __builtin_amdgcn_s_waitcnt(0xc07f);
    K1_RD(0u, 0); K1_RD(0u, 1);
#pragma unroll 1
    for (int st = 0; st < 10; ++st) {
        const unsigned sl = (st & 3) * K1_STAGE, sn = ((st + 1) & 3) * K1_STAGE;
        K1_RD(sl, 2); K1_MM(0);
        K1_RD(sl, 3); K1_MM(1);
        if (st < 9) asm volatile("s_waitcnt lgkmcnt(0)\n\ts_barrier" ::: "memory");
        K1_RD(sn, 0); K1_MM(2);
        K1_RD(sn, 1); K1_MM(3);
    }
#undef K1_RD
#undef K1_MM
    STAMP(0, 3);
    asm volatile("s_barrier" ::: "memory");
    const lds_cptr aux = (lds_cptr)lds;
    const bf16x8 tfb = lds_r128(aux + K1_TF + wm * 1024 + lane * 16);
    bf16x8 ffb[NF];
#pragma unroll
    for (int f = 0; f < NF; ++f) ffb[f] = lds_r128(aux + K1_FF + (F0 + f) * 1024 + lane * 16);
    const float* rsl = (const float*)(lds + K1_RS) + wm * 32;
    float rs[VORI ? 16 : 1];
    if (VORI) {
#pragma unroll
        for (int g = 0; g < 4; ++g) { const f32x4 t = *(const f32x4*)(rsl + 8 * g + 4 * hh); rs[4 * g] = t[0]; rs[4 * g + 1] = t[1]; rs[4 * g + 2] = t[2]; rs[4 * g + 3] = t[3]; }
    } else rs[0] = rsl[r] * (isq ? QSCALE : 1.f);
#pragma unroll
    for (int f = 0; f < NF; ++f) acc[f] = VORI ? mfma16(tfb, ffb[f], acc[f]) : mfma16(ffb[f], tfb, acc[f]);
    STAMP(0, 4);
#pragma unroll
    for (int f = 0; f < NF; ++f) {
        unsigned u[8];
#pragma unroll
        for (int i = 0; i < 8; ++i) u[i] = pk2(acc[f][2 * i] * rs[VORI ? 2 * i : 0], acc[f][2 * i + 1] * rs[VORI ? 2 * i + 1 : 0]);
        if (!VORI) {
            QST16(base + so[f][0], (u32x4{u[0], u[1], u[2], u[3]}));
            QST8(base + so[f][1], (u32x2{u[4], u[5]}));
            QST8(base + so[f][2], (u32x2{u[6], u[7]}));
        } else {
            char* p = base + so[f][0];
            QST16(p, (u32x4{u[0], u[1], u[2], u[3]}));
            QST16(p + 2 * VROW_B, (u32x4{u[4], u[5], u[6], u[7]}));
            if (so[f][1] == 19) {
                QST16(p + 16, (u32x4{ONES2, ONES2, ONES2, ONES2})); QST16(p + 2 * VROW_B + 16, (u32x4{ONES2, ONES2, ONES2, ONES2}));
                QST16(p + 32, (u32x4{0, 0, 0, 0})); QST16(p + 2 * VROW_B + 32, (u32x4{0, 0, 0, 0}));
            }
        }
    }
}

template <bool VORI>
__device__ __forceinline__ void k1_helper(const __bf16* __restrict__ x, const __bf16* __restrict__ w, char* __restrict__ ws, char* lds, int mt, int nt, int hw) {
    const int lane = threadIdx.x & 63, r = lane & 31, hh = lane >> 5;
    const unsigned lds0 = (unsigned)(uintptr_t)lds;
    const unsigned t_start = now_ticks();
    unsigned xo[4], wo[5];
#pragma unroll
    for (int i = 0; i < 4; ++i) {
        const int row = 8 * (4 * hw + i) + (lane >> 3);
        xo[i] = row * (D_ * 2) + (((lane & 7) ^ ((row >> 1) & 7)) * 16) - i * 1024;
    }
#pragma unroll
    for (int i = 0; i < 5; ++i) {
        const int idx = (5 * hw + i) * 64 + lane, krow = idx / 20, ch = idx - krow * 20;
        wo[i] = krow * (3 * D_ * 2) + ch * 16 - (i & 3) * 1024;
    }
    const char* xs = (const char*)x + (size_t)mt * 128 * (D_ * 2);
    const char* wsr = (const char*)w + nt * 320;
#define K1_M0(v) asm volatile("s_mov_b32 m0, %0\n\ts_nop 0" :: "s"(v) : "memory")
#define K1_DMA(vo, sp, imm) asm volatile("global_load_lds_dwordx4 %0, %1 offset:" #imm :: "v"(vo), "s"(sp) : "memory")
#define K1_PIECE(i, xp, wp, lb) do { \
        if ((i) == 0) { K1_M0((lb) + hw * 4096); K1_DMA(xo[0], xp, 0); } \
        if ((i) == 1) K1_DMA(xo[1], xp, 1024); \
        if ((i) == 2) K1_DMA(xo[2], xp, 2048); \
        if ((i) == 3) K1_DMA(xo[3], xp, 3072); \
        if ((i) == 4) { K1_M0((lb) + 16384 + hw * 5120); K1_DMA(wo[0], wp, 0); } \
        if ((i) == 5) K1_DMA(wo[1], wp, 1024); \
        if ((i) == 6) K1_DMA(wo[2], wp, 2048); \
        if ((i) == 7) K1_DMA(wo[3], wp, 3072); \
        if ((i) == 8) { K1_M0((lb) + 16384 + hw * 5120 + 4096); K1_DMA(wo[4], wp, 0); } \
        SBAR(); } while (0)
#pragma unroll 1
    for (int st = 0; st < 4; ++st) {
        const char* xp = xs + st * 128; const char* wp = wsr + (size_t)st * 64 * (3 * D_ * 2);
        const unsigned lb = lds0 + st * K1_STAGE;
#pragma unroll
        for (int i = 0; i < 9; ++i) K1_PIECE(i, xp, wp, lb);
    }
    WAIT_BAR(27);
#pragma unroll 1
    for (int st = 0; st < 7; ++st) {
        if (st) {
            const char* xp = xs + (st + 3) * 128; const char* wp = wsr + (size_t)(st + 3) * 64 * (3 * D_ * 2);
            const unsigned lb = lds0 + ((st + 3) & 3) * K1_STAGE;
#pragma unroll
            for (int i = 0; i < 9; ++i) K1_PIECE(i, xp, wp, lb);
        }
        WAIT_BAR(18);
    }
#undef K1_PIECE
#undef K1_DMA
#undef K1_M0
    const int tok = mt * 128 + 32 * hw + r;
    int col[2];
#pragma unroll
    for (int f = 0; f < 2; ++f) {
        const int t = f ? 0 : hw + 1;
        const int pc = VORI ? (r >> 2) : (int)((k1_perm(t) >> (4 * (r >> 2))) & 7);
        col[f] = nt * 160 + t * 32 + pc * 4 + (r & 3);
    }
    const u64* rt = (const u64*)(ws + WS_RT) + tok * 2 + hh;
    const u64* rc = (const u64*)(ws + WS_RC) + hh * (2 * 3 * D_);
    u64 rec[5];
#define K1_POLL() do { rec[0] = ld_agent(rt); rec[1] = ld_agent(rc + col[0]); rec[2] = ld_agent(rc + 3 * D_ + col[0]); rec[3] = ld_agent(rc + col[1]); rec[4] = ld_agent(rc + 3 * D_ + col[1]); } while (0)
    K1_POLL();
    WAIT_BAR(14);
    WAIT_BAR(5);
    bool ok = false;
#pragma unroll 1
    for (int it = 0; it < K1_SPIN; ++it) {
        WAIT_VM(0);
        const unsigned span = now_ticks() - t_start + 32u;
        bool good = true;
#pragma unroll
        for (int i = 0; i < 5; ++i) good = good && (unsigned)(rec[i] >> 32) - t_start <= span;
        if (__all(good)) { ok = true; break; }
        __builtin_amdgcn_s_sleep(8);
        K1_POLL();
    }
#undef K1_POLL
    float tv = __uint_as_float((unsigned)rec[0]);
    float cs0 = __uint_as_float((unsigned)rec[1]) + __uint_as_float((unsigned)rec[2]), cs1 = __uint_as_float((unsigned)rec[3]) + __uint_as_float((unsigned)rec[4]);
    if (__builtin_expect(!ok, 0)) {
        const u32x4* rowp = (const u32x4*)(x + (size_t)tok * D_) + hh * 8;
        float mu = 0.f;
#pragma unroll 1
        for (int pass = 0; pass < 2; ++pass) {
            float s1 = 0.f, s2 = 0.f, s3 = 0.f;
#pragma unroll 1
            for (int c = 0; c < 8; ++c) {
                float a0 = 0.f, a1 = 0.f;
#pragma unroll 1
                for (int i = 0; i < 5; ++i) {
                    const u32x4 u = rowp[c + 16 * i];
#pragma unroll
                    for (int j = 0; j < 4; ++j) {
                        if (pass == 0) a0 = dot2(u[j], ONES2, a0);
                        else { const float d0 = bf_lo(u[j]) - mu, d1 = bf_hi(u[j]) - mu; a0 = __builtin_fmaf(d0, d0, a0); a1 = __builtin_fmaf(d1, d1, a1); }
                    }
                }
                if (pass) a0 += a1;
                s1 = (c & 1) ? s1 + a0 : a0;
                if (c & 1) s2 = (c & 2) ? s2 + s1 : s1;
                if ((c & 3) == 3) s3 = (c & 4) ? s3 + s2 : s2;
            }
            float tot = swap32_add(s3) * (1.f / D_);
            PIN(tot);
            if (pass == 0) mu = tot; else tv = hh ? __builtin_amdgcn_rsqf(tot + EPS) : mu;
        }
        float q[2][2];
#pragma unroll
        for (int kq = 0; kq < 2; ++kq) {
            const unsigned short* wc = (const unsigned short*)w + (size_t)(2 * hh + kq) * 160 * (3 * D_);
            float t0[4] = {0.f, 0.f, 0.f, 0.f}, t1[4] = {0.f, 0.f, 0.f, 0.f};
#pragma unroll 1
            for (int wv = 0; wv < 8; ++wv) {
#pragma unroll
                for (int g = 0; g < 4; ++g) {
                    const unsigned short* rw = wc + (wv * 20 + g) * (3 * D_);
                    float c0 = bf_lo(rw[col[0]]), c1 = bf_lo(rw[col[1]]);
#pragma unroll
                    for (int i = 1; i < 5; ++i) { c0 += bf_lo(rw[4 * i * (3 * D_) + col[0]]); c1 += bf_lo(rw[4 * i * (3 * D_) + col[1]]); }
                    t0[g] += c0; t1[g] += c1;
                }
            }
            q[0][kq] = (t0[0] + t0[1]) + (t0[2] + t0[3]); q[1][kq] = (t1[0] + t1[1]) + (t1[2] + t1[3]);
        }
        cs0 = q[0][0] + q[0][1]; cs1 = q[1][0] + q[1][1];
    }
    {
        unsigned m1, m2, m3; split3(-tv, m1, m2, m3);
        u32x4 tf = {m1 | (m1 << 16), m2 | (m1 << 16), m2 | (m3 << 16), m2 | (m3 << 16)};
        if (hh) tf = u32x4{0, 0, 0, 0};
        *(u32x4*)(lds + K1_TF + hw * 1024 + lane * 16) = tf;
        unsigned a1, a2, a3; split3(swap32_add(cs0), a1, a2, a3);
        *(u32x4*)(lds + K1_FF + (hw + 1) * 1024 + lane * 16) = u32x4{a1 | (a2 << 16), a1 | (a3 << 16), a2 | (a1 << 16), a3 | (a2 << 16)};
        if (hw == 0) {
            split3(swap32_add(cs1), a1, a2, a3);
            *(u32x4*)(lds + K1_FF + lane * 16) = u32x4{a1 | (a2 << 16), a1 | (a3 << 16), a2 | (a1 << 16), a3 | (a2 << 16)};
        }
        if (hh) ((float*)(lds + K1_RS))[32 * hw + r] = tv;
    }
    asm volatile("s_waitcnt lgkmcnt(0)\n\ts_barrier" ::: "memory");
}

struct K1Stat { u32x4 xr[5], wr[5]; };
__device__ __forceinline__ void k1_stat_load(K1Stat& sv, const __bf16* __restrict__ x, const __bf16* __restrict__ w, char* __restrict__ ws, int job) {
    const int tid = threadIdx.x, lane = tid & 63, wid = tid >> 6;
    const u32x4* xp = (const u32x4*)(x + (size_t)(job * 32 + wid * 4 + (lane >> 4)) * D_) + (lane & 15);
    const int jc = job < 60 ? job : 59;
    const u32x4* wp = (const u32x4*)(w + (size_t)((jc / 15) * 160 + wid * 20 + (lane >> 4)) * (3 * D_) + (jc % 15) * 128) + (lane & 15);
#pragma unroll
    for (int i = 0; i < 5; ++i) sv.xr[i] = xp[16 * i];
#pragma unroll
    for (int i = 0; i < 5; ++i) sv.wr[i] = wp[(size_t)i * 4 * (3 * D_ / 8)];
}
__device__ __forceinline__ float red16(float v) {
    v += __shfl_xor(v, 1); v += __shfl_xor(v, 2); v += __shfl_xor(v, 4); v += __shfl_xor(v, 8);
    return v;
}
__device__ __forceinline__ void k1_stat(const K1Stat& sv, char* __restrict__ ws, char* lds, int job) {
    const int tid = threadIdx.x, lane = tid & 63, wid = tid >> 6;
    float a = 0.f;
#pragma unroll
    for (int i = 0; i < 5; ++i)
#pragma unroll
        for (int j = 0; j < 4; ++j) a = dot2(sv.xr[i][j], ONES2, a);
    float mu = red16(a) * (1.f / D_);
    PIN(mu);
    float a0 = 0.f, a1 = 0.f;
#pragma unroll
    for (int i = 0; i < 5; ++i)
#pragma unroll
        for (int j = 0; j < 4; ++j) { const float d0 = bf_lo(sv.xr[i][j]) - mu, d1 = bf_hi(sv.xr[i][j]) - mu; a0 = __builtin_fmaf(d0, d0, a0); a1 = __builtin_fmaf(d1, d1, a1); }
    float var = red16(a0 + a1) * (1.f / D_);
    PIN(var);
    const int tok = job * 32 + wid * 4 + (lane >> 4), c = lane & 15;
    float pub = c ? __builtin_amdgcn_rsqf(var + EPS) : mu;
    PIN(pub);
    if (c < 2) st_agent((u64*)(ws + WS_RT) + tok * 2 + c, pub, now_ticks());
    if (c == 2) {
        const float sig2 = __builtin_sqrtf(var * (1.f + EPS) + EPS * EPS);
        *(f32x4*)(ws + WS_TOK + (size_t)tok * 16) = f32x4{mu, 1.0f / sig2, sig2, 0.f};
    }
    float cs[8];
#pragma unroll
    for (int j = 0; j < 4; ++j) {
        cs[2 * j] = bf_lo(sv.wr[0][j]); cs[2 * j + 1] = bf_hi(sv.wr[0][j]);
#pragma unroll
        for (int i = 1; i < 5; ++i) { cs[2 * j] += bf_lo(sv.wr[i][j]); cs[2 * j + 1] += bf_hi(sv.wr[i][j]); }
    }
    float* red = (float*)(lds + 86016);
    *(f32x4*)(red + (wid * 4 + (lane >> 4)) * 128 + c * 8) = f32x4{cs[0], cs[1], cs[2], cs[3]};
    *(f32x4*)(red + (wid * 4 + (lane >> 4)) * 128 + c * 8 + 4) = f32x4{cs[4], cs[5], cs[6], cs[7]};
    asm volatile("s_waitcnt lgkmcnt(0)\n\ts_barrier" ::: "memory");
    if (tid < 128 && job < 60) {
        float t[4] = {0.f, 0.f, 0.f, 0.f};
#pragma unroll
        for (int i = 0; i < 32; ++i) t[i & 3] += red[i * 128 + tid];
        st_agent((u64*)(ws + WS_RC) + (job / 15) * (3 * D_) + (job % 15) * 128 + tid, (t[0] + t[1]) + (t[2] + t[3]), now_ticks());
    }
}

template <int KIND>
__device__ __forceinline__ void k1_pack(const __bf16* __restrict__ wsrc, char* __restrict__ ws, char* lds, int strip, int third, const K1Stat& sv, int job) {
    constexpr int NI = KIND == 1 ? 8 : 10, NKS = KIND == 1 ? 4 : 5, ROWB = (KIND == 0 ? F_ : D_) * 2;
    const int tid = threadIdx.x, lane = tid & 63, r = lane & 31, hh = lane >> 5;
    const int wid = __builtin_amdgcn_readfirstlane(tid >> 6);
    const unsigned lds0 = (unsigned)(uintptr_t)lds + wid * 10240;
    const int row0 = third * 512 + wid * NI * 8;
    const char* src = (const char*)wsrc + (size_t)(row0 + (lane >> 3)) * ROWB + strip * 128 + (((lane & 7) ^ (((lane >> 4) & 1) * 4)) * 16);
#pragma unroll
    for (int i = 0; i < NI; ++i) glds16(src + (size_t)i * 8 * ROWB, lds0 + i * 1024);
    const int q4 = (lane & 15) >> 2, p4 = lane & 3, gi = (lane >> 4) & 1;
    int pc = 4 * gi + p4;
    if (KIND != 0) pc = (0x75643120u >> (4 * pc)) & 7;
    const lds_cptr sb = (lds_cptr)lds + wid * 10240;
    float cs[2] = {0.f, 0.f};
    k1_stat(sv, ws, lds, job);
    WAIT_VM(0);
#pragma unroll
    for (int ks = 0; ks < NKS; ++ks) {
        int ra, rb;
        if (KIND == 0) { ra = 16 * ks + 8 * hh; rb = ra + 4; }
        else if (KIND == 1) { ra = 16 * ks + 4 * hh; rb = ra + 8; }
        else if (ks < 4) { ra = 20 * ks + 4 * hh; rb = ra + 8; }
        else { ra = 40 * hh + 16; rb = ra + 20; }
#pragma unroll
        for (int f = 0; f < 2; ++f) {
            const int co = ((((f * 8 + pc) >> 1) ^ ((q4 >> 1) * 4)) * 16) + (pc & 1) * 8;
            const bf16x8 fr = cat8(lds_tr(sb + (ra + q4) * 128 + co), lds_tr(sb + (rb + q4) * 128 + co));
            const int ft = strip * 2 + f;
            size_t dst;
            if (KIND == 0) dst = WS_PW + ((size_t)(ft / 3) * 240 + (wid * 5 + ks) * 3 + ft % 3) * 1024;
            else if (KIND == 1) { const int g = third * 32 + wid * 4 + ks; const int f5 = ft % 5; dst = WS_PW + ((size_t)((g / 24) * 4 + ft / 5) * 240 + (f5 < 3 ? 120 + (g % 24) * 3 + f5 : 192 + (g % 24) * 2 + f5 - 3)) * 1024; }
            else dst = WS_PWO + ((size_t)ft * 40 + (ks < 4 ? wid * 4 + ks : 32 + wid)) * 1024;
            PST16(ws + dst + lane * 16, __builtin_bit_cast(u32x4, fr));
            if (KIND == 0) {
                const u32x4 a = __builtin_bit_cast(u32x4, fr);
#pragma unroll
                for (int i = 0; i < 4; ++i) cs[f] = dot2(a[i], ONES2, cs[f]);
            }
        }
    }
    if (KIND == 0) {
        float* red = (float*)(lds + 81920);
        red[(wid * 2 + hh) * 64 + r] = cs[0];
        red[(wid * 2 + hh) * 64 + 32 + r] = cs[1];
        __syncthreads();
        if (tid < 64) {
            float s = 0.f;
#pragma unroll
            for (int i = 0; i < 16; ++i) s += red[i * 64 + tid];
            ((float*)(ws + WS_CS1))[strip * 64 + tid] = s;
        }
    }
}

__global__ __launch_bounds__(768, 3) void k1_qkv(const __bf16* __restrict__ x, const __bf16* __restrict__ wqkv, const __bf16* __restrict__ wo, const __bf16* __restrict__ w1,
                                                 const __bf16* __restrict__ w2, char* __restrict__ ws) {
    extern __shared__ __attribute__((aligned(16))) char lds[];
    const int b = blockIdx.x, xcd = b & 7, idx = b >> 3;
    const int wid = __builtin_amdgcn_readfirstlane(threadIdx.x >> 6);
    if ((CPF & 16) && idx == 0 && wid < 4) { uint64_t a; CODE_ADDR(a, K1_SYM); unsigned v = code_touch(a, wid, K1_CODE); CODE_DONE(v); }
    if (idx >= 24) {
        if (wid >= 8) {
            const int part = (idx - 24) * 4 + wid - 8;
            if ((CPF & 1) && part < 4) { uint64_t a; CODE_ADDR(a, K1_SYM); unsigned v = code_touch(a, part, K1_CODE); CODE_DONE(v); }
            else if ((CPF & 2) && part >= 4 && part < 7) { uint64_t a; CODE_ADDR(a, K2_SYM); unsigned v = code_touch(a, part - 4, K2_CODE); CODE_DONE(v); }
            return;
        }
        const int job = (idx - 24) * 8 + xcd;
        if (threadIdx.x < 2) __hip_atomic_store((unsigned*)(ws + WS_CNT + job * 256 + threadIdx.x * 128), 0u, __ATOMIC_RELAXED, __HIP_MEMORY_SCOPE_AGENT);
        K1Stat sv;
        k1_stat_load(sv, x, wqkv, ws, job);
        if (job < 24) k1_pack<0>(w1, ws, lds, job, 0, sv, job);
        else if (job < 54) k1_pack<1>(w2, ws, lds, (job - 24) % 10, (job - 24) / 10, sv, job);
        else k1_pack<2>(wo, ws, lds, job - 54, 0, sv, job);
        STAMP(3, 0);
        return;
    }
    const int mt = (xcd >> 1) * 4 + (idx & 3), nt = (xcd & 1) * 6 + (idx >> 2);
    if (__builtin_expect(wid >= 8, 1)) { if (__builtin_expect(nt < 8, 1)) k1_helper<false>(x, wqkv, ws, lds, mt, nt, wid - 8); else k1_helper<true>(x, wqkv, ws, lds, mt, nt, wid - 8); return; }
    if (nt < 8) { if (wid >= 4) k1_body<false, 1>(ws, lds, mt, nt); else k1_body<false, 0>(ws, lds, mt, nt); }
    else { if (wid >= 4) k1_body<true, 1>(ws, lds, mt, nt); else k1_body<true, 0>(ws, lds, mt, nt); }
    STAMP(0, 5);
}

#define MLP_NB 20
#define AP_HI 2
#define AP_MID 1
#define AP_LO 0
#define GELU_PRIO 1
#define MLP_SLEEP 0
#define AW 7
#define MLP_PRIO 0
template <int N> __device__ __forceinline__ void wait_vm() { asm volatile("s_waitcnt vmcnt(%0)" :: "n"(N) : "memory"); }
#define WAIT_VMX(N) wait_vm<(N)>()
constexpr int AT_SLOT = 8 * KVT_B;
constexpr int K2_X = 4 * AT_SLOT, K2_H = K2_X + 40960, K2_CNT = K2_H + 24576, K2_LDS = K2_CNT + 128;
constexpr int C_FULL = 0, C_FREE = 64, C_X = 112, C_H = 116, C_PRDY = 120, C_MDONE = 124;

__device__ __forceinline__ void attn_slow(const char* __restrict__ kv, bf16x8 qf, s16x4 qt, f32x16& o, int r, int hh) {
    float m = -INFINITY;
    o = f32x16{};
    if (hh) qt = s16x4{0, 0, 0, 0};
    for (int t = 0; t < 64; ++t) {
        const char* p = kv + (size_t)t * KVT_B;
        const bf16x8 kf = *(const bf16x8*)(p + hh * 512 + r * 16);
        const s16x4 kt = *(const s16x4*)(p + 1024 + r * 8);
        const int rv = r < 20 ? r : 20;
        const bf16x8 v0 = *(const bf16x8*)(p + 1280 + hh * VROW_B + rv * 16);
        const bf16x8 v1 = *(const bf16x8*)(p + 1280 + 2 * VROW_B + hh * VROW_B + rv * 16);
        f32x16 s = mfma16(kf, qf, f32x16{});
        s = mfma8(kt, qt, s);
        float tm = s[0];
#pragma unroll
        for (int i = 1; i < 16; ++i) tm = __builtin_fmaxf(tm, s[i]);
        tm = swap32_max(tm);
        const float mn = __builtin_fmaxf(m, tm);
        const float al = __builtin_amdgcn_exp2f(m - mn);
        m = mn;
        u32x4 p0, p1;
#pragma unroll
        for (int i = 0; i < 4; ++i) {
            p0[i] = pk2(__builtin_amdgcn_exp2f(s[2 * i] - m), __builtin_amdgcn_exp2f(s[2 * i + 1] - m));
            p1[i] = pk2(__builtin_amdgcn_exp2f(s[8 + 2 * i] - m), __builtin_amdgcn_exp2f(s[8 + 2 * i + 1] - m));
        }
#pragma unroll
        for (int i = 0; i < 16; ++i) o[i] *= al;
        o = mfma16(v0, __builtin_bit_cast(bf16x8, p0), o);
        o = mfma16(v1, __builtin_bit_cast(bf16x8, p1), o);
    }
}

__device__ __forceinline__ void attn_finish(char* __restrict__ ws, const char* kvh, bf16x8 qf, s16x4 qt, f32x16 o, int qt_idx, int head, int lane) {
    const int r = lane & 31, hh = lane >> 5;
    float l = swap32_add(hh ? o[8] : 0.f);
    bool bad = !(l < 1e30f);
#pragma unroll
    for (int i = 0; i < 8; ++i) bad |= !(__builtin_fabsf(o[i]) < 3e38f);
#pragma unroll
    for (int i = 8; i < 12; ++i) bad |= (hh == 0) && !(__builtin_fabsf(o[i]) < 3e38f);
    if (__builtin_expect(__any(bad), 0)) {
        attn_slow(kvh, qf, qt, o, r, hh);
        l = swap32_add(hh ? o[8] : 0.f);
    }
    const float inv = 1.0f / l;
    char* ob = ws + WS_O + (size_t)qt_idx * 40 * 1024;
    st16(ob + head * 1024 + lane * 16, u32x4{pk2(o[0] * inv, o[1] * inv), pk2(o[2] * inv, o[3] * inv), pk2(o[4] * inv, o[5] * inv), pk2(o[6] * inv, o[7] * inv)});
    if (hh == 0) st8(ob + (32 + (head >> 2)) * 1024 + (head & 3) * 256 + r * 8, u32x2{pk2(o[8] * inv, o[9] * inv), pk2(o[10] * inv, o[11] * inv)});
}

#define K2_SPIN 400000
#define AP_CUT 8
#define TAILP 0
#define TAIL_PD 6
#define WO_PRE 12
__device__ __forceinline__ int fin_tt() { return (blockIdx.x & 7) * 8 + (blockIdx.x >> 5); }
__device__ __forceinline__ int fin_ft(int f) { return ((blockIdx.x >> 3) & 3) * 5 + f; }
__device__ __forceinline__ int wo_frag(int kq, int k) { return k < 16 ? kq * 16 + k : 32 + kq * 4 + (k - 16); }
#define BAR() asm volatile("s_waitcnt lgkmcnt(0)\n\ts_barrier" ::: "memory")
__device__ __forceinline__ void k2_count(char* ws, int off) {
    if ((threadIdx.x & 63) == 0) __hip_atomic_fetch_add((unsigned*)(ws + WS_CNT + off), 1u, __ATOMIC_RELAXED, __HIP_MEMORY_SCOPE_AGENT);
}
__device__ __forceinline__ void k2_wait(const char* ws, int off, unsigned target) {
    const unsigned* c = (const unsigned*)(ws + WS_CNT + off);
#pragma unroll 1
    for (int it = 0; it < K2_SPIN; ++it) {
        if (__builtin_amdgcn_readfirstlane(__hip_atomic_load(c, __ATOMIC_RELAXED, __HIP_MEMORY_SCOPE_AGENT)) >= target) break;
        __builtin_amdgcn_s_sleep(1);
    }
    asm volatile("" ::: "memory");
}
constexpr int T_O = 0, T_RED = 40960, T_SUM = K2_X;
__device__ __forceinline__ void k2_gather(const __bf16* __restrict__ x, const __bf16* __restrict__ b2, char* ws, char* lds, const __bf16* out, int w) {
    const int lane = threadIdx.x & 63, r = lane & 31, hh = lane >> 5;
    const unsigned cnt = (unsigned)(uintptr_t)lds + K2_CNT;
    const int tt = fin_tt(), c0 = fin_ft(0) * 32 + hh * 8;
    u32x4 xv[3], bv[3], pv[3][4];
    unsigned touch = 0;
    if (CPF & 8) asm volatile("global_load_dword %0, %1, %2" : "=v"(touch) : "v"(((tt * 32 + r) * D_ + c0) * 2), "s"(out) : "memory");
    SBAR();
#pragma unroll
    for (int i = 0; i < 3; ++i) {
        const int u = w + 4 * i < 10 ? w + 4 * i : w;
        xv[i] = *(const u32x4*)(x + (size_t)(tt * 32 + r) * D_ + c0 + u * 16); bv[i] = *(const u32x4*)(b2 + c0 + u * 16);
    }
    SBAR();
    if (w == 0) { k2_wait(ws, tt * 256 + 128, 4u); lds_signal(cnt + C_PRDY); }
    else lds_await(cnt + C_PRDY, lds_peek(cnt + C_PRDY), 1);
    const __amdgpu_buffer_rsrc_t rp = __builtin_amdgcn_make_buffer_rsrc(ws + WS_PART + ((size_t)tt * 20 + fin_ft(0)) * 2048, 0, 3 * 64 * 20 * 2048 + 10240, 0x00020000);
#pragma unroll
    for (int i = 0; i < 3; ++i) {
        const int u = w + 4 * i < 10 ? w + 4 * i : w;
#pragma unroll
        for (int k = 0; k < 4; ++k) pv[i][k] = __builtin_amdgcn_raw_buffer_load_b128(rp, lane * 16, k * (64 * 20 * 2048) + u * 1024, 16);
    }
    SBAR();
#pragma unroll
    for (int i = 0; i < 3; ++i) {
        const int u = w + 4 * i < 10 ? w + 4 * i : w;
        float m[8];
#pragma unroll
        for (int k = 0; k < 4; ++k) {
            m[2 * k] = (bf_lo(xv[i][k]) + bf_lo(bv[i][k])) + ((bf_lo(pv[i][0][k]) + bf_lo(pv[i][1][k])) + (bf_lo(pv[i][2][k]) + bf_lo(pv[i][3][k])));
            m[2 * k + 1] = (bf_hi(xv[i][k]) + bf_hi(bv[i][k])) + ((bf_hi(pv[i][0][k]) + bf_hi(pv[i][1][k])) + (bf_hi(pv[i][2][k]) + bf_hi(pv[i][3][k])));
        }
        f32x4* d = (f32x4*)(lds + T_SUM + u * 2048) + lane;
        d[0] = f32x4{m[0], m[1], m[2], m[3]}; d[64] = f32x4{m[4], m[5], m[6], m[7]};
    }
    if (CPF & 8) asm volatile("" :: "v"(touch));
    STAMPW(2, 11, 8);
}
__device__ __forceinline__ void k2_tail(char* ws, char* lds, __bf16* __restrict__ out, int f, int kq, const bf16x8 (&wf)[20]) {
    const int lane = threadIdx.x & 63, r = lane & 31, hh = lane >> 5;
    const int tt = fin_tt(), ft = fin_ft(f), g = kq * 5 + f;
    const __amdgpu_buffer_rsrc_t ro = __builtin_amdgcn_make_buffer_rsrc(ws + WS_O + ((size_t)tt * 40 + g * 4) * 1024, 0, 4096, 0x00020000);
    u32x4 ov[4];
    STAMP(4, 0);
    BAR();
    STAMP(4, 1);
#pragma unroll
    for (int k = 0; k < 4; ++k) ov[k] = __builtin_amdgcn_raw_buffer_load_b128(ro, lane * 16 + k * 1024, 0, 16);
    SBAR();
    const f32x4* sp = (const f32x4*)(lds + T_SUM + (f * 2 + kq) * 2048) + lane;
    const f32x4 s0 = sp[0], s1 = sp[64];
    SBAR();
#pragma unroll
    for (int k = 0; k < 4; ++k) *(u32x4*)(lds + T_O + (g * 4 + k) * 1024 + lane * 16) = ov[k];
    BAR();
    STAMP(4, 2);
    f32x16 acc = f32x16{};
    const lds_cptr ob = (lds_cptr)lds + T_O + kq * 16384 + lane * 16, ot = (lds_cptr)lds + T_O + (32 + kq * 4) * 1024 + hh * 512 + r * 8;
    constexpr int PD = TAIL_PD;
    bf16x8 of[PD];
#define OFRAG(k) ((k) < 16 ? lds_r128(ob + (k) * 1024) : cat8(lds_r64(ot + ((k) - 16) * 1024), lds_r64(ot + ((k) - 16) * 1024 + 256)))
#pragma unroll
    for (int k = 0; k < PD; ++k) of[k] = OFRAG(k);
    SBAR();
#pragma unroll
    for (int k = 0; k < 20; ++k) {
        acc = mfma16(wf[k], of[k % PD], acc);
        SBAR();
        if (k + PD < 20) of[k % PD] = OFRAG(k + PD);
        SBAR();
    }
#undef OFRAG
    STAMP(4, 3);
    f32x4* red = (f32x4*)(lds + T_RED + f * 4096);
    {
        const int c = 1 - kq;
        red[(2 * c) * 64 + lane] = kq ? f32x4{acc[0], acc[1], acc[2], acc[3]} : f32x4{acc[8], acc[9], acc[10], acc[11]};
        red[(2 * c + 1) * 64 + lane] = kq ? f32x4{acc[4], acc[5], acc[6], acc[7]} : f32x4{acc[12], acc[13], acc[14], acc[15]};
    }
    float m[8];
#pragma unroll
    for (int k = 0; k < 8; ++k) m[k] = (kq ? acc[8 + k] : acc[k]) + (k < 4 ? s0[k] : s1[k - 4]);
    BAR();
    {
        const f32x4 a = red[(2 * kq) * 64 + lane], bb = red[(2 * kq + 1) * 64 + lane];
        u32x4 u;
        u[0] = pk2(a[0] + m[0], a[1] + m[1]); u[1] = pk2(a[2] + m[2], a[3] + m[3]);
        u[2] = pk2(bb[0] + m[4], bb[1] + m[5]); u[3] = pk2(bb[2] + m[6], bb[3] + m[7]);
        *(u32x4*)(out + (size_t)(tt * 32 + r) * D_ + ft * 32 + kq * 16 + hh * 8) = u;
    }
    STAMP(4, 4);
}

__device__ __forceinline__ void k2_attn(char* ws, char* lds, __bf16* __restrict__ out, int wid, int head, int qb) {
    const int lane = threadIdx.x & 63, r = lane & 31, hh = lane >> 5;
    const unsigned lds0 = (unsigned)(uintptr_t)lds, cnt = lds0 + K2_CNT;
    const int qt_idx = qb * 8 + wid;
    const char* kvh = ws + WS_KV + (size_t)head * 64 * KVT_B;
    const char* src = kvh + wid * KVT_B + lane * 16;
    auto tile_issue = [&](int c) {
        const unsigned base = lds0 + (c & 3) * AT_SLOT + wid * KVT_B;
        glds16(src + c * AT_SLOT, base);
        glds16(src + c * AT_SLOT + 1024, base + 1024);
        if (lane < (KVT_B - 2048) / 16) glds16(src + c * AT_SLOT + 2048, base + 2048);
    };
    tile_issue(0);
    {
        const char* qp = ws + WS_Q + ((size_t)head * 64 + qt_idx) * QT_B + lane * 16;
        glds16(qp, lds0 + K2_H + wid * QT_B);
        if (lane < 16) glds16(qp + 1024, lds0 + K2_H + wid * QT_B + 1024);
    }
    if (wid == 0 && lane < 32) *(__attribute__((address_space(3))) unsigned*)(uintptr_t)(cnt + lane * 4) = lane == 8 ? 8u : 0u;
    asm volatile("s_waitcnt lgkmcnt(0)\n\ts_barrier" ::: "memory");
    const int koff = hh * 512 + r * 16, ktoff = hh ? 1600 : 1024 + r * 8, voff = 1280 + hh * VROW_B + (r < 21 ? r : 21) * 16;
    WAIT_VM(0);
    const lds_cptr qp = (lds_cptr)lds + K2_H + wid * QT_B;
    bf16x8 qf = lds_r128(qp + koff);
    s16x4 qt = lds_r64(qp + 1024 + r * 8);
    asm volatile("s_waitcnt lgkmcnt(0)" : "+v"(qf), "+v"(qt) :: "memory");
    lds_signal(cnt + C_FULL);
    tile_issue(1); tile_issue(2); tile_issue(3);
    lds_await(cnt + C_FULL, lds_peek(cnt + C_FULL), 8);
    STAMPW(1, 1, AW);
    f32x16 o = f32x16{}, sA, sB;
    u32x4 pA0, pA1, pB0 = {0, 0, 0, 0}, pB1 = {0, 0, 0, 0};
    bf16x8 kf, v0, v1; s16x4 kt;
    {
        const lds_cptr sb = (lds_cptr)lds;
        kf = lds_r128(sb + koff); kt = lds_r64(sb + ktoff);
        if (hh) qt = s16x4{0, 0, 0, 0};
        sA = mfma16(kf, qf, f32x16{});
        sA = mfma8(kt, qt, sA);
        float m0 = sA[0];
#pragma unroll
        for (int i = 1; i < 16; ++i) m0 = __builtin_fmaxf(m0, sA[i]);
        m0 = swap32_max(m0);
        const unsigned nm = pk2(-m0, 0.f) & 0xffffu;
        if (hh) qt[0] = (short)nm;
#pragma unroll
        for (int i = 0; i < 16; ++i) sA[i] += bf_lo(nm);
        kf = lds_r128(sb + KVT_B + koff); kt = lds_r64(sb + KVT_B + ktoff);
        v0 = lds_r128(sb + voff); v1 = v0;
    }
#define EX(v) __builtin_amdgcn_exp2f(v)
#define GRP(MF, SC, i, PW, j) do { MF; SBAR(); const float e0 = EX(SC[i]), e1 = EX(SC[i + 1]), e2 = EX(SC[i + 2]), e3 = EX(SC[i + 3]); PW[j] = pk2(e0, e1); PW[j + 1] = pk2(e2, e3); PIN(PW); SBAR(); } while (0)
#define STEP(SC, SN, PC0, PC1, PP0, PP1, KP, VP) do { \
        GRP(SN = mfma16(kf, qf, f32x16{}), SC, 0, PC0, 0); \
        kf = lds_r128((KP) + koff); SBAR(); \
        GRP(o = mfma16(v0, __builtin_bit_cast(bf16x8, PP0), o), SC, 4, PC0, 2); \
        v0 = lds_r128((VP) + voff); SBAR(); \
        GRP(SN = mfma8(kt, qt, SN), SC, 8, PC1, 0); \
        kt = lds_r64((KP) + ktoff); SBAR(); \
        GRP(o = mfma16(v1, __builtin_bit_cast(bf16x8, PP1), o), SC, 12, PC1, 2); \
        v1 = lds_r128((VP) + voff + 2 * VROW_B); SBAR(); \
    } while (0)
    if (wid >= 4) __builtin_amdgcn_s_setprio(AP_MID);
    for (int c = 0; c < 7; ++c) {
        const lds_cptr sb = (lds_cptr)lds + (c & 3) * AT_SLOT;
        const lds_cptr nb = (lds_cptr)lds + ((c + 1) & 3) * AT_SLOT;
        if (wid < 4) __builtin_amdgcn_s_setprio(AP_HI);
        STEP(sA, sB, pA0, pA1, pB0, pB1, sb + 2 * KVT_B, sb + 0 * KVT_B);
        STEP(sB, sA, pB0, pB1, pA0, pA1, sb + 3 * KVT_B, sb + 1 * KVT_B);
        STEP(sA, sB, pA0, pA1, pB0, pB1, sb + 4 * KVT_B, sb + 2 * KVT_B);
        if (c == 0) { WAIT_VM(6); lds_signal(cnt + C_FULL + 4); }
        STEP(sB, sA, pB0, pB1, pA0, pA1, sb + 5 * KVT_B, sb + 3 * KVT_B);
        STEP(sA, sB, pA0, pA1, pB0, pB1, sb + 6 * KVT_B, sb + 4 * KVT_B);
        if (wid < 4 && c < AP_CUT) __builtin_amdgcn_s_setprio(AP_LO);
        const unsigned pk = lds_peek(cnt + C_FULL + 4 * (c + 1)), pf = lds_peek(cnt + C_FREE + 4 * c - 4);
        SBAR();
        STEP(sB, sA, pB0, pB1, pA0, pA1, sb + 7 * KVT_B, sb + 5 * KVT_B);
        lds_await(cnt + C_FULL + 4 * (c + 1), pk, 8);
        if (c <= 4) {
            if (c >= 1) { lds_await(cnt + C_FREE + 4 * c - 4, pf, 8); tile_issue(c + 3); }
            WAIT_VM(3);
        } else WAIT_VM(0);
        if (c <= 5) lds_signal(cnt + C_FULL + 4 * (c + 2));
        SBAR();
        STEP(sA, sB, pA0, pA1, pB0, pB1, nb + 0 * KVT_B, sb + 6 * KVT_B);
        STEP(sB, sA, pB0, pB1, pA0, pA1, nb + 1 * KVT_B, sb + 7 * KVT_B);
        lds_signal(cnt + C_FREE + 4 * c);
        STAMPW(1, 2 + c, AW);
    }
    const int tf = 2 + (wid >> 1), tkq = wid & 1;
    const char* wop = ws + WS_PWO + (size_t)fin_ft(tf < 5 ? tf : 4) * 40 * 1024 + lane * 16;
    bf16x8 wf[20];
#pragma unroll
    for (int k = 0; k < WO_PRE; ++k) { wf[k] = *(const bf16x8*)(wop + (wid < 6 ? wo_frag(tkq, k) * 1024 : 0)); SBAR(); }
    {
        const lds_cptr sb = (lds_cptr)lds + 3 * AT_SLOT;
        if (wid < 4) __builtin_amdgcn_s_setprio(AP_HI);
        STEP(sA, sB, pA0, pA1, pB0, pB1, sb + 2 * KVT_B, sb + 0 * KVT_B);
        STEP(sB, sA, pB0, pB1, pA0, pA1, sb + 3 * KVT_B, sb + 1 * KVT_B);
        STEP(sA, sB, pA0, pA1, pB0, pB1, sb + 4 * KVT_B, sb + 2 * KVT_B);
        STEP(sB, sA, pB0, pB1, pA0, pA1, sb + 5 * KVT_B, sb + 3 * KVT_B);
        STEP(sA, sB, pA0, pA1, pB0, pB1, sb + 6 * KVT_B, sb + 4 * KVT_B);
        if (wid < 4 && 7 < AP_CUT) __builtin_amdgcn_s_setprio(AP_LO);
        STEP(sB, sA, pB0, pB1, pA0, pA1, sb + 7 * KVT_B, sb + 5 * KVT_B);
        STEP(sA, sB, pA0, pA1, pB0, pB1, sb + 7 * KVT_B, sb + 6 * KVT_B);
        STEP(sB, sA, pB0, pB1, pA0, pA1, sb + 7 * KVT_B, sb + 7 * KVT_B);
        STAMPW(1, 9, AW);
    }
    o = mfma16(v0, __builtin_bit_cast(bf16x8, pB0), o);
    o = mfma16(v1, __builtin_bit_cast(bf16x8, pB1), o);
#undef STEP
#undef GRP
#undef EX
    attn_finish(ws, kvh, qf, qt, o, qt_idx, head, lane);
    WAIT_VM(0);
    k2_count(ws, qt_idx * 256);
    STAMPW(1, 10, AW);
    __builtin_amdgcn_s_setprio(0);
    if (wid >= 6) {
        if (wid == 7) { k2_wait(ws, fin_tt() * 256, 32u); BAR(); }
        return;
    }
    SBAR();
#pragma unroll
    for (int k = WO_PRE; k < 20; ++k) { wf[k] = *(const bf16x8*)(wop + wo_frag(tkq, k) * 1024); SBAR(); }
    k2_tail(ws, lds, out, tf, tkq, wf);
}

constexpr int mlp_cnt(int i, int nb) {
    int n = nb - 1;
    if (192 <= i && i < 192 + nb) n += 6;
    return n;
}
__device__ __forceinline__ f32x2 pk_mul(f32x2 a, f32x2 b) { return a * b; }
__device__ __forceinline__ f32x2 pk_fma(f32x2 a, f32x2 b, f32x2 c) { return __builtin_elementwise_fma(a, b, c); }

__device__ __forceinline__ void k2_mlp(const __bf16* __restrict__ x, const __bf16* __restrict__ b1, const __bf16* __restrict__ b2, char* ws, char* lds, __bf16* __restrict__ out, int w, int j, int tt) {
    constexpr int NB = 20;
    const int lane = threadIdx.x & 63, r = lane & 31, hh = lane >> 5;
    const unsigned lds0 = (unsigned)(uintptr_t)lds, cnt = lds0 + K2_CNT;
    unsigned cv = 0;
    if ((CPF & 4) && blockIdx.x < 8 && w < 3) { uint64_t a; CODE_ADDR(a, K2_SYM); cv = code_touch(a, w, K2_CODE); }
    asm volatile("s_barrier" ::: "memory");
    __builtin_amdgcn_s_setprio(MLP_PRIO);
    lds_await(cnt + C_FULL, lds_peek(cnt + C_FULL), 8);
    if (CPF & 4) CODE_DONE(cv);
    const int l16 = lane * 16;
    f32x4 ts; float csv[3]; unsigned b1v[3];
    {
        const char* tp = ws + WS_TOK + (size_t)tt * 512;
        const char* cp = ws + WS_CS1 + (j * 384 + w * 96) * 4;
        const char* bp = (const char*)b1 + (j * 384 + w * 96) * 2;
        asm volatile("global_load_dwordx4 %0, %1, %2" : "=v"(ts) : "v"(r * 16), "s"(tp));
        asm volatile("global_load_dword %0, %3, %4\n\tglobal_load_dword %1, %3, %4 offset:128\n\tglobal_load_dword %2, %3, %4 offset:256" : "=&v"(csv[0]), "=&v"(csv[1]), "=&v"(csv[2]) : "v"(r * 4), "s"(cp));
        asm volatile("global_load_ushort %0, %3, %4\n\tglobal_load_ushort %1, %3, %4 offset:64\n\tglobal_load_ushort %2, %3, %4 offset:128" : "=&v"(b1v[0]), "=&v"(b1v[1]), "=&v"(b1v[2]) : "v"(r * 2), "s"(bp));
    }
    {
        const int row = 8 * w + (lane >> 3);
        const char* xs = (const char*)x + (size_t)(tt * 32 + row) * (D_ * 2) + (((lane & 7) ^ ((row >> 1) & 7)) * 16);
#pragma unroll
        for (int st = 0; st < 10; ++st) glds16(xs + st * 128, lds0 + K2_X + st * 4096 + w * 1024);
    }
    const uint64_t wbase = (uint64_t)(ws + WS_PW + (size_t)(j * 4 + w) * 240 * 1024);
    const u32x4 wr = {(unsigned)wbase, (unsigned)(wbase >> 32) & 0xffffu, 240u * 1024u, 0x00020000u};
    const uint64_t obase = (uint64_t)(ws + WS_PWO + (size_t)fin_ft(w >> 1) * 40 * 1024);
    const u32x4 wr2 = {(unsigned)obase, (unsigned)(obase >> 32) & 0xffffu, 40u * 1024u, 0x00020000u};
    const int okq = w & 1;
    bf16x8 wb[NB];
#define ABL_LIM 240
#define WLD(i) do { if ((i) < NB) asm volatile("buffer_load_dwordx4 %0, %1, %2, %3 offen offset:%4" : "=v"(wb[(i) % NB]) : "v"(l16), "s"(wr), "s"(((i) >> 2) * 4096), "n"(((i) & 3) * 1024)); \
        else if ((i) < ABL_LIM) asm volatile("buffer_load_dwordx4 %0, %1, %2, %3 offen offset:%4" : "+v"(wb[(i) % NB]) : "v"(l16), "s"(wr), "s"(((i) >> 2) * 4096), "n"(((i) & 3) * 1024)); \
        else if ((i) >= 240) asm volatile("buffer_load_dwordx4 %0, %1, %2, %3 offen offset:%4" : "+v"(wb[(i) % NB]) : "v"(l16), "s"(wr2), "s"(wo_frag(okq, ((i) - 240) & ~3) * 1024), "n"(((i) & 3) * 1024)); \
        SBAR(); } while (0)
#define WWAIT(i) asm volatile("s_waitcnt vmcnt(%1)" : "+v"(wb[(i) % NB]) : "n"(mlp_cnt(i, NB)))
#define MMA(i, b, c) ({ if (MLP_SLEEP) __builtin_amdgcn_s_sleep(MLP_SLEEP); WWAIT(i); mfma16(wb[(i) % NB], b, c); })
#pragma unroll
    for (int i = 0; i < NB; ++i) WLD(i);
    asm volatile("s_waitcnt vmcnt(%7)" : "+v"(ts), "+v"(csv[0]), "+v"(csv[1]), "+v"(csv[2]), "+v"(b1v[0]), "+v"(b1v[1]), "+v"(b1v[2]) : "n"(NB) : "memory");
    lds_signal(cnt + C_X);
    lds_await(cnt + C_X, lds_peek(cnt + C_X), 4);
    STAMPW(2, 1, 8);
#define SERVICE(NVM, cf, ci) STAMPW(2, (cf) - 1, 8)
    unsigned xa[4];
#pragma unroll
    for (int k = 0; k < 4; ++k) { xa[k] = lds0 + K2_X + r * 128 + (((2 * k + hh) ^ ((r >> 1) & 7)) * 16); PIN(xa[k]); }
    f32x16 acc[3]; acc[0] = f32x16{}; acc[1] = f32x16{}; acc[2] = f32x16{};
    bf16x8 xf[2];
    xf[0] = lds_r128((lds_cptr)(uintptr_t)xa[0]);
#pragma unroll
    for (int ks = 0; ks < 40; ++ks) {
        if (ks == 12) SERVICE(24, 3, 4);
        if (ks == 26) SERVICE(24, 4, 5);
        if (ks + 1 < 40) xf[(ks + 1) & 1] = lds_r128((lds_cptr)(uintptr_t)xa[(ks + 1) & 3] + ((ks + 1) >> 2) * 4096);
        SBAR();
#pragma unroll
        for (int f = 0; f < 3; ++f) {
            acc[f] = MMA(3 * ks + f, xf[ks & 1], acc[f]);
            SBAR();
            WLD(3 * ks + f + NB);
        }
        SBAR();
    }
    SERVICE(24, 5, 6);
    __builtin_amdgcn_s_setprio(GELU_PRIO);
    {
        unsigned m1, m2, m3; split3(hh ? ts[2] : -ts[0], m1, m2, m3);
        u32x4 tf = {m1 | (m1 << 16), m2 | (m1 << 16), m2 | (m3 << 16), m2 | (m3 << 16)};
        if (hh) tf = u32x4{m1 | (m2 << 16), m3, 0, 0};
#pragma unroll
        for (int f = 0; f < 3; ++f) {
            unsigned a1, a2, a3; split3(csv[f], a1, a2, a3);
            u32x4 ff = {a1 | (a2 << 16), a1 | (a3 << 16), a2 | (a1 << 16), a3 | (a2 << 16)};
            if (hh) ff = u32x4{b1v[f] | (b1v[f] << 16), b1v[f], 0, 0};
            acc[f] = mfma16(__builtin_bit_cast(bf16x8, ff), __builtin_bit_cast(bf16x8, tf), acc[f]);
            SBAR();
        }
    }
    {
        constexpr float C0 = -2.0f * 0.7978845608028654f * 1.4426950408889634f, C1 = C0 * 0.044715f;
        const float s2 = ts[1], sg = ts[2];
        const f32x2 k0 = {C0 * s2, C0 * s2}, k1 = {C1 * s2 * s2 * s2, C1 * s2 * s2 * s2}, sg2 = {sg, sg};
        __attribute__((address_space(3))) u32x4* hp = (__attribute__((address_space(3))) u32x4*)(uintptr_t)(lds0 + K2_H + w * 6144 + lane * 16);
#pragma unroll
        for (int f = 0; f < 3; ++f) {
#pragma unroll
            for (int s = 0; s < 2; ++s) {
                u32x4 u;
#pragma unroll
                for (int i = 0; i < 4; ++i) {
                    const f32x2 a = {acc[f][8 * s + 2 * i], acc[f][8 * s + 2 * i + 1]};
                    const f32x2 z = pk_mul(a, pk_fma(pk_mul(a, a), k1, k0));
                    const f32x2 e = {__builtin_amdgcn_exp2f(z[0]), __builtin_amdgcn_exp2f(z[1])};
                    const f32x2 d = pk_fma(e, sg2, sg2);
                    const f32x2 hv = pk_mul(a, f32x2{__builtin_amdgcn_rcpf(d[0]), __builtin_amdgcn_rcpf(d[1])});
                    u[i] = pk2(hv[0], hv[1]);
                }
                hp[(2 * f + s) * 64] = u;
                SBAR();
            }
        }
    }
    asm volatile("s_waitcnt lgkmcnt(0)" ::: "memory");
    lds_signal(cnt + C_H);
    __builtin_amdgcn_s_setprio(MLP_PRIO);
    lds_await(cnt + C_H, lds_peek(cnt + C_H), 4);
    STAMPW(2, 8, 8);
    unsigned ha = lds0 + K2_H + lane * 16;
    PIN(ha);
    const lds_cptr hb = (lds_cptr)(uintptr_t)ha;
    char* pp = ws + WS_PART + (((size_t)j * 64 + tt) * 20 + w * 5) * 2048 + lane * 16;
    auto store_tile = [&](const f32x16& t, int f) {
#pragma unroll
        for (int c = 0; c < 2; ++c) {
            float a[8];
#pragma unroll
            for (int i = 0; i < 8; ++i) a[i] = t[8 * c + i];
            st16(pp + (f * 2 + c) * 1024, u32x4{pk2(a[0], a[1]), pk2(a[2], a[3]), pk2(a[4], a[5]), pk2(a[6], a[7])});
            SBAR();
        }
    };
    acc[0] = f32x16{}; acc[1] = f32x16{}; acc[2] = f32x16{};
    xf[0] = lds_r128(hb);
#pragma unroll
    for (int ks = 0; ks < 24; ++ks) {
        if (ks == 10) SERVICE(24, 6, 7);
        xf[(ks + 1) & 1] = lds_r128(hb + ((ks + 1) % 24) * 1024);
        SBAR();
#pragma unroll
        for (int f = 0; f < 3; ++f) {
            acc[f] = MMA(120 + 3 * ks + f, xf[ks & 1], acc[f]);
            SBAR();
            WLD(120 + 3 * ks + f + NB);
        }
        SBAR();
    }
    SERVICE(24, 7, 8);
    store_tile(acc[0], 0); store_tile(acc[1], 1); store_tile(acc[2], 2);
    SBAR();
    f32x16 ac2[2]; ac2[0] = f32x16{}; ac2[1] = f32x16{};
#pragma unroll
    for (int ks = 0; ks < 24; ++ks) {
        if (ks + 1 < 24) xf[(ks + 1) & 1] = lds_r128(hb + (ks + 1) * 1024);
        SBAR();
#pragma unroll
        for (int f = 0; f < 2; ++f) {
            ac2[f] = MMA(192 + 2 * ks + f, xf[ks & 1], ac2[f]);
            SBAR();
            WLD(192 + 2 * ks + f + NB);
        }
        SBAR();
    }
    STAMPW(2, 9, 8);
#undef WLD
#undef SERVICE
    __builtin_amdgcn_s_setprio(TAILP);
    store_tile(ac2[0], 3); store_tile(ac2[1], 4);
    asm volatile("s_waitcnt vmcnt(0)" : "+v"(wb[0]), "+v"(wb[1]), "+v"(wb[2]), "+v"(wb[3]), "+v"(wb[4]), "+v"(wb[5]), "+v"(wb[6]), "+v"(wb[7]), "+v"(wb[8]), "+v"(wb[9]) :: "memory");
    asm volatile("" : "+v"(wb[10]), "+v"(wb[11]), "+v"(wb[12]), "+v"(wb[13]), "+v"(wb[14]), "+v"(wb[15]), "+v"(wb[16]), "+v"(wb[17]), "+v"(wb[18]), "+v"(wb[19]) :: "memory");
    {
        unsigned old = 0;
        if (lane == 0) old = __hip_atomic_fetch_add((__attribute__((address_space(3))) unsigned*)(uintptr_t)(cnt + C_MDONE), 1u, __ATOMIC_RELAXED, __HIP_MEMORY_SCOPE_WORKGROUP);
        if (__builtin_amdgcn_readfirstlane(old) == 3u) k2_count(ws, tt * 256 + 128);
    }
    STAMPW(2, 10, 8);
    k2_gather(x, b2, ws, lds, out, w);
    k2_tail(ws, lds, out, w >> 1, okq, wb);
}

__global__ __launch_bounds__(768, 3) void k2_main(const __bf16* __restrict__ x, const __bf16* __restrict__ b1, const __bf16* __restrict__ b2, char* ws, __bf16* __restrict__ out) {
    extern __shared__ __attribute__((aligned(16))) char lds[];
    const int wid = __builtin_amdgcn_readfirstlane(threadIdx.x >> 6);
    const int b = blockIdx.x, xcd = b & 7, head = xcd * 4 + ((b >> 3) & 3), qb = b >> 5;
    STAMPW(1, 0, AW);
    STAMPW(2, 0, 8);
    if (wid < 8) k2_attn(ws, lds, out, wid, head, qb);
    else k2_mlp(x, b1, b2, ws, lds, out, wid - 8, xcd & 3, (xcd >> 2) * 32 + (b >> 3));
}

}

extern "C" void kernel_launch(void* const* d_in, const int* in_sizes, int n_in, void* d_out, int out_size, void* d_ws, size_t ws_size, hipStream_t stream) {
    const __bf16* x = (const __bf16*)d_in[0];
    const __bf16* wqkv = (const __bf16*)d_in[1];
    const __bf16* wo = (const __bf16*)d_in[2];
    const __bf16* w1 = (const __bf16*)d_in[4];
    const __bf16* b1 = (const __bf16*)d_in[5];
    const __bf16* w2 = (const __bf16*)d_in[6];
    const __bf16* b2 = (const __bf16*)d_in[7];
    char* ws = (char*)d_ws;
    (void)hipFuncSetAttribute((const void*)k1_qkv, hipFuncAttributeMaxDynamicSharedMemorySize, K1_LDS);
    (void)hipFuncSetAttribute((const void*)k2_main, hipFuncAttributeMaxDynamicSharedMemorySize, K2_LDS);
    k1_qkv<<<256, 768, K1_LDS, stream>>>(x, wqkv, wo, w1, w2, ws);
    k2_main<<<256, 768, K2_LDS, stream>>>(x, b1, b2, ws, (__bf16*)d_out);
}
```

```cpp
#include <hip/hip_runtime.h>
#include <hip/hip_bf16.h>
#include <stdint.h>

namespace {

typedef __attribute__((ext_vector_type(8))) __bf16 bf16x8;
typedef __attribute__((ext_vector_type(2))) __bf16 bf16x2;
typedef __attribute__((ext_vector_type(8))) short s16x8;
typedef __attribute__((ext_vector_type(4))) short s16x4;
typedef __attribute__((ext_vector_type(16))) float f32x16;
typedef __attribute__((ext_vector_type(4))) float f32x4;
typedef __attribute__((ext_vector_type(2))) float f32x2;
typedef __attribute__((ext_vector_type(4))) unsigned u32x4;
typedef __attribute__((ext_vector_type(2))) unsigned u32x2;
typedef __attribute__((address_space(3))) char* lds_ptr;
typedef __attribute__((address_space(3))) const char* lds_cptr;

constexpr int S_ = 2048, D_ = 640, H_ = 32, F_ = 1536;
constexpr float EPS = 1e-5f;
constexpr float QSCALE = 0.22360679774997896f * 1.4426950408889634f;

constexpr int QT_B = 1280;
constexpr int VROW_B = 352;
constexpr int KVT_B = 1280 + 4 * VROW_B;
constexpr size_t WS_Q = 0;
constexpr size_t WS_KV = WS_Q + (size_t)H_ * 64 * QT_B;
constexpr size_t WS_TOK = WS_KV + (size_t)H_ * 64 * KVT_B + 4096;
constexpr size_t WS_CS1 = WS_TOK + (size_t)S_ * 16;
constexpr size_t WS_PW = WS_CS1 + 8192;
constexpr size_t WS_PWO = WS_PW + (size_t)16 * 240 * 1024;
constexpr size_t WS_PART = WS_PWO + (size_t)20 * 40 * 1024;
constexpr size_t WS_O = WS_PART + (size_t)4 * 64 * 20 * 2048;
constexpr size_t WS_RT = WS_O + (size_t)64 * 40 * 1024;
constexpr size_t WS_RC = WS_RT + (size_t)S_ * 16;
constexpr size_t WS_CNT = (WS_RC + (size_t)4 * 3 * D_ * 8 + 255) / 256 * 256;
constexpr size_t WS_END = WS_CNT + 64 * 256;

#define STAMPW(kid, i, w)
#define STAMP(kid, i) STAMPW(kid, i, 0)
#define SBAR() __builtin_amdgcn_sched_barrier(0)
#define PIN(x) asm volatile("" : "+v"(x))
#define WAIT_BAR(N) asm volatile("s_waitcnt vmcnt(" #N ") lgkmcnt(0)\n\ts_barrier" ::: "memory")
#define WAIT_VM(N) asm volatile("s_waitcnt vmcnt(" #N ")" ::: "memory")

__device__ __forceinline__ f32x16 mfma16(bf16x8 a, bf16x8 b, f32x16 c) { return __builtin_amdgcn_mfma_f32_32x32x16_bf16(a, b, c, 0, 0, 0); }
__device__ __forceinline__ f32x16 mfma8(s16x4 a, s16x4 b, f32x16 c) { return __builtin_amdgcn_mfma_f32_32x32x8bf16_1k(a, b, c, 0, 0, 0); }
__device__ __forceinline__ unsigned pk2(float lo, float hi) { f32x2 v = {lo, hi}; return __builtin_bit_cast(unsigned, __builtin_convertvector(v, bf16x2)); }
__device__ __forceinline__ float bf_lo(unsigned u) { return __uint_as_float(u << 16); }
__device__ __forceinline__ float bf_hi(unsigned u) { return __uint_as_float(u & 0xffff0000u); }
__device__ __forceinline__ void glds16(const void* g, unsigned lds_base) {
    unsigned sv;
    asm volatile("s_mov_b32 %0, m0\n\ts_mov_b32 m0, %2\n\ts_nop 0\n\tglobal_load_lds_dwordx4 %1, off\n\ts_mov_b32 m0, %0" : "=&s"(sv) : "v"(g), "s"(lds_base) : "memory");
}
__device__ __forceinline__ void glds4(const void* g, unsigned lds_base) {
    unsigned sv;
    asm volatile("s_mov_b32 %0, m0\n\ts_mov_b32 m0, %2\n\ts_nop 0\n\tglobal_load_lds_dword %1, off\n\ts_mov_b32 m0, %0" : "=&s"(sv) : "v"(g), "s"(lds_base) : "memory");
}
__device__ __forceinline__ bf16x8 lds_r128(lds_cptr p) { return *(const __attribute__((address_space(3))) bf16x8*)p; }
__device__ __forceinline__ s16x4 lds_r64(lds_cptr p) { return *(const __attribute__((address_space(3))) s16x4*)p; }
__device__ __forceinline__ s16x4 lds_tr(lds_cptr p) { return __builtin_amdgcn_ds_read_tr16_b64_v4i16((__attribute__((address_space(3))) s16x4*)p); }
__device__ __forceinline__ bf16x8 cat8(s16x4 a, s16x4 b) { s16x8 v = {a[0], a[1], a[2], a[3], b[0], b[1], b[2], b[3]}; return __builtin_bit_cast(bf16x8, v); }
__device__ __forceinline__ float dot2(unsigned a, unsigned b, float c) { return __builtin_amdgcn_fdot2_f32_bf16(__builtin_bit_cast(bf16x2, a), __builtin_bit_cast(bf16x2, b), c, false); }
__device__ __forceinline__ float swap32_add(float v) { auto rr = __builtin_amdgcn_permlane32_swap(__float_as_uint(v), __float_as_uint(v), false, false); return __uint_as_float(rr[0]) + __uint_as_float(rr[1]); }
__device__ __forceinline__ float swap32_max(float v) { auto rr = __builtin_amdgcn_permlane32_swap(__float_as_uint(v), __float_as_uint(v), false, false); return __builtin_fmaxf(__uint_as_float(rr[0]), __uint_as_float(rr[1])); }
__device__ __forceinline__ void split3(float v, unsigned& a, unsigned& b, unsigned& c) {
    a = pk2(v, 0.f) & 0xffffu; float r = v - bf_lo(a);
    b = pk2(r, 0.f) & 0xffffu; r = r - bf_lo(b);
    c = pk2(r, 0.f) & 0xffffu;
}
__device__ __forceinline__ void st16(void* p, u32x4 v) {
    asm volatile("global_store_dwordx4 %0, %1, off sc1\n\ts_nop 1" :: "v"(p), "v"(v) : "memory");
}
__device__ __forceinline__ void st8(void* p, u32x2 v) { asm volatile("global_store_dwordx2 %0, %1, off sc1\n\ts_nop 1" :: "v"(p), "v"(v) : "memory"); }
#define QST16(p, v) (*(u32x4*)(p) = (v))
#define QST8(p, v) (*(u32x2*)(p) = (v))
#define PST16(p, v) st16((p), (v))
constexpr unsigned ONES2 = 0x3f803f80u;
typedef unsigned long long u64;
__device__ __forceinline__ u64 ld_agent(const u64* p) { return __hip_atomic_load(p, __ATOMIC_RELAXED, __HIP_MEMORY_SCOPE_AGENT); }
__device__ __forceinline__ void st_agent(u64* p, float v, unsigned tag) { __hip_atomic_store(p, (u64)__float_as_uint(v) | ((u64)tag << 32), __ATOMIC_RELAXED, __HIP_MEMORY_SCOPE_AGENT); }
__device__ __forceinline__ unsigned now_ticks() { return (unsigned)__builtin_amdgcn_s_memrealtime(); }
#define K1_SPIN 4096
__device__ __forceinline__ void lds_signal(unsigned addr, unsigned val = 1u) {
    asm volatile("s_mov_b64 exec, 1\n\tds_add_u32 %0, %1\n\ts_mov_b64 exec, -1" :: "v"(addr), "v"(val) : "memory");
}
__device__ __forceinline__ unsigned lds_peek(unsigned addr) { return *(const volatile __attribute__((address_space(3))) unsigned*)(uintptr_t)addr; }
__device__ __forceinline__ void lds_await(unsigned addr, unsigned first, unsigned target) {
    unsigned v = __builtin_amdgcn_readfirstlane(first);
    while (v < target) { __builtin_amdgcn_s_sleep(1); v = __builtin_amdgcn_readfirstlane(lds_peek(addr)); }
    asm volatile("" ::: "memory");
}

#define K1_SYM "_ZN12_GLOBAL__N_16k1_qkvEPKDF16bS1_S1_S1_S1_Pc"
#define K2_SYM "_ZN12_GLOBAL__N_17k2_mainEPKDF16bS1_S1_PcPDF16b"
constexpr int K1_CODE = 32000, K2_CODE = 23000;
#define CODE_TOUCH(SYM, part, size) do { \
        uint64_t a_; unsigned v_; \
        asm volatile("s_getpc_b64 s[40:41]\n\ts_add_u32 s40, s40, " SYM "@rel32@lo+4\n\ts_addc_u32 s41, s41, " SYM "@rel32@hi+12\n\ts_mov_b64 %0, s[40:41]" : "=s"(a_) :: "s40", "s41", "scc"); \
        int o_ = (part) * 8192 + (int)(threadIdx.x & 63) * 128; \
        o_ = o_ < (size) ? o_ : (size); \
        asm volatile("global_load_dword %0, %1, %2" : "=v"(v_) : "v"(o_), "s"(a_) : "memory"); \
        asm volatile("s_waitcnt vmcnt(0)" : "+v"(v_) :: "memory"); } while (0)

constexpr int K1_STAGE = 16384 + 20480, K1_AUX = 4 * K1_STAGE, K1_LDS = K1_AUX + 4096 + 5120 + 1024 + 256;

__device__ __forceinline__ constexpr unsigned k1_perm(int j) {
    return j == 0 ? 0x76453120u : (j == 3 ? 0x57064231u : (j == 4 ? 0x76254130u : 0x75643120u));
}
__device__ __forceinline__ int k1_piece_off(int P, int r, int& head) {
    head = P / 5; int rr = P - head * 5; const int h7 = head & 7;
    int role = rr;
    if (h7 == 1) role = (0x32410 >> (4 * rr)) & 7;
    if (h7 == 3) role = (0x32104 >> (4 * rr)) & 7;
    return role == 4 ? 1024 + r * 8 : (role >> 1) * 512 + r * 16 + (role & 1) * 8;
}

constexpr int K1_TF = K1_AUX, K1_FF = K1_AUX + 4096, K1_RS = K1_AUX + 9216;

template <bool VORI, int WN>
__device__ __forceinline__ void k1_body(char* __restrict__ ws, char* lds, int mt, int nt) {
    constexpr int NF = WN ? 2 : 3, F0 = WN ? 3 : 0;
    const int tid = threadIdx.x, lane = tid & 63, r = lane & 31, hh = lane >> 5;
    const int wid = __builtin_amdgcn_readfirstlane(tid >> 6), wm = wid & 3;
    const unsigned lds0 = (unsigned)(uintptr_t)lds;
    STAMP(0, 0);
    const int xrow = 32 * wm + r;
    const int q4 = (lane & 15) >> 2, p4 = lane & 3, gi = (lane >> 4) & 1;
    unsigned xa[4], wa[NF];
#pragma unroll
    for (int k = 0; k < 4; ++k) xa[k] = lds0 + xrow * 128 + (((2 * k + hh) ^ ((xrow >> 1) & 7)) * 16);
#pragma unroll
    for (int f = 0; f < NF; ++f) {
        int rel = 4 * gi + p4;
        if (!VORI) rel = (k1_perm(F0 + f) >> (4 * rel)) & 7;
        wa[f] = lds0 + 16384 + (8 * hh + q4) * 320 + (F0 + f) * 64 + rel * 8;
    }
    const int tt = mt * 4 + wm;
    const bool isq = nt < 4;
    char* const base = (!VORI && isq) ? ws + WS_Q + (size_t)tt * QT_B : ws + WS_KV + (size_t)tt * KVT_B;
    unsigned so[NF][3];
#pragma unroll
    for (int f = 0; f < NF; ++f) {
        if (!VORI) {
            const unsigned hstride = isq ? 64 * QT_B : 64 * KVT_B;
            const int T = (nt & 3) * 5 + F0 + f;
            const unsigned perm = k1_perm(F0 + f);
#pragma unroll
            for (int i = 0; i < 3; ++i) {
                int head;
                const int off = k1_piece_off(8 * T + ((perm >> (4 * ((i ? 2 + 2 * i : 0) + hh))) & 7), r, head);
                so[f][i] = head * hstride + off;
                PIN(so[f][i]);
            }
        } else {
            const int col = (nt - 8) * 160 + (F0 + f) * 32 + r;
            const int head = col / 20, d = col - head * 20;
            so[f][0] = head * (64 * KVT_B) + 1280 + hh * VROW_B + d * 16;
            so[f][1] = d;
            PIN(so[f][0]); PIN(so[f][1]);
        }
    }
    f32x16 acc[NF];
#pragma unroll
    for (int f = 0; f < NF; ++f) acc[f] = f32x16{};
    bf16x8 xf[4], wf[4][NF];
#define K1_RD(sl, ks) do { xf[ks] = lds_r128((lds_cptr)(uintptr_t)(xa[ks] + (sl))); \
        _Pragma("unroll") for (int f = 0; f < NF; ++f) { const lds_cptr p = (lds_cptr)(uintptr_t)(wa[f] + (sl)) + (ks) * 5120; wf[ks][f] = cat8(lds_tr(p), lds_tr(p + 1280)); } SBAR(); } while (0)
#define K1_MM(b) do { _Pragma("unroll") for (int f = 0; f < NF; ++f) acc[f] = VORI ? mfma16(xf[b], wf[b][f], acc[f]) : mfma16(wf[b][f], xf[b], acc[f]); SBAR(); } while (0)
    asm volatile("s_barrier" ::: "memory");
    STAMP(0, 1);
    __builtin_amdgcn_s_waitcnt(0xc07f);
    K1_RD(0u, 0); K1_RD(0u, 1);
#pragma unroll 1
    for (int st = 0; st < 10; ++st) {
        const unsigned sl = (st & 3) * K1_STAGE, sn = ((st + 1) & 3) * K1_STAGE;
        K1_RD(sl, 2); K1_MM(0);
        K1_RD(sl, 3); K1_MM(1);
        if (st < 9) asm volatile("s_waitcnt lgkmcnt(0)\n\ts_barrier" ::: "memory");
        K1_RD(sn, 0); K1_MM(2);
        K1_RD(sn, 1); K1_MM(3);
    }
#undef K1_RD
#undef K1_MM
    STAMP(0, 3);
    asm volatile("s_barrier" ::: "memory");
    const lds_cptr aux = (lds_cptr)lds;
    const bf16x8 tfb = lds_r128(aux + K1_TF + wm * 1024 + lane * 16);
    bf16x8 ffb[NF];
#pragma unroll
    for (int f = 0; f < NF; ++f) ffb[f] = lds_r128(aux + K1_FF + (F0 + f) * 1024 + lane * 16);
    const float* rsl = (const float*)(lds + K1_RS) + wm * 32;
    float rs[VORI ? 16 : 1];
    if (VORI) {
#pragma unroll
        for (int g = 0; g < 4; ++g) { const f32x4 t = *(const f32x4*)(rsl + 8 * g + 4 * hh); rs[4 * g] = t[0]; rs[4 * g + 1] = t[1]; rs[4 * g + 2] = t[2]; rs[4 * g + 3] = t[3]; }
    } else rs[0] = rsl[r] * (isq ? QSCALE : 1.f);
#pragma unroll
    for (int f = 0; f < NF; ++f) acc[f] = VORI ? mfma16(tfb, ffb[f], acc[f]) : mfma16(ffb[f], tfb, acc[f]);
    STAMP(0, 4);
#pragma unroll
    for (int f = 0; f < NF; ++f) {
        unsigned u[8];
#pragma unroll
        for (int i = 0; i < 8; ++i) u[i] = pk2(acc[f][2 * i] * rs[VORI ? 2 * i : 0], acc[f][2 * i + 1] * rs[VORI ? 2 * i + 1 : 0]);
        if (!VORI) {
            QST16(base + so[f][0], (u32x4{u[0], u[1], u[2], u[3]}));
            QST8(base + so[f][1], (u32x2{u[4], u[5]}));
            QST8(base + so[f][2], (u32x2{u[6], u[7]}));
        } else {
            char* p = base + so[f][0];
            QST16(p, (u32x4{u[0], u[1], u[2], u[3]}));
            QST16(p + 2 * VROW_B, (u32x4{u[4], u[5], u[6], u[7]}));
            if (so[f][1] == 19) {
                QST16(p + 16, (u32x4{ONES2, ONES2, ONES2, ONES2})); QST16(p + 2 * VROW_B + 16, (u32x4{ONES2, ONES2, ONES2, ONES2}));
                QST16(p + 32, (u32x4{0, 0, 0, 0})); QST16(p + 2 * VROW_B + 32, (u32x4{0, 0, 0, 0}));
            }
        }
    }
}

template <bool VORI>
__device__ __forceinline__ void k1_helper(const __bf16* __restrict__ x, const __bf16* __restrict__ w, char* __restrict__ ws, char* lds, int mt, int nt, int hw) {
    const int lane = threadIdx.x & 63, r = lane & 31, hh = lane >> 5;
    const unsigned lds0 = (unsigned)(uintptr_t)lds;
    const unsigned t_start = now_ticks();
    unsigned xo[4], wo[5];
#pragma unroll
    for (int i = 0; i < 4; ++i) {
        const int row = 8 * (4 * hw + i) + (lane >> 3);
        xo[i] = row * (D_ * 2) + (((lane & 7) ^ ((row >> 1) & 7)) * 16) - i * 1024;
    }
#pragma unroll
    for (int i = 0; i < 5; ++i) {
        const int idx = (5 * hw + i) * 64 + lane, krow = idx / 20, ch = idx - krow * 20;
        wo[i] = krow * (3 * D_ * 2) + ch * 16 - (i & 3) * 1024;
    }
    const char* xs = (const char*)x + (size_t)mt * 128 * (D_ * 2);
    const char* wsr = (const char*)w + nt * 320;
#define K1_M0(v) asm volatile("s_mov_b32 m0, %0\n\ts_nop 0" :: "s"(v) : "memory")
#define K1_DMA(vo, sp, imm) asm volatile("global_load_lds_dwordx4 %0, %1 offset:" #imm :: "v"(vo), "s"(sp) : "memory")
#define K1_PIECE(i, xp, wp, lb) do { \
        if ((i) == 0) { K1_M0((lb) + hw * 4096); K1_DMA(xo[0], xp, 0); } \
        if ((i) == 1) K1_DMA(xo[1], xp, 1024); \
        if ((i) == 2) K1_DMA(xo[2], xp, 2048); \
        if ((i) == 3) K1_DMA(xo[3], xp, 3072); \
        if ((i) == 4) { K1_M0((lb) + 16384 + hw * 5120); K1_DMA(wo[0], wp, 0); } \
        if ((i) == 5) K1_DMA(wo[1], wp, 1024); \
        if ((i) == 6) K1_DMA(wo[2], wp, 2048); \
        if ((i) == 7) K1_DMA(wo[3], wp, 3072); \
        if ((i) == 8) { K1_M0((lb) + 16384 + hw * 5120 + 4096); K1_DMA(wo[4], wp, 0); } \
        SBAR(); } while (0)
#pragma unroll 1
    for (int st = 0; st < 4; ++st) {
        const char* xp = xs + st * 128; const char* wp = wsr + (size_t)st * 64 * (3 * D_ * 2);
        const unsigned lb = lds0 + st * K1_STAGE;
#pragma unroll
        for (int i = 0; i < 9; ++i) K1_PIECE(i, xp, wp, lb);
    }
    WAIT_BAR(27);
#pragma unroll 1
    for (int st = 0; st < 7; ++st) {
        if (st) {
            const char* xp = xs + (st + 3) * 128; const char* wp = wsr + (size_t)(st + 3) * 64 * (3 * D_ * 2);
            const unsigned lb = lds0 + ((st + 3) & 3) * K1_STAGE;
#pragma unroll
            for (int i = 0; i < 9; ++i) K1_PIECE(i, xp, wp, lb);
        }
        WAIT_BAR(18);
    }
#undef K1_PIECE
#undef K1_DMA
#undef K1_M0
    const int tok = mt * 128 + 32 * hw + r;
    int col[2];
#pragma unroll
    for (int f = 0; f < 2; ++f) {
        const int t = f ? 0 : hw + 1;
        const int pc = VORI ? (r >> 2) : (int)((k1_perm(t) >> (4 * (r >> 2))) & 7);
        col[f] = nt * 160 + t * 32 + pc * 4 + (r & 3);
    }
    const u64* rt = (const u64*)(ws + WS_RT) + tok * 2 + hh;
    const u64* rc = (const u64*)(ws + WS_RC) + hh * (2 * 3 * D_);
    u64 rec[5];
#define K1_POLL() do { rec[0] = ld_agent(rt); rec[1] = ld_agent(rc + col[0]); rec[2] = ld_agent(rc + 3 * D_ + col[0]); rec[3] = ld_agent(rc + col[1]); rec[4] = ld_agent(rc + 3 * D_ + col[1]); } while (0)
    K1_POLL();
    WAIT_BAR(14);
    WAIT_BAR(5);
    bool ok = false;
#pragma unroll 1
    for (int it = 0; it < K1_SPIN; ++it) {
        WAIT_VM(0);
        const unsigned span = now_ticks() - t_start + 32u;
        bool good = true;
#pragma unroll
        for (int i = 0; i < 5; ++i) good = good && (unsigned)(rec[i] >> 32) - t_start <= span;
        if (__all(good)) { ok = true; break; }
        __builtin_amdgcn_s_sleep(8);
        K1_POLL();
    }
#undef K1_POLL
    float tv = __uint_as_float((unsigned)rec[0]);
    float cs0 = __uint_as_float((unsigned)rec[1]) + __uint_as_float((unsigned)rec[2]), cs1 = __uint_as_float((unsigned)rec[3]) + __uint_as_float((unsigned)rec[4]);
    if (__builtin_expect(!ok, 0)) {
        const u32x4* rowp = (const u32x4*)(x + (size_t)tok * D_) + hh * 8;
        float mu = 0.f;
#pragma unroll 1
        for (int pass = 0; pass < 2; ++pass) {
            float s1 = 0.f, s2 = 0.f, s3 = 0.f;
#pragma unroll 1
            for (int c = 0; c < 8; ++c) {
                float a0 = 0.f, a1 = 0.f;
#pragma unroll 1
                for (int i = 0; i < 5; ++i) {
                    const u32x4 u = rowp[c + 16 * i];
#pragma unroll
                    for (int j = 0; j < 4; ++j) {
                        if (pass == 0) a0 = dot2(u[j], ONES2, a0);
                        else { const float d0 = bf_lo(u[j]) - mu, d1 = bf_hi(u[j]) - mu; a0 = __builtin_fmaf(d0, d0, a0); a1 = __builtin_fmaf(d1, d1, a1); }
                    }
                }
                if (pass) a0 += a1;
                s1 = (c & 1) ? s1 + a0 : a0;
                if (c & 1) s2 = (c & 2) ? s2 + s1 : s1;
                if ((c & 3) == 3) s3 = (c & 4) ? s3 + s2 : s2;
            }
            float tot = swap32_add(s3) * (1.f / D_);
            PIN(tot);
            if (pass == 0) mu = tot; else tv = hh ? __builtin_amdgcn_rsqf(tot + EPS) : mu;
        }
        float q[2][2];
#pragma unroll
        for (int kq = 0; kq < 2; ++kq) {
            const unsigned short* wc = (const unsigned short*)w + (size_t)(2 * hh + kq) * 160 * (3 * D_);
            float t0[4] = {0.f, 0.f, 0.f, 0.f}, t1[4] = {0.f, 0.f, 0.f, 0.f};
#pragma unroll 1
            for (int wv = 0; wv < 8; ++wv) {
#pragma unroll
                for (int g = 0; g < 4; ++g) {
                    const unsigned short* rw = wc + (wv * 20 + g) * (3 * D_);
                    float c0 = bf_lo(rw[col[0]]), c1 = bf_lo(rw[col[1]]);
#pragma unroll
                    for (int i = 1; i < 5; ++i) { c0 += bf_lo(rw[4 * i * (3 * D_) + col[0]]); c1 += bf_lo(rw[4 * i * (3 * D_) + col[1]]); }
                    t0[g] += c0; t1[g] += c1;
                }
            }
            q[0][kq] = (t0[0] + t0[1]) + (t0[2] + t0[3]); q[1][kq] = (t1[0] + t1[1]) + (t1[2] + t1[3]);
        }
        cs0 = q[0][0] + q[0][1]; cs1 = q[1][0] + q[1][1];
    }
    {
        unsigned m1, m2, m3; split3(-tv, m1, m2, m3);
        u32x4 tf = {m1 | (m1 << 16), m2 | (m1 << 16), m2 | (m3 << 16), m2 | (m3 << 16)};
        if (hh) tf = u32x4{0, 0, 0, 0};
        *(u32x4*)(lds + K1_TF + hw * 1024 + lane * 16) = tf;
        unsigned a1, a2, a3; split3(swap32_add(cs0), a1, a2, a3);
        *(u32x4*)(lds + K1_FF + (hw + 1) * 1024 + lane * 16) = u32x4{a1 | (a2 << 16), a1 | (a3 << 16), a2 | (a1 << 16), a3 | (a2 << 16)};
        if (hw == 0) {
            split3(swap32_add(cs1), a1, a2, a3);
            *(u32x4*)(lds + K1_FF + lane * 16) = u32x4{a1 | (a2 << 16), a1 | (a3 << 16), a2 | (a1 << 16), a3 | (a2 << 16)};
        }
        if (hh) ((float*)(lds + K1_RS))[32 * hw + r] = tv;
    }
    asm volatile("s_waitcnt lgkmcnt(0)\n\ts_barrier" ::: "memory");
}

struct K1Stat { u32x4 xr[5], wr[5]; };
__device__ __forceinline__ void k1_stat_load(K1Stat& sv, const __bf16* __restrict__ x, const __bf16* __restrict__ w, char* __restrict__ ws, int job) {
    const int tid = threadIdx.x, lane = tid & 63, wid = tid >> 6;
    const u32x4* xp = (const u32x4*)(x + (size_t)(job * 32 + wid * 4 + (lane >> 4)) * D_) + (lane & 15);
    const int jc = job < 60 ? job : 59;
    const u32x4* wp = (const u32x4*)(w + (size_t)((jc / 15) * 160 + wid * 20 + (lane >> 4)) * (3 * D_) + (jc % 15) * 128) + (lane & 15);
#pragma unroll
    for (int i = 0; i < 5; ++i) sv.xr[i] = xp[16 * i];
#pragma unroll
    for (int i = 0; i < 5; ++i) sv.wr[i] = wp[(size_t)i * 4 * (3 * D_ / 8)];
}
__device__ __forceinline__ float red16(float v) {
    v += __shfl_xor(v, 1); v += __shfl_xor(v, 2); v += __shfl_xor(v, 4); v += __shfl_xor(v, 8);
    return v;
}
__device__ __forceinline__ void k1_stat(const K1Stat& sv, char* __restrict__ ws, char* lds, int job) {
    const int tid = threadIdx.x, lane = tid & 63, wid = tid >> 6;
    float a = 0.f;
#pragma unroll
    for (int i = 0; i < 5; ++i)
#pragma unroll
        for (int j = 0; j < 4; ++j) a = dot2(sv.xr[i][j], ONES2, a);
    float mu = red16(a) * (1.f / D_);
    PIN(mu);
    float a0 = 0.f, a1 = 0.f;
#pragma unroll
    for (int i = 0; i < 5; ++i)
#pragma unroll
        for (int j = 0; j < 4; ++j) { const float d0 = bf_lo(sv.xr[i][j]) - mu, d1 = bf_hi(sv.xr[i][j]) - mu; a0 = __builtin_fmaf(d0, d0, a0); a1 = __builtin_fmaf(d1, d1, a1); }
    float var = red16(a0 + a1) * (1.f / D_);
    PIN(var);
    const int tok = job * 32 + wid * 4 + (lane >> 4), c = lane & 15;
    float pub = c ? __builtin_amdgcn_rsqf(var + EPS) : mu;
    PIN(pub);
    if (c < 2) st_agent((u64*)(ws + WS_RT) + tok * 2 + c, pub, now_ticks());
    if (c == 2) {
        const float sig2 = __builtin_sqrtf(var * (1.f + EPS) + EPS * EPS);
        *(f32x4*)(ws + WS_TOK + (size_t)tok * 16) = f32x4{mu, 1.0f / sig2, sig2, 0.f};
    }
    float cs[8];
#pragma unroll
    for (int j = 0; j < 4; ++j) {
        cs[2 * j] = bf_lo(sv.wr[0][j]); cs[2 * j + 1] = bf_hi(sv.wr[0][j]);
#pragma unroll
        for (int i = 1; i < 5; ++i) { cs[2 * j] += bf_lo(sv.wr[i][j]); cs[2 * j + 1] += bf_hi(sv.wr[i][j]); }
    }
    float* red = (float*)(lds + 86016);
    *(f32x4*)(red + (wid * 4 + (lane >> 4)) * 128 + c * 8) = f32x4{cs[0], cs[1], cs[2], cs[3]};
    *(f32x4*)(red + (wid * 4 + (lane >> 4)) * 128 + c * 8 + 4) = f32x4{cs[4], cs[5], cs[6], cs[7]};
    asm volatile("s_waitcnt lgkmcnt(0)\n\ts_barrier" ::: "memory");
    if (tid < 128 && job < 60) {
        float t[4] = {0.f, 0.f, 0.f, 0.f};
#pragma unroll
        for (int i = 0; i < 32; ++i) t[i & 3] += red[i * 128 + tid];
        st_agent((u64*)(ws + WS_RC) + (job / 15) * (3 * D_) + (job % 15) * 128 + tid, (t[0] + t[1]) + (t[2] + t[3]), now_ticks());
    }
}

template <int KIND>
__device__ __forceinline__ void k1_pack(const __bf16* __restrict__ wsrc, char* __restrict__ ws, char* lds, int strip, int third, const K1Stat& sv, int job) {
    constexpr int NI = KIND == 1 ? 8 : 10, NKS = KIND == 1 ? 4 : 5, ROWB = (KIND == 0 ? F_ : D_) * 2;
    const int tid = threadIdx.x, lane = tid & 63, r = lane & 31, hh = lane >> 5;
    const int wid = __builtin_amdgcn_readfirstlane(tid >> 6);
    const unsigned lds0 = (unsigned)(uintptr_t)lds + wid * 10240;
    const int row0 = third * 512 + wid * NI * 8;
    const char* src = (const char*)wsrc + (size_t)(row0 + (lane >> 3)) * ROWB + strip * 128 + (((lane & 7) ^ (((lane >> 4) & 1) * 4)) * 16);
#pragma unroll
    for (int i = 0; i < NI; ++i) glds16(src + (size_t)i * 8 * ROWB, lds0 + i * 1024);
    const int q4 = (lane & 15) >> 2, p4 = lane & 3, gi = (lane >> 4) & 1;
    int pc = 4 * gi + p4;
    if (KIND != 0) pc = (0x75643120u >> (4 * pc)) & 7;
    const lds_cptr sb = (lds_cptr)lds + wid * 10240;
    float cs[2] = {0.f, 0.f};
    k1_stat(sv, ws, lds, job);
    WAIT_VM(0);
#pragma unroll
    for (int ks = 0; ks < NKS; ++ks) {
        int ra, rb;
        if (KIND == 0) { ra = 16 * ks + 8 * hh; rb = ra + 4; }
        else if (KIND == 1) { ra = 16 * ks + 4 * hh; rb = ra + 8; }
        else if (ks < 4) { ra = 20 * ks + 4 * hh; rb = ra + 8; }
        else { ra = 40 * hh + 16; rb = ra + 20; }
#pragma unroll
        for (int f = 0; f < 2; ++f) {
            const int co = ((((f * 8 + pc) >> 1) ^ ((q4 >> 1) * 4)) * 16) + (pc & 1) * 8;
            const bf16x8 fr = cat8(lds_tr(sb + (ra + q4) * 128 + co), lds_tr(sb + (rb + q4) * 128 + co));
            const int ft = strip * 2 + f;
            size_t dst;
            if (KIND == 0) dst = WS_PW + ((size_t)(ft / 3) * 240 + (wid * 5 + ks) * 3 + ft % 3) * 1024;
            else if (KIND == 1) { const int g = third * 32 + wid * 4 + ks; const int f5 = ft % 5; dst = WS_PW + ((size_t)((g / 24) * 4 + ft / 5) * 240 + (f5 < 3 ? 120 + (g % 24) * 3 + f5 : 192 + (g % 24) * 2 + f5 - 3)) * 1024; }
            else dst = WS_PWO + ((size_t)ft * 40 + (ks < 4 ? wid * 4 + ks : 32 + wid)) * 1024;
            PST16(ws + dst + lane * 16, __builtin_bit_cast(u32x4, fr));
            if (KIND == 0) {
                const u32x4 a = __builtin_bit_cast(u32x4, fr);
#pragma unroll
                for (int i = 0; i < 4; ++i) cs[f] = dot2(a[i], ONES2, cs[f]);
            }
        }
    }
    if (KIND == 0) {
        float* red = (float*)(lds + 81920);
        red[(wid * 2 + hh) * 64 + r] = cs[0];
        red[(wid * 2 + hh) * 64 + 32 + r] = cs[1];
        __syncthreads();
        if (tid < 64) {
            float s = 0.f;
#pragma unroll
            for (int i = 0; i < 16; ++i) s += red[i * 64 + tid];
            ((float*)(ws + WS_CS1))[strip * 64 + tid] = s;
        }
    }
}

__global__ __launch_bounds__(768, 3) void k1_qkv(const __bf16* __restrict__ x, const __bf16* __restrict__ wqkv, const __bf16* __restrict__ wo, const __bf16* __restrict__ w1,
                                                 const __bf16* __restrict__ w2, char* __restrict__ ws) {
    extern __shared__ __attribute__((aligned(16))) char lds[];
    const int b = blockIdx.x, xcd = b & 7, idx = b >> 3;
    const int wid = __builtin_amdgcn_readfirstlane(threadIdx.x >> 6);
    if (idx == 0 && wid < 4) CODE_TOUCH(K1_SYM, wid, K1_CODE);
    if (idx >= 24) {
        if (wid >= 8) {
            if (idx == 25 && wid < 11) CODE_TOUCH(K2_SYM, wid - 8, K2_CODE);
            return;
        }
        const int job = (idx - 24) * 8 + xcd;
        if (threadIdx.x < 2) __hip_atomic_store((unsigned*)(ws + WS_CNT + job * 256 + threadIdx.x * 128), 0u, __ATOMIC_RELAXED, __HIP_MEMORY_SCOPE_AGENT);
        K1Stat sv;
        k1_stat_load(sv, x, wqkv, ws, job);
        if (job < 24) k1_pack<0>(w1, ws, lds, job, 0, sv, job);
        else if (job < 54) k1_pack<1>(w2, ws, lds, (job - 24) % 10, (job - 24) / 10, sv, job);
        else k1_pack<2>(wo, ws, lds, job - 54, 0, sv, job);
        STAMP(3, 0);
        return;
    }
    const int mt = (xcd >> 1) * 4 + (idx & 3), nt = (xcd & 1) * 6 + (idx >> 2);
    if (wid >= 8) { if (nt < 8) k1_helper<false>(x, wqkv, ws, lds, mt, nt, wid - 8); else k1_helper<true>(x, wqkv, ws, lds, mt, nt, wid - 8); return; }
    if (nt < 8) { if (wid >= 4) k1_body<false, 1>(ws, lds, mt, nt); else k1_body<false, 0>(ws, lds, mt, nt); }
    else { if (wid >= 4) k1_body<true, 1>(ws, lds, mt, nt); else k1_body<true, 0>(ws, lds, mt, nt); }
    STAMP(0, 5);
}

#define MLP_NB 20
#define AP_HI 2
#define AP_MID 1
#define AP_LO 0
#define GELU_PRIO 1
#define MLP_SLEEP 0
#define AW 7
#define MLP_PRIO 0
template <int N> __device__ __forceinline__ void wait_vm() { asm volatile("s_waitcnt vmcnt(%0)" :: "n"(N) : "memory"); }
#define WAIT_VMX(N) wait_vm<(N)>()
constexpr int AT_SLOT = 8 * KVT_B;
constexpr int K2_X = 4 * AT_SLOT, K2_H = K2_X + 40960, K2_CNT = K2_H + 24576, K2_LDS = K2_CNT + 128;
constexpr int C_FULL = 0, C_FREE = 64, C_X = 112, C_H = 116, C_PRDY = 120, C_MDONE = 124;

__device__ __forceinline__ void attn_slow(const char* __restrict__ kv, bf16x8 qf, s16x4 qt, f32x16& o, int r, int hh) {
    float m = -INFINITY;
    o = f32x16{};
    if (hh) qt = s16x4{0, 0, 0, 0};
    for (int t = 0; t < 64; ++t) {
        const char* p = kv + (size_t)t * KVT_B;
        const bf16x8 kf = *(const bf16x8*)(p + hh * 512 + r * 16);
        const s16x4 kt = *(const s16x4*)(p + 1024 + r * 8);
        const int rv = r < 20 ? r : 20;
        const bf16x8 v0 = *(const bf16x8*)(p + 1280 + hh * VROW_B + rv * 16);
        const bf16x8 v1 = *(const bf16x8*)(p + 1280 + 2 * VROW_B + hh * VROW_B + rv * 16);
        f32x16 s = mfma16(kf, qf, f32x16{});
        s = mfma8(kt, qt, s);
        float tm = s[0];
#pragma unroll
        for (int i = 1; i < 16; ++i) tm = __builtin_fmaxf(tm, s[i]);
        tm = swap32_max(tm);
        const float mn = __builtin_fmaxf(m, tm);
        const float al = __builtin_amdgcn_exp2f(m - mn);
        m = mn;
        u32x4 p0, p1;
#pragma unroll
        for (int i = 0; i < 4; ++i) {
            p0[i] = pk2(__builtin_amdgcn_exp2f(s[2 * i] - m), __builtin_amdgcn_exp2f(s[2 * i + 1] - m));
            p1[i] = pk2(__builtin_amdgcn_exp2f(s[8 + 2 * i] - m), __builtin_amdgcn_exp2f(s[8 + 2 * i + 1] - m));
        }
#pragma unroll
        for (int i = 0; i < 16; ++i) o[i] *= al;
        o = mfma16(v0, __builtin_bit_cast(bf16x8, p0), o);
        o = mfma16(v1, __builtin_bit_cast(bf16x8, p1), o);
    }
}

__device__ __forceinline__ void attn_finish(char* __restrict__ ws, const char* kvh, bf16x8 qf, s16x4 qt, f32x16 o, int qt_idx, int head, int lane) {
    const int r = lane & 31, hh = lane >> 5;
    float l = swap32_add(hh ? o[8] : 0.f);
    bool bad = !(l < 1e30f);
#pragma unroll
    for (int i = 0; i < 8; ++i) bad |= !(__builtin_fabsf(o[i]) < 3e38f);
#pragma unroll
    for (int i = 8; i < 12; ++i) bad |= (hh == 0) && !(__builtin_fabsf(o[i]) < 3e38f);
    if (__builtin_expect(__any(bad), 0)) {
        attn_slow(kvh, qf, qt, o, r, hh);
        l = swap32_add(hh ? o[8] : 0.f);
    }
    const float inv = 1.0f / l;
    char* ob = ws + WS_O + (size_t)qt_idx * 40 * 1024;
    st16(ob + head * 1024 + lane * 16, u32x4{pk2(o[0] * inv, o[1] * inv), pk2(o[2] * inv, o[3] * inv), pk2(o[4] * inv, o[5] * inv), pk2(o[6] * inv, o[7] * inv)});
    if (hh == 0) st8(ob + (32 + (head >> 2)) * 1024 + (head & 3) * 256 + r * 8, u32x2{pk2(o[8] * inv, o[9] * inv), pk2(o[10] * inv, o[11] * inv)});
}

#define K2_SPIN 400000
#define AP_CUT 8
#define TAILP 0
#define TAIL_PD 6
#define WO_PRE 12
__device__ __forceinline__ int fin_tt() { return (blockIdx.x & 7) * 8 + (blockIdx.x >> 5); }
__device__ __forceinline__ int fin_ft(int f) { return ((blockIdx.x >> 3) & 3) * 5 + f; }
__device__ __forceinline__ int wo_frag(int kq, int k) { return k < 16 ? kq * 16 + k : 32 + kq * 4 + (k - 16); }
#define BAR() asm volatile("s_waitcnt lgkmcnt(0)\n\ts_barrier" ::: "memory")
__device__ __forceinline__ void k2_count(char* ws, int off) {
    if ((threadIdx.x & 63) == 0) __hip_atomic_fetch_add((unsigned*)(ws + WS_CNT + off), 1u, __ATOMIC_RELAXED, __HIP_MEMORY_SCOPE_AGENT);
}
__device__ __forceinline__ void k2_wait(const char* ws, int off, unsigned target) {
    const unsigned* c = (const unsigned*)(ws + WS_CNT + off);
#pragma unroll 1
    for (int it = 0; it < K2_SPIN; ++it) {
        if (__builtin_amdgcn_readfirstlane(__hip_atomic_load(c, __ATOMIC_RELAXED, __HIP_MEMORY_SCOPE_AGENT)) >= target) break;
        __builtin_amdgcn_s_sleep(1);
    }
    asm volatile("" ::: "memory");
}
constexpr int T_O = 0, T_RED = 40960, T_SUM = K2_X;
__device__ __forceinline__ void k2_gather(const __bf16* __restrict__ x, const __bf16* __restrict__ b2, char* ws, char* lds, int w) {
    const int lane = threadIdx.x & 63, r = lane & 31, hh = lane >> 5;
    const unsigned cnt = (unsigned)(uintptr_t)lds + K2_CNT;
    const int tt = fin_tt(), c0 = fin_ft(0) * 32 + hh * 8;
    u32x4 xv[3], bv[3], pv[3][4];
#pragma unroll
    for (int i = 0; i < 3; ++i) {
        const int u = w + 4 * i < 10 ? w + 4 * i : w;
        xv[i] = *(const u32x4*)(x + (size_t)(tt * 32 + r) * D_ + c0 + u * 16); bv[i] = *(const u32x4*)(b2 + c0 + u * 16);
    }
    SBAR();
    if (w == 0) { k2_wait(ws, tt * 256 + 128, 4u); lds_signal(cnt + C_PRDY); }
    else lds_await(cnt + C_PRDY, lds_peek(cnt + C_PRDY), 1);
    const __amdgpu_buffer_rsrc_t rp = __builtin_amdgcn_make_buffer_rsrc(ws + WS_PART + ((size_t)tt * 20 + fin_ft(0)) * 2048, 0, 3 * 64 * 20 * 2048 + 10240, 0x00020000);
#pragma unroll
    for (int i = 0; i < 3; ++i) {
        const int u = w + 4 * i < 10 ? w + 4 * i : w;
#pragma unroll
        for (int k = 0; k < 4; ++k) pv[i][k] = __builtin_amdgcn_raw_buffer_load_b128(rp, lane * 16, k * (64 * 20 * 2048) + u * 1024, 16);
    }
    SBAR();
#pragma unroll
    for (int i = 0; i < 3; ++i) {
        const int u = w + 4 * i < 10 ? w + 4 * i : w;
        float m[8];
#pragma unroll
        for (int k = 0; k < 4; ++k) {
            m[2 * k] = (bf_lo(xv[i][k]) + bf_lo(bv[i][k])) + ((bf_lo(pv[i][0][k]) + bf_lo(pv[i][1][k])) + (bf_lo(pv[i][2][k]) + bf_lo(pv[i][3][k])));
            m[2 * k + 1] = (bf_hi(xv[i][k]) + bf_hi(bv[i][k])) + ((bf_hi(pv[i][0][k]) + bf_hi(pv[i][1][k])) + (bf_hi(pv[i][2][k]) + bf_hi(pv[i][3][k])));
        }
        f32x4* d = (f32x4*)(lds + T_SUM + u * 2048) + lane;
        d[0] = f32x4{m[0], m[1], m[2], m[3]}; d[64] = f32x4{m[4], m[5], m[6], m[7]};
    }
    STAMPW(2, 11, 8);
}
__device__ __forceinline__ void k2_tail(char* ws, char* lds, __bf16* __restrict__ out, int f, int kq, const bf16x8 (&wf)[20]) {
    const int lane = threadIdx.x & 63, r = lane & 31, hh = lane >> 5;
    const int tt = fin_tt(), ft = fin_ft(f), g = kq * 5 + f;
    const __amdgpu_buffer_rsrc_t ro = __builtin_amdgcn_make_buffer_rsrc(ws + WS_O + ((size_t)tt * 40 + g * 4) * 1024, 0, 4096, 0x00020000);
    u32x4 ov[4];
    STAMP(4, 0);
    BAR();
    STAMP(4, 1);
#pragma unroll
    for (int k = 0; k < 4; ++k) ov[k] = __builtin_amdgcn_raw_buffer_load_b128(ro, lane * 16 + k * 1024, 0, 16);
    SBAR();
    const f32x4* sp = (const f32x4*)(lds + T_SUM + (f * 2 + kq) * 2048) + lane;
    const f32x4 s0 = sp[0], s1 = sp[64];
    SBAR();
#pragma unroll
    for (int k = 0; k < 4; ++k) *(u32x4*)(lds + T_O + (g * 4 + k) * 1024 + lane * 16) = ov[k];
    BAR();
    STAMP(4, 2);
    f32x16 acc = f32x16{};
    const lds_cptr ob = (lds_cptr)lds + T_O + kq * 16384 + lane * 16, ot = (lds_cptr)lds + T_O + (32 + kq * 4) * 1024 + hh * 512 + r * 8;
    constexpr int PD = TAIL_PD;
    bf16x8 of[PD];
#define OFRAG(k) ((k) < 16 ? lds_r128(ob + (k) * 1024) : cat8(lds_r64(ot + ((k) - 16) * 1024), lds_r64(ot + ((k) - 16) * 1024 + 256)))
#pragma unroll
    for (int k = 0; k < PD; ++k) of[k] = OFRAG(k);
    SBAR();
#pragma unroll
    for (int k = 0; k < 20; ++k) {
        acc = mfma16(wf[k], of[k % PD], acc);
        SBAR();
        if (k + PD < 20) of[k % PD] = OFRAG(k + PD);
        SBAR();
    }
#undef OFRAG
    STAMP(4, 3);
    f32x4* red = (f32x4*)(lds + T_RED + f * 4096);
    {
        const int c = 1 - kq;
        red[(2 * c) * 64 + lane] = kq ? f32x4{acc[0], acc[1], acc[2], acc[3]} : f32x4{acc[8], acc[9], acc[10], acc[11]};
        red[(2 * c + 1) * 64 + lane] = kq ? f32x4{acc[4], acc[5], acc[6], acc[7]} : f32x4{acc[12], acc[13], acc[14], acc[15]};
    }
    float m[8];
#pragma unroll
    for (int k = 0; k < 8; ++k) m[k] = (kq ? acc[8 + k] : acc[k]) + (k < 4 ? s0[k] : s1[k - 4]);
    BAR();
    {
        const f32x4 a = red[(2 * kq) * 64 + lane], bb = red[(2 * kq + 1) * 64 + lane];
        u32x4 u;
        u[0] = pk2(a[0] + m[0], a[1] + m[1]); u[1] = pk2(a[2] + m[2], a[3] + m[3]);
        u[2] = pk2(bb[0] + m[4], bb[1] + m[5]); u[3] = pk2(bb[2] + m[6], bb[3] + m[7]);
        *(u32x4*)(out + (size_t)(tt * 32 + r) * D_ + ft * 32 + kq * 16 + hh * 8) = u;
    }
    STAMP(4, 4);
}

__device__ __forceinline__ void k2_attn(char* ws, char* lds, __bf16* __restrict__ out, int wid, int head, int qb) {
    const int lane = threadIdx.x & 63, r = lane & 31, hh = lane >> 5;
    const unsigned lds0 = (unsigned)(uintptr_t)lds, cnt = lds0 + K2_CNT;
    const int qt_idx = qb * 8 + wid;
    const char* kvh = ws + WS_KV + (size_t)head * 64 * KVT_B;
    const char* src = kvh + wid * KVT_B + lane * 16;
    auto tile_issue = [&](int c) {
        const unsigned base = lds0 + (c & 3) * AT_SLOT + wid * KVT_B;
        glds16(src + c * AT_SLOT, base);
        glds16(src + c * AT_SLOT + 1024, base + 1024);
        if (lane < (KVT_B - 2048) / 16) glds16(src + c * AT_SLOT + 2048, base + 2048);
    };
    tile_issue(0);
    {
        const char* qp = ws + WS_Q + ((size_t)head * 64 + qt_idx) * QT_B + lane * 16;
        glds16(qp, lds0 + K2_H + wid * QT_B);
        if (lane < 16) glds16(qp + 1024, lds0 + K2_H + wid * QT_B + 1024);
    }
    if (wid == 0 && lane < 32) *(__attribute__((address_space(3))) unsigned*)(uintptr_t)(cnt + lane * 4) = lane == 8 ? 8u : 0u;
    asm volatile("s_waitcnt lgkmcnt(0)\n\ts_barrier" ::: "memory");
    const int koff = hh * 512 + r * 16, ktoff = hh ? 1600 : 1024 + r * 8, voff = 1280 + hh * VROW_B + (r < 21 ? r : 21) * 16;
    WAIT_VM(0);
    const lds_cptr qp = (lds_cptr)lds + K2_H + wid * QT_B;
    bf16x8 qf = lds_r128(qp + koff);
    s16x4 qt = lds_r64(qp + 1024 + r * 8);
    asm volatile("s_waitcnt lgkmcnt(0)" : "+v"(qf), "+v"(qt) :: "memory");
    lds_signal(cnt + C_FULL);
    tile_issue(1); tile_issue(2); tile_issue(3);
    lds_await(cnt + C_FULL, lds_peek(cnt + C_FULL), 8);
    STAMPW(1, 1, AW);
    f32x16 o = f32x16{}, sA, sB;
    u32x4 pA0, pA1, pB0 = {0, 0, 0, 0}, pB1 = {0, 0, 0, 0};
    bf16x8 kf, v0, v1; s16x4 kt;
    {
        const lds_cptr sb = (lds_cptr)lds;
        kf = lds_r128(sb + koff); kt = lds_r64(sb + ktoff);
        if (hh) qt = s16x4{0, 0, 0, 0};
        sA = mfma16(kf, qf, f32x16{});
        sA = mfma8(kt, qt, sA);
        float m0 = sA[0];
#pragma unroll
        for (int i = 1; i < 16; ++i) m0 = __builtin_fmaxf(m0, sA[i]);
        m0 = swap32_max(m0);
        const unsigned nm = pk2(-m0, 0.f) & 0xffffu;
        if (hh) qt[0] = (short)nm;
#pragma unroll
        for (int i = 0; i < 16; ++i) sA[i] += bf_lo(nm);
        kf = lds_r128(sb + KVT_B + koff); kt = lds_r64(sb + KVT_B + ktoff);
        v0 = lds_r128(sb + voff); v1 = v0;
    }
#define EX(v) __builtin_amdgcn_exp2f(v)
#define GRP(MF, SC, i, PW, j) do { MF; SBAR(); const float e0 = EX(SC[i]), e1 = EX(SC[i + 1]), e2 = EX(SC[i + 2]), e3 = EX(SC[i + 3]); PW[j] = pk2(e0, e1); PW[j + 1] = pk2(e2, e3); PIN(PW); SBAR(); } while (0)
#define STEP(SC, SN, PC0, PC1, PP0, PP1, KP, VP) do { \
        GRP(SN = mfma16(kf, qf, f32x16{}), SC, 0, PC0, 0); \
        kf = lds_r128((KP) + koff); SBAR(); \
        GRP(o = mfma16(v0, __builtin_bit_cast(bf16x8, PP0), o), SC, 4, PC0, 2); \
        v0 = lds_r128((VP) + voff); SBAR(); \
        GRP(SN = mfma8(kt, qt, SN), SC, 8, PC1, 0); \
        kt = lds_r64((KP) + ktoff); SBAR(); \
        GRP(o = mfma16(v1, __builtin_bit_cast(bf16x8, PP1), o), SC, 12, PC1, 2); \
        v1 = lds_r128((VP) + voff + 2 * VROW_B); SBAR(); \
    } while (0)
    if (wid >= 4) __builtin_amdgcn_s_setprio(AP_MID);
    for (int c = 0; c < 7; ++c) {
        const lds_cptr sb = (lds_cptr)lds + (c & 3) * AT_SLOT;
        const lds_cptr nb = (lds_cptr)lds + ((c + 1) & 3) * AT_SLOT;
        if (wid < 4) __builtin_amdgcn_s_setprio(AP_HI);
        STEP(sA, sB, pA0, pA1, pB0, pB1, sb + 2 * KVT_B, sb + 0 * KVT_B);
        STEP(sB, sA, pB0, pB1, pA0, pA1, sb + 3 * KVT_B, sb + 1 * KVT_B);
        STEP(sA, sB, pA0, pA1, pB0, pB1, sb + 4 * KVT_B, sb + 2 * KVT_B);
        if (c == 0) { WAIT_VM(6); lds_signal(cnt + C_FULL + 4); }
        STEP(sB, sA, pB0, pB1, pA0, pA1, sb + 5 * KVT_B, sb + 3 * KVT_B);
        STEP(sA, sB, pA0, pA1, pB0, pB1, sb + 6 * KVT_B, sb + 4 * KVT_B);
        if (wid < 4 && c < AP_CUT) __builtin_amdgcn_s_setprio(AP_LO);
        const unsigned pk = lds_peek(cnt + C_FULL + 4 * (c + 1)), pf = lds_peek(cnt + C_FREE + 4 * c - 4);
        SBAR();
        STEP(sB, sA, pB0, pB1, pA0, pA1, sb + 7 * KVT_B, sb + 5 * KVT_B);
        lds_await(cnt + C_FULL + 4 * (c + 1), pk, 8);
        if (c <= 4) {
            if (c >= 1) { lds_await(cnt + C_FREE + 4 * c - 4, pf, 8); tile_issue(c + 3); }
            WAIT_VM(3);
        } else WAIT_VM(0);
        if (c <= 5) lds_signal(cnt + C_FULL + 4 * (c + 2));
        SBAR();
        STEP(sA, sB, pA0, pA1, pB0, pB1, nb + 0 * KVT_B, sb + 6 * KVT_B);
        STEP(sB, sA, pB0, pB1, pA0, pA1, nb + 1 * KVT_B, sb + 7 * KVT_B);
        lds_signal(cnt + C_FREE + 4 * c);
        STAMPW(1, 2 + c, AW);
    }
    const int tf = 2 + (wid >> 1), tkq = wid & 1;
    const char* wop = ws + WS_PWO + (size_t)fin_ft(tf < 5 ? tf : 4) * 40 * 1024 + lane * 16;
    bf16x8 wf[20];
#pragma unroll
    for (int k = 0; k < WO_PRE; ++k) { wf[k] = *(const bf16x8*)(wop + (wid < 6 ? wo_frag(tkq, k) * 1024 : 0)); SBAR(); }
    {
        const lds_cptr sb = (lds_cptr)lds + 3 * AT_SLOT;
        if (wid < 4) __builtin_amdgcn_s_setprio(AP_HI);
        STEP(sA, sB, pA0, pA1, pB0, pB1, sb + 2 * KVT_B, sb + 0 * KVT_B);
        STEP(sB, sA, pB0, pB1, pA0, pA1, sb + 3 * KVT_B, sb + 1 * KVT_B);
        STEP(sA, sB, pA0, pA1, pB0, pB1, sb + 4 * KVT_B, sb + 2 * KVT_B);
        STEP(sB, sA, pB0, pB1, pA0, pA1, sb + 5 * KVT_B, sb + 3 * KVT_B);
        STEP(sA, sB, pA0, pA1, pB0, pB1, sb + 6 * KVT_B, sb + 4 * KVT_B);
        if (wid < 4 && 7 < AP_CUT) __builtin_amdgcn_s_setprio(AP_LO);
        STEP(sB, sA, pB0, pB1, pA0, pA1, sb + 7 * KVT_B, sb + 5 * KVT_B);
        STEP(sA, sB, pA0, pA1, pB0, pB1, sb + 7 * KVT_B, sb + 6 * KVT_B);
        STEP(sB, sA, pB0, pB1, pA0, pA1, sb + 7 * KVT_B, sb + 7 * KVT_B);
        STAMPW(1, 9, AW);
    }
    o = mfma16(v0, __builtin_bit_cast(bf16x8, pB0), o);
    o = mfma16(v1, __builtin_bit_cast(bf16x8, pB1), o);
#undef STEP
#undef GRP
#undef EX
    attn_finish(ws, kvh, qf, qt, o, qt_idx, head, lane);
    WAIT_VM(0);
    k2_count(ws, qt_idx * 256);
    STAMPW(1, 10, AW);
    __builtin_amdgcn_s_setprio(0);
    if (wid >= 6) {
        if (wid == 7) { k2_wait(ws, fin_tt() * 256, 32u); BAR(); }
        return;
    }
    SBAR();
#pragma unroll
    for (int k = WO_PRE; k < 20; ++k) { wf[k] = *(const bf16x8*)(wop + wo_frag(tkq, k) * 1024); SBAR(); }
    k2_tail(ws, lds, out, tf, tkq, wf);
}

constexpr int mlp_cnt(int i, int nb) {
    int n = nb - 1;
    if (192 <= i && i < 192 + nb) n += 6;
    return n;
}
__device__ __forceinline__ f32x2 pk_mul(f32x2 a, f32x2 b) { return a * b; }
__device__ __forceinline__ f32x2 pk_fma(f32x2 a, f32x2 b, f32x2 c) { return __builtin_elementwise_fma(a, b, c); }

__device__ __forceinline__ void k2_mlp(const __bf16* __restrict__ x, const __bf16* __restrict__ b1, const __bf16* __restrict__ b2, char* ws, char* lds, __bf16* __restrict__ out, int w, int j, int tt) {
    constexpr int NB = 20;
    const int lane = threadIdx.x & 63, r = lane & 31, hh = lane >> 5;
    const unsigned lds0 = (unsigned)(uintptr_t)lds, cnt = lds0 + K2_CNT;
    asm volatile("s_barrier" ::: "memory");
    __builtin_amdgcn_s_setprio(MLP_PRIO);
    lds_await(cnt + C_FULL, lds_peek(cnt + C_FULL), 8);
    const int l16 = lane * 16;
    f32x4 ts; float csv[3]; unsigned b1v[3];
    {
        const char* tp = ws + WS_TOK + (size_t)tt * 512;
        const char* cp = ws + WS_CS1 + (j * 384 + w * 96) * 4;
        const char* bp = (const char*)b1 + (j * 384 + w * 96) * 2;
        asm volatile("global_load_dwordx4 %0, %1, %2" : "=v"(ts) : "v"(r * 16), "s"(tp));
        asm volatile("global_load_dword %0, %3, %4\n\tglobal_load_dword %1, %3, %4 offset:128\n\tglobal_load_dword %2, %3, %4 offset:256" : "=&v"(csv[0]), "=&v"(csv[1]), "=&v"(csv[2]) : "v"(r * 4), "s"(cp));
        asm volatile("global_load_ushort %0, %3, %4\n\tglobal_load_ushort %1, %3, %4 offset:64\n\tglobal_load_ushort %2, %3, %4 offset:128" : "=&v"(b1v[0]), "=&v"(b1v[1]), "=&v"(b1v[2]) : "v"(r * 2), "s"(bp));
    }
    {
        const int row = 8 * w + (lane >> 3);
        const char* xs = (const char*)x + (size_t)(tt * 32 + row) * (D_ * 2) + (((lane & 7) ^ ((row >> 1) & 7)) * 16);
#pragma unroll
        for (int st = 0; st < 10; ++st) glds16(xs + st * 128, lds0 + K2_X + st * 4096 + w * 1024);
    }
    const uint64_t wbase = (uint64_t)(ws + WS_PW + (size_t)(j * 4 + w) * 240 * 1024);
    const u32x4 wr = {(unsigned)wbase, (unsigned)(wbase >> 32) & 0xffffu, 240u * 1024u, 0x00020000u};
    const uint64_t obase = (uint64_t)(ws + WS_PWO + (size_t)fin_ft(w >> 1) * 40 * 1024);
    const u32x4 wr2 = {(unsigned)obase, (unsigned)(obase >> 32) & 0xffffu, 40u * 1024u, 0x00020000u};
    const int okq = w & 1;
    bf16x8 wb[NB];
#define ABL_LIM 240
#define WLD(i) do { if ((i) < NB) asm volatile("buffer_load_dwordx4 %0, %1, %2, %3 offen offset:%4" : "=v"(wb[(i) % NB]) : "v"(l16), "s"(wr), "s"(((i) >> 2) * 4096), "n"(((i) & 3) * 1024)); \
        else if ((i) < ABL_LIM) asm volatile("buffer_load_dwordx4 %0, %1, %2, %3 offen offset:%4" : "+v"(wb[(i) % NB]) : "v"(l16), "s"(wr), "s"(((i) >> 2) * 4096), "n"(((i) & 3) * 1024)); \
        else if ((i) >= 240) asm volatile("buffer_load_dwordx4 %0, %1, %2, %3 offen offset:%4" : "+v"(wb[(i) % NB]) : "v"(l16), "s"(wr2), "s"(wo_frag(okq, ((i) - 240) & ~3) * 1024), "n"(((i) & 3) * 1024)); \
        SBAR(); } while (0)
#define WWAIT(i) asm volatile("s_waitcnt vmcnt(%1)" : "+v"(wb[(i) % NB]) : "n"(mlp_cnt(i, NB)))
#define MMA(i, b, c) ({ if (MLP_SLEEP) __builtin_amdgcn_s_sleep(MLP_SLEEP); WWAIT(i); mfma16(wb[(i) % NB], b, c); })
#pragma unroll
    for (int i = 0; i < NB; ++i) WLD(i);
    asm volatile("s_waitcnt vmcnt(%7)" : "+v"(ts), "+v"(csv[0]), "+v"(csv[1]), "+v"(csv[2]), "+v"(b1v[0]), "+v"(b1v[1]), "+v"(b1v[2]) : "n"(NB) : "memory");
    lds_signal(cnt + C_X);
    lds_await(cnt + C_X, lds_peek(cnt + C_X), 4);
    STAMPW(2, 1, 8);
#define SERVICE(NVM, cf, ci) STAMPW(2, (cf) - 1, 8)
    unsigned xa[4];
#pragma unroll
    for (int k = 0; k < 4; ++k) { xa[k] = lds0 + K2_X + r * 128 + (((2 * k + hh) ^ ((r >> 1) & 7)) * 16); PIN(xa[k]); }
    f32x16 acc[3]; acc[0] = f32x16{}; acc[1] = f32x16{}; acc[2] = f32x16{};
    bf16x8 xf[2];
    xf[0] = lds_r128((lds_cptr)(uintptr_t)xa[0]);
#pragma unroll
    for (int ks = 0; ks < 40; ++ks) {
        if (ks == 12) SERVICE(24, 3, 4);
        if (ks == 26) SERVICE(24, 4, 5);
        if (ks + 1 < 40) xf[(ks + 1) & 1] = lds_r128((lds_cptr)(uintptr_t)xa[(ks + 1) & 3] + ((ks + 1) >> 2) * 4096);
        SBAR();
#pragma unroll
        for (int f = 0; f < 3; ++f) {
            acc[f] = MMA(3 * ks + f, xf[ks & 1], acc[f]);
            SBAR();
            WLD(3 * ks + f + NB);
        }
        SBAR();
    }
    SERVICE(24, 5, 6);
    __builtin_amdgcn_s_setprio(GELU_PRIO);
    {
        unsigned m1, m2, m3; split3(hh ? ts[2] : -ts[0], m1, m2, m3);
        u32x4 tf = {m1 | (m1 << 16), m2 | (m1 << 16), m2 | (m3 << 16), m2 | (m3 << 16)};
        if (hh) tf = u32x4{m1 | (m2 << 16), m3, 0, 0};
#pragma unroll
        for (int f = 0; f < 3; ++f) {
            unsigned a1, a2, a3; split3(csv[f], a1, a2, a3);
            u32x4 ff = {a1 | (a2 << 16), a1 | (a3 << 16), a2 | (a1 << 16), a3 | (a2 << 16)};
            if (hh) ff = u32x4{b1v[f] | (b1v[f] << 16), b1v[f], 0, 0};
            acc[f] = mfma16(__builtin_bit_cast(bf16x8, ff), __builtin_bit_cast(bf16x8, tf), acc[f]);
            SBAR();
        }
    }
    {
        constexpr float C0 = -2.0f * 0.7978845608028654f * 1.4426950408889634f, C1 = C0 * 0.044715f;
        const float s2 = ts[1], sg = ts[2];
        const f32x2 k0 = {C0 * s2, C0 * s2}, k1 = {C1 * s2 * s2 * s2, C1 * s2 * s2 * s2}, sg2 = {sg, sg};
        __attribute__((address_space(3))) u32x4* hp = (__attribute__((address_space(3))) u32x4*)(uintptr_t)(lds0 + K2_H + w * 6144 + lane * 16);
#pragma unroll
        for (int f = 0; f < 3; ++f) {
#pragma unroll
            for (int s = 0; s < 2; ++s) {
                u32x4 u;
#pragma unroll
                for (int i = 0; i < 4; ++i) {
                    const f32x2 a = {acc[f][8 * s + 2 * i], acc[f][8 * s + 2 * i + 1]};
                    const f32x2 z = pk_mul(a, pk_fma(pk_mul(a, a), k1, k0));
                    const f32x2 e = {__builtin_amdgcn_exp2f(z[0]), __builtin_amdgcn_exp2f(z[1])};
                    const f32x2 d = pk_fma(e, sg2, sg2);
                    const f32x2 hv = pk_mul(a, f32x2{__builtin_amdgcn_rcpf(d[0]), __builtin_amdgcn_rcpf(d[1])});
                    u[i] = pk2(hv[0], hv[1]);
                }
                hp[(2 * f + s) * 64] = u;
                SBAR();
            }
        }
    }
    asm volatile("s_waitcnt lgkmcnt(0)" ::: "memory");
    lds_signal(cnt + C_H);
    __builtin_amdgcn_s_setprio(MLP_PRIO);
    lds_await(cnt + C_H, lds_peek(cnt + C_H), 4);
    STAMPW(2, 8, 8);
    unsigned ha = lds0 + K2_H + lane * 16;
    PIN(ha);
    const lds_cptr hb = (lds_cptr)(uintptr_t)ha;
    char* pp = ws + WS_PART + (((size_t)j * 64 + tt) * 20 + w * 5) * 2048 + lane * 16;
    auto store_tile = [&](const f32x16& t, int f) {
#pragma unroll
        for (int c = 0; c < 2; ++c) {
            float a[8];
#pragma unroll
            for (int i = 0; i < 8; ++i) a[i] = t[8 * c + i];
            st16(pp + (f * 2 + c) * 1024, u32x4{pk2(a[0], a[1]), pk2(a[2], a[3]), pk2(a[4], a[5]), pk2(a[6], a[7])});
            SBAR();
        }
    };
    acc[0] = f32x16{}; acc[1] = f32x16{}; acc[2] = f32x16{};
    xf[0] = lds_r128(hb);
#pragma unroll
    for (int ks = 0; ks < 24; ++ks) {
        if (ks == 10) SERVICE(24, 6, 7);
        xf[(ks + 1) & 1] = lds_r128(hb + ((ks + 1) % 24) * 1024);
        SBAR();
#pragma unroll
        for (int f = 0; f < 3; ++f) {
            acc[f] = MMA(120 + 3 * ks + f, xf[ks & 1], acc[f]);
            SBAR();
            WLD(120 + 3 * ks + f + NB);
        }
        SBAR();
    }
    SERVICE(24, 7, 8);
    store_tile(acc[0], 0); store_tile(acc[1], 1); store_tile(acc[2], 2);
    SBAR();
    f32x16 ac2[2]; ac2[0] = f32x16{}; ac2[1] = f32x16{};
#pragma unroll
    for (int ks = 0; ks < 24; ++ks) {
        if (ks + 1 < 24) xf[(ks + 1) & 1] = lds_r128(hb + (ks + 1) * 1024);
        SBAR();
#pragma unroll
        for (int f = 0; f < 2; ++f) {
            ac2[f] = MMA(192 + 2 * ks + f, xf[ks & 1], ac2[f]);
            SBAR();
            WLD(192 + 2 * ks + f + NB);
        }
        SBAR();
    }
    STAMPW(2, 9, 8);
#undef WLD
#undef SERVICE
    __builtin_amdgcn_s_setprio(TAILP);
    store_tile(ac2[0], 3); store_tile(ac2[1], 4);
    asm volatile("s_waitcnt vmcnt(0)" : "+v"(wb[0]), "+v"(wb[1]), "+v"(wb[2]), "+v"(wb[3]), "+v"(wb[4]), "+v"(wb[5]), "+v"(wb[6]), "+v"(wb[7]), "+v"(wb[8]), "+v"(wb[9]) :: "memory");
    asm volatile("" : "+v"(wb[10]), "+v"(wb[11]), "+v"(wb[12]), "+v"(wb[13]), "+v"(wb[14]), "+v"(wb[15]), "+v"(wb[16]), "+v"(wb[17]), "+v"(wb[18]), "+v"(wb[19]) :: "memory");
    {
        unsigned old = 0;
        if (lane == 0) old = __hip_atomic_fetch_add((__attribute__((address_space(3))) unsigned*)(uintptr_t)(cnt + C_MDONE), 1u, __ATOMIC_RELAXED, __HIP_MEMORY_SCOPE_WORKGROUP);
        if (__builtin_amdgcn_readfirstlane(old) == 3u) k2_count(ws, tt * 256 + 128);
    }
    STAMPW(2, 10, 8);
    k2_gather(x, b2, ws, lds, w);
    k2_tail(ws, lds, out, w >> 1, okq, wb);
}

__global__ __launch_bounds__(768, 3) void k2_main(const __bf16* __restrict__ x, const __bf16* __restrict__ b1, const __bf16* __restrict__ b2, char* ws, __bf16* __restrict__ out) {
    extern __shared__ __attribute__((aligned(16))) char lds[];
    const int wid = __builtin_amdgcn_readfirstlane(threadIdx.x >> 6);
    const int b = blockIdx.x, xcd = b & 7, head = xcd * 4 + ((b >> 3) & 3), qb = b >> 5;
    STAMPW(1, 0, AW);
    STAMPW(2, 0, 8);
    if (wid < 8) k2_attn(ws, lds, out, wid, head, qb);
    else k2_mlp(x, b1, b2, ws, lds, out, wid - 8, xcd & 3, (xcd >> 2) * 32 + (b >> 3));
}

}

extern "C" void kernel_launch(void* const* d_in, const int* in_sizes, int n_in, void* d_out, int out_size, void* d_ws, size_t ws_size, hipStream_t stream) {
    const __bf16* x = (const __bf16*)d_in[0];
    const __bf16* wqkv = (const __bf16*)d_in[1];
    const __bf16* wo = (const __bf16*)d_in[2];
    const __bf16* w1 = (const __bf16*)d_in[4];
    const __bf16* b1 = (const __bf16*)d_in[5];
    const __bf16* w2 = (const __bf16*)d_in[6];
    const __bf16* b2 = (const __bf16*)d_in[7];
    char* ws = (char*)d_ws;
    (void)hipFuncSetAttribute((const void*)k1_qkv, hipFuncAttributeMaxDynamicSharedMemorySize, K1_LDS);
    (void)hipFuncSetAttribute((const void*)k2_main, hipFuncAttributeMaxDynamicSharedMemorySize, K2_LDS);
    k1_qkv<<<256, 768, K1_LDS, stream>>>(x, wqkv, wo, w1, w2, ws);
    k2_main<<<256, 768, K2_LDS, stream>>>(x, b1, b2, ws, (__bf16*)d_out);
}
```
